```python
import torch

def _softmax(x, axis=-1):
    return torch.softmax(x, dim=axis)
def _gelu(x):
    return torch.nn.functional.gelu(x, approximate="tanh")
def _silu(x):
    return torch.nn.functional.silu(x)
def _sigmoid(x):
    return torch.sigmoid(x)
def _layernorm(x, axis=-1, eps=1e-5):
    m = x.mean(dim=axis, keepdim=True)
    v = x.var(dim=axis, keepdim=True, unbiased=False)
    return (x - m) / torch.sqrt(v + eps)
def _scatter_add(tgt, idx, src, axis=0):
    return tgt.clone().index_add_(axis, idx, src)
def _segment_sum(src, seg, n):
    out = torch.zeros((n,) + src.shape[1:], dtype=src.dtype)
    return out.index_add_(0, seg, src)
def _pad(x, pw):
    flat = tuple(v for pair in reversed(pw) for v in pair)
    return torch.nn.functional.pad(x, flat)

kernel_name = 'sequential_preLN__sliding_block__dense_activation_gelu__prompt'

def setup_inputs():

    _w = (lambda a: (lambda t: t.to(torch.bfloat16) if t.is_floating_point() else t)(torch.as_tensor(a)))
    def _A_setup():
        B, S, D = 1, 2048, 576
        return {
            "x": _w((lambda *s: torch.randn(*s, dtype=torch.bfloat16) * 0.05)(B, S, D)),
            "wqkv": _w((lambda *s: torch.randn(*s, dtype=torch.bfloat16) * 0.05)(D, 3 * D)),
            "wo": _w((lambda *s: torch.randn(*s, dtype=torch.bfloat16) * 0.05)(D, D)),
        }
    _A_in = _A_setup()

    def _B_setup():
        B, S, D, F = 1, 2048, 576, 4608
        return {
            "x": _w((lambda *s: torch.randn(*s, dtype=torch.bfloat16) * 0.05)(B, S, D)),
            "w1": _w((lambda *s: torch.randn(*s, dtype=torch.bfloat16) * 0.8)(D, F) * (D ** -0.5)),
            "b1": _w((lambda *s: torch.randn(*s, dtype=torch.bfloat16) * 0.8)(F) * 0.01),
            "w2": _w((lambda *s: torch.randn(*s, dtype=torch.bfloat16) * 0.8)(F, D) * (F ** -0.5)),
            "b2": _w((lambda *s: torch.randn(*s, dtype=torch.bfloat16) * 0.8)(D) * 0.01),
        }
    _B_in = _B_setup()

    return {
        "A_x": _A_in["x"],
        "A_wqkv": _A_in["wqkv"],
        "A_wo": _A_in["wo"],
        "B_x": _B_in["x"],
        "B_w1": _B_in["w1"],
        "B_b1": _B_in["b1"],
        "B_w2": _B_in["w2"],
        "B_b2": _B_in["b2"],
    }

def reference(A_x, A_wqkv, A_wo, B_x, B_w1, B_b1, B_w2, B_b2):
    def _A_ref(x, wqkv, wo):
        B, S, D = x.shape
        H = 32; Dh = D // H; W = 256
        qkv = (lambda x, *d: x.permute(*d))((x @ wqkv).reshape(B, S, 3, H, Dh), 2, 0, 3, 1, 4)
        q, k, v = qkv[0], qkv[1], qkv[2]
        scores = torch.einsum("bhqd,bhkd->bhqk", q, k) * (Dh ** -0.5)
        idx = torch.arange(S)
        mask = (idx[:, None] - idx[None, :] >= 0) & (idx[:, None] - idx[None, :] < W)
        scores = torch.where(mask[None, None], scores, -1e9)
        p = _softmax(scores, axis=-1)
        o = torch.einsum("bhqk,bhkd->bhqd", p, v)
        return (lambda x, *d: x.permute(*d))(o, 0, 2, 1, 3).reshape(B, S, D) @ wo

    def _B_ref(x, w1, b1, w2, b2):
        h = torch.einsum("bsd,df->bsf", x, w1) + b1
        h = _gelu(h)
        return torch.einsum("bsf,fd->bsd", h, w2) + b2

    x = A_x
    h = x + _A_ref(_layernorm(x, axis=-1), A_wqkv, A_wo)
    h2 = h + _B_ref(_layernorm(h, axis=-1), B_w1, B_b1, B_w2, B_b2)
    return h2
```

```cpp
#include <hip/hip_runtime.h>
#include <hip/hip_bf16.h>
#include <stdint.h>

#define GAS __attribute__((address_space(1)))
#define LAS __attribute__((address_space(3)))
#define DI __device__ __forceinline__
typedef unsigned v4u __attribute__((ext_vector_type(4)));
typedef unsigned v2u __attribute__((ext_vector_type(2)));
typedef float f32x4 __attribute__((ext_vector_type(4)));
typedef float f32x16 __attribute__((ext_vector_type(16)));
typedef short bf16x8 __attribute__((ext_vector_type(8)));
typedef short s16x4 __attribute__((ext_vector_type(4)));
typedef __bf16 bf2 __attribute__((ext_vector_type(2)));
typedef GAS unsigned gu32;
typedef __amdgpu_buffer_rsrc_t rsrc_t;

#define STAMPS 0

constexpr int S = 2048, D = 576, NH = 32, DH = 18, FF = 4608, N3 = 3 * D;
constexpr int NR = 32;
constexpr float LN_EPS = 1e-5f;
constexpr float QSCALE = 0.23570226039551584f * 1.4426950408889634f;

constexpr size_t FRAG = 1024;
constexpr size_t OFF_FLAG = 0;
constexpr size_t OFF_STAMP = 16384;
constexpr size_t OFF_QKV = 65536;
constexpr size_t QKV_SEG = (size_t)8 * S * 72 * 2;
constexpr size_t OFF_WS = OFF_QKV + 3 * QKV_SEG;
constexpr size_t WS_J = (size_t)2 * 36 * 18 * FRAG;
constexpr size_t OFF_WO = OFF_WS + 8 * WS_J;
constexpr size_t OFF_O = OFF_WO + (size_t)36 * 18 * FRAG;
constexpr size_t TILE_FR = (size_t)18 * 4 * FRAG;
constexpr size_t OFF_H = OFF_O + NR * TILE_FR;
constexpr size_t OFF_P = OFF_H + NR * TILE_FR;
constexpr size_t P_SRC = (size_t)36 * 2 * FRAG;
constexpr size_t WS_END = OFF_P + (size_t)NR * 8 * P_SRC;

__host__ __device__ constexpr int ffn_T(int wv) { return wv < 4 ? 5 : 4; }
__host__ __device__ constexpr size_t ffn_stream_base(int wv) { return wv < 4 ? (size_t)wv * 180 * FRAG : (size_t)720 * FRAG + (size_t)(wv - 4) * 144 * FRAG; }

DI f32x4 mfma16(v4u a, v4u b, f32x4 c) { return __builtin_amdgcn_mfma_f32_16x16x32_bf16(__builtin_bit_cast(bf16x8, a), __builtin_bit_cast(bf16x8, b), c, 0, 0, 0); }
DI f32x16 mfma32(v4u a, v4u b, f32x16 c) { return __builtin_amdgcn_mfma_f32_32x32x16_bf16(__builtin_bit_cast(bf16x8, a), __builtin_bit_cast(bf16x8, b), c, 0, 0, 0); }
DI unsigned pk2(float a, float b) { bf2 v = {(__bf16)a, (__bf16)b}; return __builtin_bit_cast(unsigned, v); }
DI float blo(unsigned u) { return __builtin_bit_cast(float, u << 16); }
DI float bhi(unsigned u) { return __builtin_bit_cast(float, u & 0xffff0000u); }
DI rsrc_t mkrsrc(const void* p, unsigned bytes) { return __builtin_amdgcn_make_buffer_rsrc((void*)p, 0, bytes, 0x00020000); }
DI v4u bld128(rsrc_t r, int voff, int soff) { return __builtin_amdgcn_raw_buffer_load_b128(r, voff, soff, 0); }
DI v4u bld128_sc1(rsrc_t r, int voff, int soff) { return __builtin_amdgcn_raw_buffer_load_b128(r, voff, soff, 16); }
DI v2u bld64(rsrc_t r, int voff, int soff) { return __builtin_amdgcn_raw_buffer_load_b64(r, voff, soff, 0); }
DI v4u lds128(const LAS char* p) { return *(const LAS v4u*)p; }
DI v2u lds64(const LAS char* p) { return *(const LAS v2u*)p; }
typedef short v4i16_t __attribute__((ext_vector_type(4)));
DI v2u ldstr(const LAS char* p) { return __builtin_bit_cast(v2u, __builtin_amdgcn_ds_read_tr16_b64_v4i16((LAS v4i16_t*)p)); }
DI float xor16(float v) { return __shfl_xor(v, 16); }
DI float xor32(float v) { return __shfl_xor(v, 32); }
DI float gelu_tanh(float x) {
    const float c0 = -2.302208198f, c1 = -0.1029432397f;
    float z = x * __builtin_fmaf(x * x, c1, c0);
    return x * __builtin_amdgcn_rcpf(1.0f + __builtin_amdgcn_exp2f(z));
}
#define STAMP(ws, i) do { } while (0)
#define TS(n) do { } while (0)
#define TSW(n) do { } while (0)
#define TSDECL
#define TSDUMP(ws, kidx) do { } while (0)

DI void ln_tile(const v4u (&x)[9], LAS char* xn, LAS float* st, int w, int lane) {
    const int tt = w & 3, kh = w >> 2, i = lane & 15;
    float v[72];
    float s = 0.f;
#pragma unroll
    for (int m = 0; m < 9; ++m)
#pragma unroll
        for (int q = 0; q < 4; ++q) { v[m * 8 + 2 * q] = blo(x[m][q]); v[m * 8 + 2 * q + 1] = bhi(x[m][q]); s += v[m * 8 + 2 * q] + v[m * 8 + 2 * q + 1]; }
    s += xor16(s); s += xor32(s);
    if (lane < 16) st[kh * 64 + tt * 16 + i] = s;
    __syncthreads();
    const float mean = (st[tt * 16 + i] + st[64 + tt * 16 + i]) * (1.0f / D);
    float q2 = 0.f;
#pragma unroll
    for (int e = 0; e < 72; ++e) { v[e] -= mean; q2 = __builtin_fmaf(v[e], v[e], q2); }
    q2 += xor16(q2); q2 += xor32(q2);
    if (lane < 16) st[128 + kh * 64 + tt * 16 + i] = q2;
    __syncthreads();
    const float var = (st[128 + tt * 16 + i] + st[192 + tt * 16 + i]) * (1.0f / D);
    const float rstd = 1.0f / __builtin_sqrtf(var + LN_EPS);
#pragma unroll
    for (int m = 0; m < 9; ++m) {
        v4u o;
#pragma unroll
        for (int q = 0; q < 4; ++q) o[q] = pk2(v[m * 8 + 2 * q] * rstd, v[m * 8 + 2 * q + 1] * rstd);
        *(LAS v4u*)(xn + ((9 * kh + m) * 4 + tt) * 1024 + lane * 16) = o;
    }
    __syncthreads();
}

struct K1Args { const void* x; const void* wqkv; const void* wo; const void* w1; const void* w2; char* ws; };
constexpr int K1_LDS = 73728 + 1024 + 8 * 4096;
constexpr int NUNIT = 1296 + 1296 + 162;
constexpr int NWORK = 224 * 4 + 32 * 8;

DI void retile_unit(const K1Args& a, int u, LAS char* scr, int lane) {
    const int g = lane >> 4, q = (lane & 15) >> 2, p = lane & 3;
    const char* src; int ld; bool w2order = false; size_t dst[4];
    if (u < 1296) {
        const int ks = u % 18, G = u / 18, j = G / 9, gl = G % 9;
        src = (const char*)a.w1 + ((size_t)(32 * ks) * FF + 64 * G) * 2; ld = FF * 2;
#pragma unroll
        for (int c = 0; c < 4; ++c) {
            const int ft = 4 * gl + c, s = ft / 9, o = ft % 9;
            const int wv = o < 5 ? s : 4 + s, t = o < 5 ? o : o - 5, T = o < 5 ? 5 : 4;
            dst[c] = OFF_WS + j * WS_J + ffn_stream_base(wv) + (size_t)(ks * T + t) * FRAG;
        }
    } else if (u < 2592) {
        const int uu = u - 1296, G = uu % 9, ks = (uu / 9) % 18, j = uu / 162;
        src = (const char*)a.w2 + ((size_t)(576 * j + 32 * ks) * D + 64 * G) * 2; ld = D * 2; w2order = true;
#pragma unroll
        for (int c = 0; c < 4; ++c) {
            const int dt = 4 * G + c;
            int wv, t;
            if (G == j) { wv = c; t = 4; }
            else { const int idx = dt < 4 * j ? dt : dt - 4; const int s = idx >> 3, o = idx & 7; wv = o < 4 ? s : 4 + s; t = o & 3; }
            const int T = wv < 4 ? 5 : 4;
            dst[c] = OFF_WS + j * WS_J + ffn_stream_base(wv) + (size_t)(18 * T + ks * T + t) * FRAG;
        }
    } else {
        const int uu = u - 2592, ks = uu % 18, G = uu / 18;
        src = (const char*)a.wo + ((size_t)(32 * ks) * D + 64 * G) * 2; ld = D * 2;
#pragma unroll
        for (int c = 0; c < 4; ++c) dst[c] = OFF_WO + (size_t)((4 * G + c) * 18 + ks) * FRAG;
    }
    v4u r[4];
#pragma unroll
    for (int m = 0; m < 4; ++m) r[m] = *(const GAS v4u*)(src + (size_t)(8 * m + (lane >> 3)) * ld + (lane & 7) * 16);
#pragma unroll
    for (int m = 0; m < 4; ++m) *(LAS v4u*)(scr + (8 * m + (lane >> 3)) * 128 + (lane & 7) * 16) = r[m];
    asm volatile("s_waitcnt lgkmcnt(0)" ::: "memory");
#pragma unroll
    for (int c = 0; c < 4; ++c) {
        const int row0 = w2order ? 4 * g + q : 8 * g + q, rstep = w2order ? 16 : 4;
        const v2u lo = ldstr(scr + row0 * 128 + (16 * c + 4 * p) * 2);
        const v2u hi = ldstr(scr + (row0 + rstep) * 128 + (16 * c + 4 * p) * 2);
        *(GAS v4u*)(a.ws + dst[c] + lane * 16) = v4u{lo.x, lo.y, hi.x, hi.y};
    }
    asm volatile("s_waitcnt lgkmcnt(0)" ::: "memory");
}

__global__ __launch_bounds__(512, 2) void k1_qkv(K1Args a) {
    extern __shared__ __attribute__((aligned(16))) char smem_[];
    LAS char* smem = (LAS char*)smem_;
    const int tid = threadIdx.x, lane = tid & 63, w = __builtin_amdgcn_readfirstlane(tid >> 6), b = blockIdx.x;
    STAMP(a.ws, 0);
    TSDECL; TS(0);
    if (b == 0 && tid < 32) *(GAS unsigned*)(a.ws + OFF_FLAG + tid * 256) = 0u;
    LAS char* scr = smem + 73728 + 1024 + w * 4096;
    if (b >= 224) {
        for (int u = 896 + (b - 224) * 8 + w; u < NUNIT; u += NWORK) retile_unit(a, u, scr, lane);
        STAMP(a.ws, 2);
        return;
    }
    const int r = 4 * (b & 7) + ((b >> 3) & 3), c = b >> 5;
    const int g = lane >> 4, i = lane & 15;
    {
        const int tt = w & 3, kh = w >> 2;
        const char* xp = (const char*)a.x + (size_t)(64 * r + 16 * tt + i) * (D * 2) + (9 * kh * 32 + 8 * g) * 2;
        v4u x[9];
#pragma unroll
        for (int m = 0; m < 9; ++m) x[m] = *(const GAS v4u*)(xp + m * 64);
        TS(1); TSW(2);
        ln_tile(x, smem, (LAS float*)(smem + 73728), w, lane);
    }
    TS(3);
    STAMP(a.ws, 1);
    if (w >= 4) {
        for (int u = b * 4 + (w - 4); u < NUNIT; u += NWORK) retile_unit(a, u, scr, lane);
        TS(4); TSW(5);
        TSDUMP(a.ws, 0);
        return;
    }
    const int n0 = 256 * c + 64 * w;
    if (n0 >= N3) return;
    const rsrc_t rw = mkrsrc(a.wqkv, D * N3 * 2);
    const int voff = (8 * g) * (N3 * 2) + (n0 + 4 * i) * 2;
    v2u L[3][8];
#pragma unroll
    for (int u = 0; u < 3; ++u)
#pragma unroll
        for (int jj = 0; jj < 8; ++jj) L[u][jj] = bld64(rw, voff + jj * (N3 * 2), u * 32 * N3 * 2);
    f32x4 acc[4][4];
#pragma unroll
    for (int cc = 0; cc < 4; ++cc)
#pragma unroll
        for (int tt = 0; tt < 4; ++tt) acc[cc][tt] = f32x4{0.f, 0.f, 0.f, 0.f};
    const LAS char* bp = smem + lane * 16;
    for (int it = 0; it < 6; ++it) {
#pragma unroll
        for (int u = 0; u < 3; ++u) {
            const int ks = 3 * it + u;
            v4u bf[4];
#pragma unroll
            for (int tt = 0; tt < 4; ++tt) bf[tt] = lds128(bp + (u * 4 + tt) * 1024);
            v4u af[4];
#pragma unroll
            for (int m = 0; m < 4; ++m) {
                af[0][m] = __builtin_amdgcn_perm(L[u][2 * m + 1].x, L[u][2 * m].x, 0x05040100u);
                af[1][m] = __builtin_amdgcn_perm(L[u][2 * m + 1].x, L[u][2 * m].x, 0x07060302u);
                af[2][m] = __builtin_amdgcn_perm(L[u][2 * m + 1].y, L[u][2 * m].y, 0x05040100u);
                af[3][m] = __builtin_amdgcn_perm(L[u][2 * m + 1].y, L[u][2 * m].y, 0x07060302u);
            }
#pragma unroll
            for (int jj = 0; jj < 8; ++jj) L[u][jj] = bld64(rw, voff + jj * (N3 * 2), (ks + 3) * 32 * N3 * 2);
#pragma unroll
            for (int cc = 0; cc < 4; ++cc)
#pragma unroll
                for (int tt = 0; tt < 4; ++tt) acc[cc][tt] = mfma16(af[cc], bf[tt], acc[cc][tt]);
        }
        bp += 3 * 4096;
    }
    TS(4);
    const float sc = n0 < D ? QSCALE : 1.0f;
#pragma unroll
    for (int hh = 0; hh < 2; ++hh) {
        const int n8 = n0 + 16 * g + 8 * hh, seg = n8 / D, rem = n8 % D, j = rem / 72, o = rem % 72;
        char* dp = a.ws + OFF_QKV + seg * QKV_SEG + ((size_t)j * S + 64 * r + i) * 144 + o * 2;
#pragma unroll
        for (int tt = 0; tt < 4; ++tt) {
            v4u o4;
#pragma unroll
            for (int m = 0; m < 4; ++m) { const int rr = 2 * hh + (m >> 1), cc = 2 * (m & 1); o4[m] = pk2(acc[cc][tt][rr] * sc, acc[cc + 1][tt][rr] * sc); }
            *(GAS v4u*)(dp + tt * 16 * 144) = o4;
        }
    }
    STAMP(a.ws, 2);
    TS(5); TSW(6);
    TSDUMP(a.ws, 0);
}

constexpr int KV_HEAD = 320 * 40;
constexpr int K2_K = 0, K2_V = 4 * KV_HEAD, K2_O = 8 * KV_HEAD, K2_LDS = K2_O + 64 * 144;
__global__ __launch_bounds__(512, 2) void k2_attn(char* ws) {
    extern __shared__ __attribute__((aligned(16))) char smem_[];
    LAS char* smem = (LAS char*)smem_;
    const int tid = threadIdx.x, lane = tid & 63, w = __builtin_amdgcn_readfirstlane(tid >> 6), b = blockIdx.x;
    const int j = b & 7, r = b >> 3;
    STAMP(ws, 3);
    TSDECL; TS(0);
    const int hl = w & 3, qh = w >> 2, l31 = lane & 31, hh = lane >> 5;
    const char* qp = ws + OFF_QKV + ((size_t)j * S + 64 * r + 32 * qh + l31) * 144 + 36 * hl;
    v4u q0, q1;
    { const GAS unsigned* qq = (const GAS unsigned*)(qp + 16 * hh); q0 = v4u{qq[0], qq[1], qq[2], qq[3]};
      const unsigned t = *(const GAS unsigned*)(qp + 32); q1 = v4u{hh == 0 ? t : 0u, 0u, 0u, 0u}; }
    const rsrc_t rk = mkrsrc(ws + OFF_QKV + QKV_SEG + (size_t)j * S * 144, S * 144), rv = mkrsrc(ws + OFF_QKV + 2 * QKV_SEG + (size_t)j * S * 144, S * 144);
    const int gbase = (64 * r - 256) * 144;
    v4u kk[6], vv[6];
#pragma unroll
    for (int m = 0; m < 6; ++m) { const int n = tid + 512 * m; const int o_ = gbase + n * 16; const int off = (n < 2880 && o_ >= 0) ? o_ : 0x7ffffff0;
        kk[m] = bld128(rk, off, 0); vv[m] = bld128(rv, off, 0); }
    TS(1); TSW(2);
    for (int n = tid; n < 1280; n += 512) { *(LAS unsigned*)(smem + K2_K + n * 40 + 36) = 0u; *(LAS unsigned*)(smem + K2_V + n * 40 + 36) = 0x3f80u; }
#pragma unroll
    for (int m = 0; m < 6; ++m) { const int n = tid + 512 * m;
        if (n < 2880) { const int tok = n / 9, mm = n % 9;
#pragma unroll
            for (int e = 0; e < 4; ++e) { const int dw = 4 * mm + e, h = dw / 9, dd = dw % 9; const int o = (h * 320 + tok) * 40 + dd * 4;
                *(LAS unsigned*)(smem + K2_K + o) = kk[m][e]; *(LAS unsigned*)(smem + K2_V + o) = vv[m][e]; } } }
    TS(3);
    __syncthreads();
    TS(4);
    STAMP(ws, 4);
    const int tmin = (256 - 64 * r - 32 * qh) > 0 ? (256 - 64 * r - 32 * qh) >> 5 : 0;
    const LAS char* kp = smem + K2_K + (hl * 320 + 32 * qh + l31) * 40;
    f32x16 sc[9];
    const f32x16 zero = {};
#pragma unroll
    for (int t = 0; t < 9; ++t) {
        const v2u a0 = lds64(kp + t * 1280 + 16 * hh), a1 = lds64(kp + t * 1280 + 16 * hh + 8), a2 = lds64(kp + t * 1280 + 32);
        f32x16 s = mfma32(v4u{a0.x, a0.y, a1.x, a1.y}, q0, zero);
        sc[t] = mfma32(v4u{a2.x, a2.y, 0u, 0u}, q1, s);
    }
    TS(5);
    const float ninf = -__builtin_inff();
#pragma unroll
    for (int rg = 0; rg < 16; ++rg) { const int key = (rg & 3) + 8 * (rg >> 2) + 4 * hh;
        if (key <= l31) sc[0][rg] = ninf;
        if (key > l31) sc[8][rg] = ninf; }
    float mx = ninf;
#pragma unroll
    for (int t = 0; t < 9; ++t) {
        if (t < tmin) {
#pragma unroll
            for (int rg = 0; rg < 16; ++rg) sc[t][rg] = ninf;
        }
#pragma unroll
        for (int rg = 0; rg < 16; ++rg) mx = __builtin_fmaxf(mx, sc[t][rg]);
    }
    mx = __builtin_fmaxf(mx, xor32(mx));
    TS(6);
    const LAS char* vp = smem + K2_V + (hl * 320 + 32 * qh + 4 * hh + ((lane & 15) >> 2)) * 40 + (((lane >> 4) & 1) ? 32 : 8 * (lane & 3));
    f32x16 oacc = {};
#pragma unroll
    for (int t = 0; t < 9; ++t) {
        unsigned pw[8];
#pragma unroll
        for (int e = 0; e < 8; ++e) pw[e] = pk2(__builtin_amdgcn_exp2f(sc[t][2 * e] - mx), __builtin_amdgcn_exp2f(sc[t][2 * e + 1] - mx));
#pragma unroll
        for (int s = 0; s < 2; ++s) {
            const v2u lo = ldstr(vp + (t * 32 + 16 * s) * 40), hi = ldstr(vp + (t * 32 + 16 * s + 8) * 40);
            oacc = mfma32(v4u{lo.x, lo.y, hi.x, hi.y}, v4u{pw[4 * s], pw[4 * s + 1], pw[4 * s + 2], pw[4 * s + 3]}, oacc);
        }
    }
    TS(7);
    float l = oacc[10]; { const float lo = __shfl(l, l31); l = lo; }
    const float inv = 1.0f / l;
    LAS char* op = smem + K2_O + (32 * qh + l31) * 144 + 36 * hl + 8 * hh;
    *(LAS unsigned*)(op) = pk2(oacc[0] * inv, oacc[1] * inv); *(LAS unsigned*)(op + 4) = pk2(oacc[2] * inv, oacc[3] * inv);
    *(LAS unsigned*)(op + 16) = pk2(oacc[4] * inv, oacc[5] * inv); *(LAS unsigned*)(op + 20) = pk2(oacc[6] * inv, oacc[7] * inv);
    if (hh == 0) *(LAS unsigned*)(op + 32) = pk2(oacc[8] * inv, oacc[9] * inv);
    __syncthreads();
    for (int n = tid; n < 576; n += 512) { const int i = n & 15, tt = (n >> 4) & 3, m = n >> 6;
        const v4u v = lds128(smem + K2_O + (16 * tt + i) * 144 + 16 * m);
        const int n8 = 72 * j + 8 * m, ks = n8 >> 5, g = (n8 & 31) >> 3;
        *(GAS v4u*)(ws + OFF_O + r * TILE_FR + (ks * 4 + tt) * 1024 + (g * 16 + i) * 16) = v; }
    STAMP(ws, 5);
    TS(8); TSW(9);
    TSDUMP(ws, 1);
}

constexpr int K3_LDS = 73728;
DI void oproj_unit(const char* ws, const void* x, int r, int dt, int tt0, int ntt, const LAS char* smem, int lane) {
    const rsrc_t rw = mkrsrc(ws + OFF_WO + (size_t)dt * 18 * FRAG, 18 * 1024);
    v4u af[18];
#pragma unroll
    for (int ks = 0; ks < 18; ++ks) af[ks] = bld128(rw, lane * 16 + (ks & 3) * 1024, (ks >> 2) * 4096);
    const int g = lane >> 4, i = lane & 15;
    for (int u = 0; u < ntt; ++u) { const int tt = tt0 + u;
        const v2u xr = *(const GAS v2u*)((const char*)x + (size_t)(64 * r + 16 * tt + i) * (D * 2) + (16 * dt + 4 * g) * 2);
        f32x4 acc = {0.f, 0.f, 0.f, 0.f};
#pragma unroll
        for (int ks = 0; ks < 18; ++ks) acc = mfma16(af[ks], lds128(smem + (ks * 4 + tt) * 1024 + lane * 16), acc);
        const v2u o = {pk2(acc[0] + blo(xr.x), acc[1] + bhi(xr.x)), pk2(acc[2] + blo(xr.y), acc[3] + bhi(xr.y))};
        *(GAS v2u*)((char*)ws + OFF_H + r * TILE_FR + ((dt >> 1) * 4 + tt) * 1024 + ((2 * (dt & 1) + (g >> 1)) * 16 + i) * 16 + (g & 1) * 8) = o;
    }
}
__global__ __launch_bounds__(512, 2) void k3_oproj(char* ws, const void* x) {
    extern __shared__ __attribute__((aligned(16))) char smem_[];
    LAS char* smem = (LAS char*)smem_;
    const int tid = threadIdx.x, lane = tid & 63, w = __builtin_amdgcn_readfirstlane(tid >> 6), b = blockIdx.x;
    const int j = b & 7, r = b >> 3;
    STAMP(ws, 6);
    TSDECL; TS(0);
    v4u t[9];
#pragma unroll
    for (int m = 0; m < 9; ++m) t[m] = *(const GAS v4u*)(ws + OFF_O + r * TILE_FR + (tid + 512 * m) * 16);
#pragma unroll
    for (int m = 0; m < 9; ++m) *(LAS v4u*)(smem + (tid + 512 * m) * 16) = t[m];
    TS(1);
    __syncthreads();
    TS(2);
    oproj_unit(ws, x, r, 4 * j + (w & 3), 2 * (w >> 2), 2, smem, lane);
    TS(3);
    if (w < 2) oproj_unit(ws, x, r, 32 + (j >> 1), 2 * (j & 1) + w, 1, smem, lane);
    TS(4); TSW(5);
    TSDUMP(ws, 2);
    STAMP(ws, 7);
}

struct K4Args { char* ws; const void* b1; const void* b2; void* out; };
constexpr int K4_XN = 0, K4_H = 73728, K4_ST = 147456, K4_LDS = K4_ST + 4096 + 64;

template <int T> DI void ffn_pass(f32x4 (&acc)[5][4], v4u (&ring)[15], rsrc_t rw, int soff, const LAS char* bp, int lane) {
    v4u bf[4];
#pragma unroll
    for (int tt = 0; tt < 4; ++tt) bf[tt] = lds128(bp + tt * 1024);
    for (int it = 0; it < 6; ++it) {
#pragma unroll
        for (int u = 0; u < 3; ++u) {
            v4u bn[4];
#pragma unroll
            for (int tt = 0; tt < 4; ++tt) bn[tt] = lds128(bp + ((u + 1) * 4 + tt) * 1024);
#pragma unroll
            for (int t = 0; t < T; ++t) {
                const int n = u * T + t;
#pragma unroll
                for (int tt = 0; tt < 4; ++tt) acc[t][tt] = mfma16(ring[n], bf[tt], acc[t][tt]);
                ring[n] = bld128(rw, lane * 16 + (n & 3) * 1024, soff + (n >> 2) * 4096);
            }
#pragma unroll
            for (int tt = 0; tt < 4; ++tt) bf[tt] = bn[tt];
            __builtin_amdgcn_sched_barrier(0);
        }
        bp += 3 * 4096; soff += 3 * T * 1024;
    }
}

template <int T> DI void ffn_body(const K4Args& a, LAS char* smem, int r, int j, int w, int lane) {
    const int g = lane >> 4, i = lane & 15, s = w & 3;
    const rsrc_t rw = mkrsrc(a.ws + OFF_WS + j * WS_J + ffn_stream_base(w), 2 * 18 * T * 1024);
    TSDECL; TS(0);
    v4u ring[15];
#pragma unroll
    for (int n = 0; n < 3 * T; ++n) ring[n] = bld128(rw, lane * 16 + (n & 3) * 1024, (n >> 2) * 4096);
    {
        const int tt = w & 3, kh = w >> 2;
        const char* hp = a.ws + OFF_H + r * TILE_FR + ((9 * kh) * 4 + tt) * 1024 + lane * 16;
        v4u x[9];
#pragma unroll
        for (int m = 0; m < 9; ++m) x[m] = *(const GAS v4u*)(hp + m * 4096);
        TS(1); TSW(2);
        ln_tile(x, smem + K4_XN, (LAS float*)(smem + K4_ST), w, lane);
    }
    TS(3);
    STAMP(a.ws, 9);
    const int ft0 = 9 * s + (T == 5 ? 0 : 5);
    f32x4 acc[5][4];
#pragma unroll
    for (int t = 0; t < T; ++t) {
        const v2u bb = *(const GAS v2u*)((const char*)a.b1 + (576 * j + 16 * (ft0 + t) + 4 * g) * 2);
        const f32x4 bv = {blo(bb.x), bhi(bb.x), blo(bb.y), bhi(bb.y)};
#pragma unroll
        for (int tt = 0; tt < 4; ++tt) acc[t][tt] = bv;
    }
    ffn_pass<T>(acc, ring, rw, 3 * T * 1024, smem + K4_XN + lane * 16, lane);
    STAMP(a.ws, 10);
    TS(4);
#pragma unroll
    for (int t = 0; t < T; ++t) {
        const int ft = ft0 + t;
#pragma unroll
        for (int tt = 0; tt < 4; ++tt) {
            const v2u hv = {pk2(gelu_tanh(acc[t][tt][0]), gelu_tanh(acc[t][tt][1])), pk2(gelu_tanh(acc[t][tt][2]), gelu_tanh(acc[t][tt][3]))};
            *(LAS v2u*)(smem + K4_H + ((ft >> 1) * 4 + tt) * 1024 + lane * 16 + (ft & 1) * 8) = hv;
            acc[t][tt] = f32x4{0.f, 0.f, 0.f, 0.f};
        }
    }
    TS(5);
    __syncthreads();
    TS(6);
    STAMP(a.ws, 11);
    ffn_pass<T>(acc, ring, rw, (18 * T + 3 * T) * 1024, smem + K4_H + lane * 16, lane);
    STAMP(a.ws, 12);
    TS(7);
    const rsrc_t rp = mkrsrc(a.ws + OFF_P + (size_t)r * 8 * P_SRC, 8 * P_SRC);
#pragma unroll
    for (int t = 0; t < 4; ++t) {
        const int idx = 8 * s + (T == 5 ? 0 : 4) + t, dt = idx < 4 * j ? idx : idx + 4;
#pragma unroll
        for (int tp = 0; tp < 2; ++tp) {
            const v4u o = {pk2(acc[t][2 * tp][0], acc[t][2 * tp][1]), pk2(acc[t][2 * tp][2], acc[t][2 * tp][3]),
                           pk2(acc[t][2 * tp + 1][0], acc[t][2 * tp + 1][1]), pk2(acc[t][2 * tp + 1][2], acc[t][2 * tp + 1][3])};
            __builtin_amdgcn_raw_buffer_store_b128(o, rp, lane * 16, j * (int)P_SRC + (dt * 2 + tp) * 1024, 16);
        }
    }
    TS(8);
    asm volatile("s_waitcnt vmcnt(0)" ::: "memory");
    TS(9);
    __syncthreads();
    gu32* flag = (gu32*)(a.ws + OFF_FLAG + r * 256);
    if (threadIdx.x == 0) {
        __hip_atomic_fetch_add(flag, 1u, __ATOMIC_RELAXED, __HIP_MEMORY_SCOPE_AGENT);
        for (unsigned spins = 0; __hip_atomic_load(flag, __ATOMIC_RELAXED, __HIP_MEMORY_SCOPE_AGENT) < 8u && spins < (1u << 20); ++spins) __builtin_amdgcn_s_sleep(1);
        __builtin_amdgcn_fence(__ATOMIC_ACQUIRE, "wavefront");
    }
    __syncthreads();
    STAMP(a.ws, 13);
    TS(10);
    if (T == 5 || w == 4) {
        const int dt = T == 5 ? 4 * j + s : 32 + (j >> 1);
        const int d0 = 16 * dt + 4 * g;
        const v2u bb = *(const GAS v2u*)((const char*)a.b2 + d0 * 2);
#pragma unroll
        for (int tp = 0; tp < 2; ++tp) {
            if (T == 4 && tp != (j & 1)) continue;
            v4u pv[8];
#pragma unroll
            for (int src = 0; src < 8; ++src) pv[src] = bld128_sc1(rp, lane * 16, src * (int)P_SRC + (dt * 2 + tp) * 1024);
#pragma unroll
            for (int u = 0; u < 2; ++u) { const int tt = 2 * tp + u;
                const v2u hv = *(const GAS v2u*)(a.ws + OFF_H + r * TILE_FR + ((dt >> 1) * 4 + tt) * 1024 + ((2 * (dt & 1) + (g >> 1)) * 16 + i) * 16 + (g & 1) * 8);
                f32x4 o = {blo(hv.x) + blo(bb.x), bhi(hv.x) + bhi(bb.x), blo(hv.y) + blo(bb.y), bhi(hv.y) + bhi(bb.y)};
                if (T == 5) o += acc[4][tt];
#pragma unroll
                for (int src = 0; src < 8; ++src) if (T == 4 || src != j) {
                    o[0] += blo(pv[src][2 * u]); o[1] += bhi(pv[src][2 * u]); o[2] += blo(pv[src][2 * u + 1]); o[3] += bhi(pv[src][2 * u + 1]); }
                *(GAS v2u*)((char*)a.out + (size_t)(64 * r + 16 * tt + i) * (D * 2) + d0 * 2) = v2u{pk2(o[0], o[1]), pk2(o[2], o[3])};
            }
        }
    }
    STAMP(a.ws, 14);
    TSW(11);
    TSDUMP(a.ws, 3);
}

__global__ __launch_bounds__(512, 2) void k4_ffn(K4Args a) {
    extern __shared__ __attribute__((aligned(16))) char smem_[];
    LAS char* smem = (LAS char*)smem_;
    const int tid = threadIdx.x, lane = tid & 63, w = __builtin_amdgcn_readfirstlane(tid >> 6), b = blockIdx.x;
    STAMP(a.ws, 8);
    if (w < 4) ffn_body<5>(a, smem, b >> 3, b & 7, w, lane);
    else ffn_body<4>(a, smem, b >> 3, b & 7, w, lane);
}

extern "C" void kernel_launch(void* const* d_in, const int* in_sizes, int n_in, void* d_out, int out_size, void* d_ws, size_t ws_size, hipStream_t stream) {
    (void)in_sizes; (void)n_in; (void)out_size; (void)ws_size;
    char* ws = (char*)d_ws;
    hipFuncSetAttribute((const void*)k1_qkv, hipFuncAttributeMaxDynamicSharedMemorySize, K1_LDS);
    hipFuncSetAttribute((const void*)k2_attn, hipFuncAttributeMaxDynamicSharedMemorySize, K2_LDS);
    hipFuncSetAttribute((const void*)k3_oproj, hipFuncAttributeMaxDynamicSharedMemorySize, K3_LDS);
    hipFuncSetAttribute((const void*)k4_ffn, hipFuncAttributeMaxDynamicSharedMemorySize, K4_LDS);
    K1Args a1{d_in[0], d_in[1], d_in[2], d_in[4], d_in[6], ws};
    k1_qkv<<<256, 512, K1_LDS, stream>>>(a1);
    k2_attn<<<256, 512, K2_LDS, stream>>>(ws);
    k3_oproj<<<256, 512, K3_LDS, stream>>>(ws, d_in[0]);
    K4Args a4{ws, d_in[5], d_in[7], d_out};
    k4_ffn<<<256, 512, K4_LDS, stream>>>(a4);
}
```

```cpp
#include <hip/hip_runtime.h>
#include <hip/hip_bf16.h>
#include <stdint.h>

#define GAS __attribute__((address_space(1)))
#define LAS __attribute__((address_space(3)))
#define DI __device__ __forceinline__
typedef unsigned v4u __attribute__((ext_vector_type(4)));
typedef unsigned v2u __attribute__((ext_vector_type(2)));
typedef float f32x4 __attribute__((ext_vector_type(4)));
typedef float f32x16 __attribute__((ext_vector_type(16)));
typedef short bf16x8 __attribute__((ext_vector_type(8)));
typedef __bf16 bf2 __attribute__((ext_vector_type(2)));
typedef GAS unsigned gu32;
typedef __amdgpu_buffer_rsrc_t rsrc_t;

#define STAMPS 0

constexpr int S = 2048, D = 576, FF = 4608, N3 = 3 * D;
constexpr int NR = 32;
constexpr float LN_EPS = 1e-5f;
constexpr float QSCALE = 0.23570226039551584f * 1.4426950408889634f;

constexpr size_t FRAG = 1024;
constexpr size_t OFF_FLAG = 0;
constexpr size_t OFF_STAMP = 16384;
constexpr size_t OFF_QKV = 65536;
constexpr size_t QKV_G = (size_t)S * 144;
constexpr size_t OFF_WS = OFF_QKV + 24 * QKV_G;
constexpr size_t WS_J = (size_t)2 * 36 * 18 * FRAG;
constexpr size_t OFF_WO = OFF_WS + 8 * WS_J;
constexpr size_t OFF_O = OFF_WO + (size_t)36 * 18 * FRAG;
constexpr size_t TILE_FR = (size_t)18 * 4 * FRAG;
constexpr size_t OFF_H = OFF_O + NR * TILE_FR;
constexpr size_t OFF_P = OFF_H + NR * TILE_FR;
constexpr size_t P_SRC = (size_t)36 * 2 * FRAG;
constexpr size_t OFF_CSP = OFF_P + (size_t)NR * 8 * P_SRC;
constexpr size_t OFF_CS = OFF_CSP + (size_t)18 * FF * 4;
constexpr size_t WS_END = OFF_CS + (size_t)FF * 4;

__host__ __device__ constexpr int ffn_stream_base(int wv) { return (wv < 4 ? wv * 180 : 720 + (wv - 4) * 144) * 1024; }
__host__ __device__ constexpr int ffn_pos(bool a, bool g2, int t, int ks) {
    return !g2 ? (a ? (t < 3 ? ks * 3 + t : 54 + ks * 2 + (t - 3)) : (t < 2 ? ks * 2 + t : 36 + ks * 2 + (t - 2)))
               : (a ? 90 + (t < 2 ? ks * 2 + t : t < 4 ? 36 + ks * 2 + (t - 2) : 72 + ks) : 72 + (t < 2 ? ks * 2 + t : 36 + ks * 2 + (t - 2)));
}
#define JM 4
DI int wg_r(int b) { return ((b & 7) / (8 / JM)) * (32 / JM) + (b >> 3) % (32 / JM); }
DI int wg_j(int b) { return ((b & 7) % (8 / JM)) * JM + (b >> 3) / (32 / JM); }

DI f32x4 mfma16(v4u a, v4u b, f32x4 c) { return __builtin_amdgcn_mfma_f32_16x16x32_bf16(__builtin_bit_cast(bf16x8, a), __builtin_bit_cast(bf16x8, b), c, 0, 0, 0); }
DI f32x16 mfma32(v4u a, v4u b, f32x16 c) { return __builtin_amdgcn_mfma_f32_32x32x16_bf16(__builtin_bit_cast(bf16x8, a), __builtin_bit_cast(bf16x8, b), c, 0, 0, 0); }
DI unsigned pk2(float a, float b) { bf2 v = {(__bf16)a, (__bf16)b}; return __builtin_bit_cast(unsigned, v); }
DI float blo(unsigned u) { return __builtin_bit_cast(float, u << 16); }
DI float bhi(unsigned u) { return __builtin_bit_cast(float, u & 0xffff0000u); }
DI rsrc_t mkrsrc(const void* p, unsigned bytes) { return __builtin_amdgcn_make_buffer_rsrc((void*)p, 0, bytes, 0x00020000); }
DI v4u bld128(rsrc_t r, int voff, int soff) { return __builtin_amdgcn_raw_buffer_load_b128(r, voff, soff, 0); }
DI v4u bld128_sc1(rsrc_t r, int voff, int soff) { return __builtin_amdgcn_raw_buffer_load_b128(r, voff, soff, 16); }
DI v2u bld64(rsrc_t r, int voff, int soff) { return __builtin_amdgcn_raw_buffer_load_b64(r, voff, soff, 0); }
DI v4u lds128(const LAS char* p) { return *(const LAS v4u*)p; }
DI v2u lds64(const LAS char* p) { return *(const LAS v2u*)p; }
typedef short v4i16_t __attribute__((ext_vector_type(4)));
DI v2u ldstr(const LAS char* p) { return __builtin_bit_cast(v2u, __builtin_amdgcn_ds_read_tr16_b64_v4i16((LAS v4i16_t*)p)); }
DI float dot2(unsigned a, float c) { return __builtin_amdgcn_fdot2_f32_bf16(__builtin_bit_cast(bf2, a), __builtin_bit_cast(bf2, 0x3f803f80u), c, false); }
DI float dot2sq(unsigned a, float c) { const bf2 v = __builtin_bit_cast(bf2, a); return __builtin_amdgcn_fdot2_f32_bf16(v, v, c, false); }
DI void dma16(rsrc_t r, int voff, int soff, unsigned lds) {
    asm volatile("s_mov_b32 m0, %0\n\ts_nop 0\n\tbuffer_load_dwordx4 %1, %2, %3 offen lds" :: "s"(lds), "v"(voff), "s"(r), "s"(soff) : "memory");
}
#define WAITV(n) asm volatile("s_waitcnt vmcnt(" #n ")" ::: "memory")
#define BAR() asm volatile("s_barrier" ::: "memory")
#define LBAR() asm volatile("s_waitcnt lgkmcnt(0)\n\ts_barrier" ::: "memory")
#define SB() __builtin_amdgcn_sched_barrier(0)
#define PIN(x) asm volatile("" : "+v"(x))
DI float sum16(float v) { const unsigned u = __builtin_bit_cast(unsigned, v); const auto r = __builtin_amdgcn_permlane16_swap(u, u, false, false);
    return __builtin_bit_cast(float, (unsigned)r[0]) + __builtin_bit_cast(float, (unsigned)r[1]); }
DI float sum32(float v) { const unsigned u = __builtin_bit_cast(unsigned, v); const auto r = __builtin_amdgcn_permlane32_swap(u, u, false, false);
    return __builtin_bit_cast(float, (unsigned)r[0]) + __builtin_bit_cast(float, (unsigned)r[1]); }
DI float max32(float v) { const unsigned u = __builtin_bit_cast(unsigned, v); const auto r = __builtin_amdgcn_permlane32_swap(u, u, false, false);
    return __builtin_fmaxf(__builtin_bit_cast(float, (unsigned)r[0]), __builtin_bit_cast(float, (unsigned)r[1])); }
DI float lo32(float v) { const unsigned u = __builtin_bit_cast(unsigned, v); const auto r = __builtin_amdgcn_permlane32_swap(u, u, false, false);
    return __builtin_bit_cast(float, (unsigned)r[0]); }
typedef float f32x2 __attribute__((ext_vector_type(2)));
DI f32x2 fma2(f32x2 a, f32x2 b, f32x2 c) { return __builtin_elementwise_fma(a, b, c); }
DI unsigned gelu2(f32x2 x) {
    const f32x2 c0 = {-2.302208198f, -2.302208198f}, c1 = {-0.1029432397f, -0.1029432397f}, one = {1.0f, 1.0f};
    const f32x2 z = x * fma2(x * x, c1, c0);
    const f32x2 d = f32x2{__builtin_amdgcn_exp2f(z[0]), __builtin_amdgcn_exp2f(z[1])} + one;
    const f32x2 y = x * f32x2{__builtin_amdgcn_rcpf(d[0]), __builtin_amdgcn_rcpf(d[1])};
    return pk2(y[0], y[1]);
}
DI float gelu_tanh(float x) {
    const float c0 = -2.302208198f, c1 = -0.1029432397f;
    float z = x * __builtin_fmaf(x * x, c1, c0);
    return x * __builtin_amdgcn_rcpf(1.0f + __builtin_amdgcn_exp2f(z));
}
#define STAMP(ws, i) do { } while (0)
#define TS(n) do { } while (0)
#define TSW(n) do { } while (0)
#define TSDECL
#define TSDUMP(ws, kidx) do { } while (0)

struct K1Args { const void* x; const void* wqkv; char* ws; };
constexpr int K1_STAGE = 32768;
constexpr int K1_TAB = 4 * K1_STAGE;
constexpr int K1_LDS = K1_TAB + 4096;

__global__ __launch_bounds__(512, 2) void k1_qkv(K1Args a) {
    extern __shared__ __attribute__((aligned(16))) char smem_[];
    LAS char* smem = (LAS char*)smem_;
    const unsigned lds0 = (unsigned)(size_t)smem_;
    const int tid = threadIdx.x, lane = tid & 63, w = __builtin_amdgcn_readfirstlane(tid >> 6), b = blockIdx.x;
    STAMP(a.ws, 0);
    TSDECL; TS(0);
    if (b == 0 && tid < 32) *(GAS unsigned*)(a.ws + OFF_FLAG + tid * 256) = 0u;
    const int xc = b & 7, idx = b >> 3;
    const int rt = 4 * (xc >> 1) + (idx & 3), ct = 7 * (xc & 1) + (idx >> 2);
    if (w >= 4) {
        const int lw = w - 4;
        const rsrc_t rx = mkrsrc(a.x, S * D * 2), rw = mkrsrc(a.wqkv, D * N3 * 2);
        const int vx = (128 * rt + 32 * lw + (lane >> 3)) * (D * 2) + ((lane & 7) ^ (lane >> 3)) * 16;
        const int c16 = lane & 15, q = lane >> 4;
        const int vw0 = (16 * lw + q) * (N3 * 2) + 256 * ct + ((((c16 >> 1) ^ q) << 1) | (c16 & 1)) * 16;
        const int vw1 = (16 * lw + q) * (N3 * 2) + 256 * ct + ((((c16 >> 1) ^ (q | 4)) << 1) | (c16 & 1)) * 16;
        auto stage = [&](int s) {
            const unsigned sb = lds0 + (s & 3) * K1_STAGE;
#pragma unroll
            for (int m = 0; m < 4; ++m) {
                dma16(rw, m < 2 ? vw0 : vw1, (64 * s + 4 * m) * (N3 * 2), sb + 16384 + (4 * lw + m) * 1024);
                dma16(rx, vx, 128 * s + m * 8 * (D * 2), sb + (4 * lw + m) * 1024);
            }
        };
        const int g = lane >> 4, i = lane & 15, q4 = i >> 2, p = lane & 3;
        const int sxo = (32 * lw + i) * 128, swo = 16384 + (8 * g + q4) * 256 + 8 * p, key = q4 | ((g & 1) << 2);
        float sx[2] = {0.f, 0.f}, sq[2] = {0.f, 0.f}, cs[2] = {0.f, 0.f};
        auto stats = [&](int s) {
            const LAS char* sb = smem + (s & 3) * K1_STAGE;
#pragma unroll
            for (int k2 = 0; k2 < 2; ++k2)
#pragma unroll
                for (int u = 0; u < 2; ++u) {
                    const v4u xv = lds128(sb + sxo + u * 2048 + ((4 * k2 + g) ^ (i & 7)) * 16);
                    const LAS char* ap = sb + swo + k2 * 8192 + (((2 * lw + u) ^ key) * 32);
                    const v2u lo = ldstr(ap), hi = ldstr(ap + 1024);
#pragma unroll
                    for (int e = 0; e < 4; ++e) { sx[u] = dot2(xv[e], sx[u]); sq[u] = dot2sq(xv[e], sq[u]); }
                    cs[u] = dot2(hi.y, dot2(hi.x, dot2(lo.y, dot2(lo.x, cs[u]))));
                }
        };
        stage(0); stage(1); stage(2);
        WAITV(16); TS(1); BAR();
        WAITV(8); BAR();
#pragma unroll
        for (int s = 3; s < 9; ++s) { stage(s); stats(s - 3); WAITV(8); asm volatile("s_waitcnt lgkmcnt(0)" ::: "memory"); TS(s - 1); BAR(); }
        stats(6); WAITV(0); BAR();
        stats(7); stats(8);
        LAS float* tabx = (LAS float*)(smem + K1_TAB);
        LAS float* tabc = tabx + 256;
#pragma unroll
        for (int u = 0; u < 2; ++u) { const float s1 = sum32(sum16(sx[u])), s2 = sum32(sum16(sq[u])), s3 = sum32(sum16(cs[u]));
            if (g == 0) { const float mean = s1 * (1.0f / D), var = __builtin_fmaxf(__builtin_fmaf(-mean, mean, s2 * (1.0f / D)), 0.f);
                tabx[(32 * lw + 16 * u + i) * 2] = mean; tabx[(32 * lw + 16 * u + i) * 2 + 1] = __builtin_amdgcn_rsqf(var + LN_EPS); tabc[32 * lw + 16 * u + i] = s3; } }
        LBAR();
        TS(9);
        TSDUMP(a.ws, 0);
        return;
    }
    const int th = w & 1, ch = w >> 1, g = lane >> 4, i = lane & 15;
    const int q = i >> 2, p = lane & 3;
    const int xoff0 = (64 * th + i) * 128 + ((g) ^ (i & 7)) * 16;
    const int xoff1 = (64 * th + i) * 128 + ((4 + g) ^ (i & 7)) * 16;
    const int woff = 16384 + (8 * g + q) * 256 + (((4 * ch) ^ (q | ((g & 1) << 2))) * 32) + 8 * p;
    f32x4 acc[4][4];
#pragma unroll
    for (int c4 = 0; c4 < 4; ++c4)
#pragma unroll
        for (int tt = 0; tt < 4; ++tt) acc[c4][tt] = f32x4{0.f, 0.f, 0.f, 0.f};
    v4u bA[4], aA[4], bB[4], aB[4];
    auto rd = [&](v4u (&bf)[4], v4u (&af)[4], const LAS char* sb, int k2) {
#pragma unroll
        for (int c4 = 0; c4 < 4; ++c4) { const LAS char* ap = sb + ((woff ^ (c4 << 5)) + k2 * 8192);
            const v2u lo = ldstr(ap), hi = ldstr(ap + 1024); af[c4] = v4u{lo.x, lo.y, hi.x, hi.y}; }
#pragma unroll
        for (int tt = 0; tt < 4; ++tt) bf[tt] = lds128(sb + (k2 ? xoff1 : xoff0) + tt * 2048);
    };
    auto mm = [&](const v4u (&bf)[4], const v4u (&af)[4]) {
#pragma unroll
        for (int c4 = 0; c4 < 4; ++c4)
#pragma unroll
            for (int tt = 0; tt < 4; ++tt) acc[c4][tt] = mfma16(af[c4], bf[tt], acc[c4][tt]);
    };
    int dst[4];
#pragma unroll
    for (int c4 = 0; c4 < 4; ++c4) { const int n4 = 128 * ct + 64 * ch + 16 * c4 + 4 * g, ja = n4 / 72;
        dst[c4] = (int)OFF_QKV + ja * (int)QKV_G + (128 * rt + 64 * th + i) * 144 + (n4 - 72 * ja) * 2; }
    BAR();
    TS(1);
    rd(bA, aA, smem, 0);
    for (int s = 0; s < 9; ++s) {
        const LAS char* sb = smem + (s & 3) * K1_STAGE;
        rd(bB, aB, sb, 1);
        SB();
        mm(bA, aA);
        SB();
        if (s == 0) BAR();
        if (s < 8) rd(bA, aA, smem + ((s + 1) & 3) * K1_STAGE, 0);
        SB();
        mm(bB, aB);
        SB();
        if (s < 7) { asm volatile("s_waitcnt lgkmcnt(12)\n\ts_barrier" ::: "memory"); }
    }
    TS(4);
    LBAR();
    const LAS float* tabx = (const LAS float*)(smem + K1_TAB);
    const LAS float* tabc = tabx + 256;
    float mean[4], rstd[4];
#pragma unroll
    for (int tt = 0; tt < 4; ++tt) { const int t = 64 * th + 16 * tt + i; mean[tt] = tabx[t * 2]; rstd[tt] = tabx[t * 2 + 1]; }
    TS(5);
    const rsrc_t rws = mkrsrc(a.ws, (unsigned)OFF_WS);
#pragma unroll
    for (int c4 = 0; c4 < 4; ++c4) {
        const int nt = 128 * ct + 64 * ch + 16 * c4;
        if (nt < N3) {
            const f32x4 c = *(const LAS f32x4*)(tabc + 64 * ch + 16 * c4 + 4 * g);
            const float sc = nt < D ? QSCALE : 1.0f;
#pragma unroll
            for (int tt = 0; tt < 4; ++tt) { const float rs = rstd[tt] * sc, m = mean[tt];
                const v2u o2 = {pk2(rs * __builtin_fmaf(-m, c[0], acc[c4][tt][0]), rs * __builtin_fmaf(-m, c[1], acc[c4][tt][1])),
                                pk2(rs * __builtin_fmaf(-m, c[2], acc[c4][tt][2]), rs * __builtin_fmaf(-m, c[3], acc[c4][tt][3]))};
                __builtin_amdgcn_raw_buffer_store_b64(o2, rws, dst[c4], tt * 16 * 144, 0); }
        }
    }
    STAMP(a.ws, 2);
    TS(6); TSW(7);
    TSDUMP(a.ws, 0);
}

struct Unit { rsrc_t rs; int soff, ld, w2; int dst, dstep; int cs; };
DI Unit unit_decode(const void* w1, const void* w2, const void* wo, int j, int u) {
    const bool isw1 = u < 162, isw2 = !isw1 && u < 324;
    const int uo = (u - 324) * 8 + j;
    const bool iswo = u >= 324 && uo < 162, none = u >= 324 && !iswo;
    const int v = isw1 ? u : isw2 ? u - 162 : uo, ks = v % 18, G = v / 18;
    const int gi = G < j ? G : G - 1;
    const bool A = isw1 ? G < 5 : (G == j || gi < 4);
    const int t = isw1 ? (A ? G : G - 5) : (G == j ? 4 : gi & 3);
    Unit r;
    r.rs = mkrsrc(isw1 ? w1 : isw2 ? w2 : wo, none ? 0u : iswo ? D * D * 2 : D * FF * 2);
    r.ld = isw1 ? FF * 2 : D * 2; r.w2 = isw2;
    r.soff = isw1 ? (32 * ks * FF + 576 * j + 64 * G) * 2 : ((isw2 ? 576 * j : 0) + 32 * ks) * (D * 2) + 128 * G;
    const int fp = isw1 ? ffn_pos(A, false, t, ks) : ffn_pos(A, true, t, ks);
    r.dst = none ? 0x7fff0000 : iswo ? (int)OFF_WO + (4 * G * 18 + ks) * 1024 : (int)OFF_WS + j * (int)WS_J + (A ? 0 : 720 * 1024) + fp * 1024;
    r.dstep = none ? 0 : iswo ? 18 * 1024 : A ? 180 * 1024 : 144 * 1024;
    r.cs = isw1 ? (int)OFF_CSP + (ks * FF + 576 * j + 64 * G) * 4 : 0x7fff0000;
    return r;
}
#define UST 16
DI void unit_load(const Unit& t, unsigned scr, int lane) {
    const int voff = (lane >> 3) * t.ld + (lane & 7) * 16;
#pragma unroll
    for (int m = 0; m < 4; ++m) dma16(t.rs, voff, t.soff + 8 * m * t.ld, scr + m * 1024);
}
DI void unit_store(const Unit& t, const LAS char* scr, rsrc_t rws, int lane) {
    const int g = lane >> 4, q = (lane & 15) >> 2, p = lane & 3;
    const int row0 = t.w2 ? 4 * g + q : 8 * g + q, rstep = t.w2 ? 16 : 4;
    float cs[4];
#pragma unroll
    for (int c = 0; c < 4; ++c) {
        const v2u lo = ldstr(scr + row0 * 128 + (16 * c + 4 * p) * 2);
        const v2u hi = ldstr(scr + (row0 + rstep) * 128 + (16 * c + 4 * p) * 2);
        __builtin_amdgcn_raw_buffer_store_b128(v4u{lo.x, lo.y, hi.x, hi.y}, rws, lane * 16, t.dst + c * t.dstep, UST);
        cs[c] = sum32(sum16(dot2(hi.y, dot2(hi.x, dot2(lo.y, dot2(lo.x, 0.f))))));
    }
    const float mine = g == 0 ? cs[0] : g == 1 ? cs[1] : g == 2 ? cs[2] : cs[3];
    __builtin_amdgcn_raw_buffer_store_b32(__builtin_bit_cast(unsigned, mine), rws, lane * 4, t.cs, 16);
}

DI int UJ(int b) { const int x = b & 7; return (x % (8 / JM)) * JM + x / (8 / JM); }
struct K2Args { char* ws; const void* wo; const void* w1; const void* w2; };
constexpr int KV_HEAD = 320 * 40;
constexpr int K2_K = 0, K2_V = 4 * KV_HEAD, K2_O = 8 * KV_HEAD, K2_SCR = K2_O + 64 * 144, K2_LDS = K2_SCR + 8 * 4096;

template <bool EDGE> DI void attn_core(LAS char* smem, const v4u q0, const v4u q1, int r, int w, int lane, f32x16& oacc, const Unit& ua, const Unit& ub, rsrc_t rws, unsigned lds0) {
    const int hl = w & 3, qh = w >> 2, l31 = lane & 31, hh = lane >> 5;
    const int tmin = EDGE ? ((256 - 64 * r - 32 * qh) >> 5) : 0;
    const float ninf = -__builtin_inff();
    const LAS char* kp = smem + K2_K + (hl * 320 + 32 * qh + l31) * 40;
    v4u ka[9], kb[9];
#pragma unroll
    for (int t = 0; t < 9; ++t) { const v2u a0 = lds64(kp + t * 1280 + 16 * hh), a1 = lds64(kp + t * 1280 + 16 * hh + 8);
        ka[t] = v4u{a0.x, a0.y, a1.x, a1.y};
        const v2u c0 = lds64(kp + t * 1280 + 32), c1 = lds64(kp + t * 1280 + 24);
        kb[t] = v4u{c0.x, c0.y, c1.x, c1.y}; }
    auto score = [&](int t, const f32x16& c) { return mfma32(kb[t], q1, mfma32(ka[t], q0, c)); };
    bool le[16];
#pragma unroll
    for (int rg = 0; rg < 16; ++rg) le[rg] = (rg & 3) + 8 * (rg >> 2) + 4 * hh <= l31;
    auto merged = [&](const f32x16& c) { const f32x16 s8 = score(8, c); f32x16 s0;
        if (EDGE) { s0 = s8;
#pragma unroll
            for (int rg = 0; rg < 16; ++rg) s0[rg] = ninf; } else s0 = score(0, c);
        f32x16 m;
#pragma unroll
        for (int rg = 0; rg < 16; ++rg) m[rg] = le[rg] ? s8[rg] : s0[rg];
        return m; };
    const f32x16 zero = {};
    float mx;
    { const f32x16 m = merged(zero); mx = __builtin_fmaxf(m[0], m[1]);
#pragma unroll
      for (int rg = 2; rg < 16; rg += 2) mx = __builtin_fmaxf(__builtin_fmaxf(mx, m[rg]), m[rg + 1]); }
#pragma unroll
    for (int t = 1; t < 8; ++t) if (!EDGE || t >= tmin) { const f32x16 s = score(t, zero);
#pragma unroll
        for (int rg = 0; rg < 16; rg += 2) mx = __builtin_fmaxf(__builtin_fmaxf(mx, s[rg]), s[rg + 1]); }
    mx = max32(mx);
    WAITV(0); unit_store(ua, smem + K2_SCR + w * 4096, rws, lane); unit_load(ub, lds0 + K2_SCR + w * 4096, lane);
    f32x16 cm;
#pragma unroll
    for (int rg = 0; rg < 16; ++rg) cm[rg] = -mx;
    const LAS char* vp = smem + K2_V + (hl * 320 + 32 * qh + 4 * hh + ((lane & 15) >> 2)) * 40 + (((lane >> 4) & 1) ? 32 : 8 * (lane & 3));
    auto pv = [&](int t, const unsigned (&pw)[8]) {
#pragma unroll
        for (int s = 0; s < 2; ++s) {
            const v2u lo = ldstr(vp + (t * 32 + 16 * s) * 40), hi = ldstr(vp + (t * 32 + 16 * s + 8) * 40);
            oacc = mfma32(v4u{lo.x, lo.y, hi.x, hi.y}, v4u{pw[4 * s], pw[4 * s + 1], pw[4 * s + 2], pw[4 * s + 3]}, oacc);
        } };
    oacc = zero;
    { const f32x16 m = merged(cm); unsigned pw[8], p8[8], p0[8];
#pragma unroll
      for (int e = 0; e < 8; ++e) { pw[e] = pk2(__builtin_amdgcn_exp2f(m[2 * e]), __builtin_amdgcn_exp2f(m[2 * e + 1]));
          const unsigned mk = (le[2 * e] ? 0xffffu : 0u) | (le[2 * e + 1] ? 0xffff0000u : 0u); p8[e] = pw[e] & mk; p0[e] = pw[e] & ~mk; }
      pv(8, p8); if (!EDGE) pv(0, p0); }
#pragma unroll
    for (int t = 1; t < 8; ++t) if (!EDGE || t >= tmin) { const f32x16 s = score(t, cm); unsigned pw[8];
#pragma unroll
        for (int e = 0; e < 8; ++e) pw[e] = pk2(__builtin_amdgcn_exp2f(s[2 * e]), __builtin_amdgcn_exp2f(s[2 * e + 1]));
        pv(t, pw); }
}

__global__ __launch_bounds__(512, 2) void k2_attn(K2Args a) {
    extern __shared__ __attribute__((aligned(16))) char smem_[];
    LAS char* smem = (LAS char*)smem_;
    const unsigned lds0 = (unsigned)(size_t)smem_;
    char* ws = a.ws;
    const int tid = threadIdx.x, lane = tid & 63, w = __builtin_amdgcn_readfirstlane(tid >> 6), b = blockIdx.x;
    const int j = wg_j(b), r = wg_r(b);
    STAMP(ws, 3);
    TSDECL; TS(0);
    const int hl = w & 3, qh = w >> 2, l31 = lane & 31, hh = lane >> 5;
    const rsrc_t rk = mkrsrc(ws + OFF_QKV + (8 + j) * QKV_G, S * 144), rv = mkrsrc(ws + OFF_QKV + (16 + j) * QKV_G, S * 144);
    const int tok0 = tid / 9, mm = tid - 9 * tok0;
    v4u kk[6], vv[6];
#pragma unroll
    for (int m = 0; m < 6; ++m) { const int tok = tok0 + 56 * m, o_ = (64 * r - 256) * 144 + tid * 16 + m * 56 * 144;
        const int off = (tid < 504 && tok < 320 && o_ >= 0) ? o_ : 0x7ffffff0;
        kk[m] = bld128(rk, off, 0); vv[m] = bld128(rv, off, 0); }
    const char* qp = ws + OFF_QKV + j * QKV_G + (size_t)(64 * r + 32 * qh + l31) * 144 + 36 * hl;
    v4u q0, q1;
    { const GAS unsigned* qq = (const GAS unsigned*)(qp + 16 * hh); q0 = v4u{qq[0], qq[1], qq[2], qq[3]};
      const unsigned t = *(const GAS unsigned*)(qp + 32); q1 = v4u{hh == 0 ? t : 0u, 0u, 0u, 0u}; }
    const int js = UJ(b), ul = (b >> 3) * 8 + w;
    const Unit ua = unit_decode(a.w1, a.w2, a.wo, js, ul), ub = unit_decode(a.w1, a.w2, a.wo, js, 256 + ul);
    for (int n = tid; n < 1280; n += 512) { *(LAS unsigned*)(smem + K2_K + n * 40 + 36) = 0u; *(LAS unsigned*)(smem + K2_V + n * 40 + 36) = 0x3f80u; }
    int lo_[4];
#pragma unroll
    for (int e = 0; e < 4; ++e) { const int dw = 4 * mm + e, h = dw / 9; lo_[e] = (h * 320 + tok0) * 40 + (dw - 9 * h) * 4; }
    TS(1);
    if (tid < 504) {
#pragma unroll
        for (int m = 0; m < 6; ++m) if (m < 5 || tok0 + 280 < 320) {
#pragma unroll
            for (int e = 0; e < 4; ++e) { *(LAS unsigned*)(smem + K2_K + lo_[e] + m * 56 * 40) = kk[m][e]; *(LAS unsigned*)(smem + K2_V + lo_[e] + m * 56 * 40) = vv[m][e]; } }
    }
    unit_load(ua, lds0 + K2_SCR + w * 4096, lane);
    TS(3);
    LBAR();
    TS(4);
    STAMP(ws, 4);
    const rsrc_t rws = mkrsrc(ws, (unsigned)WS_END);
    f32x16 oacc;
    if (r < 4) attn_core<true>(smem, q0, q1, r, w, lane, oacc, ua, ub, rws, lds0);
    else attn_core<false>(smem, q0, q1, r, w, lane, oacc, ua, ub, rws, lds0);
    TS(7);
    const float inv = __builtin_amdgcn_rcpf(lo32(oacc[10]));
    LAS char* op = smem + K2_O + (32 * qh + l31) * 144 + 36 * hl + 8 * hh;
    *(LAS unsigned*)(op) = pk2(oacc[0] * inv, oacc[1] * inv); *(LAS unsigned*)(op + 4) = pk2(oacc[2] * inv, oacc[3] * inv);
    *(LAS unsigned*)(op + 16) = pk2(oacc[4] * inv, oacc[5] * inv); *(LAS unsigned*)(op + 20) = pk2(oacc[6] * inv, oacc[7] * inv);
    if (hh == 0) *(LAS unsigned*)(op + 32) = pk2(oacc[8] * inv, oacc[9] * inv);
    WAITV(0); unit_store(ub, smem + K2_SCR + w * 4096, rws, lane);
    LBAR();
    for (int n = tid; n < 576; n += 512) { const int i = n & 15, tt = (n >> 4) & 3, m = n >> 6;
        const v4u v = lds128(smem + K2_O + (16 * tt + i) * 144 + 16 * m);
        const int n8 = 72 * j + 8 * m, ks = n8 >> 5, g = (n8 & 31) >> 3;
        *(GAS v4u*)(ws + OFF_O + r * TILE_FR + (ks * 4 + tt) * 1024 + (g * 16 + i) * 16) = v; }
    STAMP(ws, 5);
    TS(8); TSW(9);
    TSDUMP(ws, 1);
}

constexpr int K3_LDS = 73728;
__global__ __launch_bounds__(512, 2) void k3_oproj(char* ws, const void* x) {
    extern __shared__ __attribute__((aligned(16))) char smem_[];
    LAS char* smem = (LAS char*)smem_;
    const unsigned lds0 = (unsigned)(size_t)smem_;
    const int tid = threadIdx.x, lane = tid & 63, w = __builtin_amdgcn_readfirstlane(tid >> 6), b = blockIdx.x;
    const int j = wg_j(b), r = wg_r(b), g = lane >> 4, i = lane & 15;
    STAMP(ws, 6);
    TSDECL; TS(0);
    const rsrc_t ro = mkrsrc(ws + OFF_O + r * TILE_FR, (unsigned)TILE_FR);
#pragma unroll
    for (int m = 0; m < 9; ++m) dma16(ro, lane * 16, (w * 9 + m) * 1024, lds0 + (w * 9 + m) * 1024);
    const int dt = w < 4 ? 4 * j + w : 32 + (j >> 1), tt0 = w < 4 ? 0 : 2 * (j & 1);
    const rsrc_t rw = mkrsrc(ws + OFF_WO, 36 * 18 * 1024), rx = mkrsrc(x, S * D * 2);
    v4u af[18]; v2u xr[4];
    if (w < 5) {
#pragma unroll
        for (int ks = 0; ks < 18; ++ks) af[ks] = bld128(rw, lane * 16 + (ks & 3) * 1024, dt * 18432 + (ks >> 2) * 4096);
#pragma unroll
        for (int u = 0; u < 4; ++u) xr[u] = bld64(rx, (64 * r + 16 * ((tt0 + u) & 3) + i) * (D * 2) + (16 * dt + 4 * g) * 2, 0);
    }
    if (w == 5) { const rsrc_t rc = mkrsrc(ws + OFF_CSP, 18 * FF * 4); const int col = 18 * b + (lane < 18 ? lane : 0); unsigned pc[18];
#pragma unroll
        for (int ks = 0; ks < 18; ++ks) pc[ks] = __builtin_amdgcn_raw_buffer_load_b32(rc, col * 4, ks * FF * 4, 0);
        SB();
        float s = 0.f;
#pragma unroll
        for (int ks = 0; ks < 18; ++ks) s += __builtin_bit_cast(float, pc[ks]);
        if (lane < 18) *(GAS float*)(ws + OFF_CS + col * 4) = s; }
    TS(1);
    WAITV(0); BAR();
    TS(2);
    auto fin = [&](f32x4 acc, v2u xv, int d_t, int tt) {
        const v2u o = {pk2(acc[0] + blo(xv.x), acc[1] + bhi(xv.x)), pk2(acc[2] + blo(xv.y), acc[3] + bhi(xv.y))};
        *(GAS v2u*)(ws + OFF_H + r * TILE_FR + ((d_t >> 1) * 4 + tt) * 1024 + ((2 * (d_t & 1) + (g >> 1)) * 16 + i) * 16 + (g & 1) * 8) = o; };
    if (w < 5) {
        f32x4 acc[4];
#pragma unroll
        for (int u = 0; u < 4; ++u) acc[u] = f32x4{0.f, 0.f, 0.f, 0.f};
        const LAS char* bp = smem + lane * 16;
        int to[4];
#pragma unroll
        for (int u = 0; u < 4; ++u) to[u] = ((tt0 + u) & 3) * 1024;
        v4u bf[2][2][4];
#pragma unroll
        for (int k = 0; k < 2; ++k)
#pragma unroll
            for (int u = 0; u < 4; ++u) bf[0][k][u] = lds128(bp + k * 4096 + to[u]);
#pragma unroll
        for (int gq = 0; gq < 9; ++gq) {
            if (gq < 8) {
#pragma unroll
                for (int k = 0; k < 2; ++k)
#pragma unroll
                    for (int u = 0; u < 4; ++u) bf[(gq + 1) & 1][k][u] = lds128(bp + (2 * gq + 2 + k) * 4096 + to[u]); }
            SB();
#pragma unroll
            for (int k = 0; k < 2; ++k)
#pragma unroll
                for (int u = 0; u < 4; ++u) acc[u] = mfma16(af[2 * gq + k], bf[gq & 1][k][u], acc[u]);
            SB();
        }
#pragma unroll
        for (int u = 0; u < 4; ++u) if (w < 4 || u < 2) fin(acc[u], xr[u], dt, (tt0 + u) & 3);
    }
    STAMP(ws, 7);
    TS(3); TSW(4);
    TSDUMP(ws, 2);
}

struct K4Args { char* ws; const void* b1; const void* b2; void* out; };
constexpr int K4_X = 0, K4_H = 73728, K4_ST = 147456 + 4096, K4_CNT = K4_ST + 1024, K4_LDS = K4_CNT + 64;

template <int T> DI void ffn_pass(f32x4 (&acc)[3][4], v4u (&ring)[18], rsrc_t rw, int pos, const LAS char* bp, int lane) {
    constexpr int U = 18 / T;
    v4u bf[4];
#pragma unroll
    for (int tt = 0; tt < 4; ++tt) bf[tt] = lds128(bp + tt * 1024);
#pragma nounroll
    for (int it = 0; it < T; ++it) {
#pragma unroll
        for (int u = 0; u < U; ++u) {
            v4u bn[4];
#pragma unroll
            for (int tt = 0; tt < 4; ++tt) bn[tt] = lds128(bp + ((u + 1) * 4 + tt) * 1024);
            SB();
#pragma unroll
            for (int t = 0; t < T; ++t) {
                const int n = u * T + t;
#pragma unroll
                for (int tt = 0; tt < 4; ++tt) acc[t][tt] = mfma16(ring[n], bf[tt], acc[t][tt]);
                ring[n] = bld128(rw, lane * 16 + (n & 3) * 1024, pos + (n >> 2) * 4096);
            }
            SB();
#pragma unroll
            for (int tt = 0; tt < 4; ++tt) bf[tt] = bn[tt];
        }
        bp += U * 4096; pos += 18 * 1024;
    }
#pragma unroll
    for (int t = 0; t < T; ++t)
#pragma unroll
        for (int tt = 0; tt < 4; ++tt) PIN(acc[t][tt]);
}

template <bool A> DI void ffn_body(const K4Args& a, LAS char* smem, unsigned lds0, int r, int j, int w, int lane) {
    const int g = lane >> 4, i = lane & 15, s = w & 3;
    TSDECL; TS(0);
    constexpr int NT = A ? 5 : 4;
    const int gl0 = A ? 0 : 5;
    f32x4 cs[NT]; v2u bb[NT];
#pragma unroll
    for (int t = 0; t < NT; ++t) { const int f = 576 * j + 16 * (4 * (gl0 + t) + s) + 4 * g;
        cs[t] = *(const GAS f32x4*)(a.ws + OFF_CS + f * 4); bb[t] = *(const GAS v2u*)((const char*)a.b1 + f * 2); }
    const rsrc_t rh = mkrsrc(a.ws + OFF_H + r * TILE_FR, (unsigned)TILE_FR);
#pragma unroll
    for (int m = 0; m < 9; ++m) dma16(rh, lane * 16, (w * 9 + m) * 1024, lds0 + K4_X + (w * 9 + m) * 1024);
    if (threadIdx.x == 0) *(LAS unsigned*)(smem + K4_CNT) = 0u;
    const rsrc_t rw = mkrsrc(a.ws + OFF_WS + j * WS_J + ffn_stream_base(w), (A ? 180 : 144) * 1024);
    v4u ring[18];
#pragma unroll
    for (int n = 0; n < 18; ++n) ring[n] = bld128(rw, lane * 16 + (n & 3) * 1024, (n >> 2) * 4096);
    TS(1);
    asm volatile("s_waitcnt vmcnt(18) lgkmcnt(0)\n\ts_barrier" ::: "memory");
    TS(2);
    LAS float* st = (LAS float*)(smem + K4_ST);
    { float s1 = 0.f, s2 = 0.f;
#pragma unroll
      for (int m = 0; m < 9; ++m) { const v4u v = lds128(smem + K4_X + ((9 * (w >> 2) + m) * 4 + (w & 3)) * 1024 + lane * 16);
#pragma unroll
          for (int e = 0; e < 4; ++e) { s1 = dot2(v[e], s1); s2 = dot2sq(v[e], s2); } }
      s1 = sum32(sum16(s1)); s2 = sum32(sum16(s2));
      if (lane < 16) { st[(((w >> 2) * 64) + 16 * (w & 3) + lane) * 2] = s1; st[(((w >> 2) * 64) + 16 * (w & 3) + lane) * 2 + 1] = s2; }
      asm volatile("s_waitcnt lgkmcnt(0)" ::: "memory");
      if (lane == 0) __hip_atomic_fetch_add((LAS unsigned*)(smem + K4_CNT), 1u, __ATOMIC_RELAXED, __HIP_MEMORY_SCOPE_WORKGROUP); }
    TS(3);
    STAMP(a.ws, 9);
    float mean[4], rstd[4];
    f32x4 acc[3][4];
    auto zero = [&]() {
#pragma unroll
        for (int t = 0; t < 3; ++t)
#pragma unroll
            for (int tt = 0; tt < 4; ++tt) acc[t][tt] = f32x4{0.f, 0.f, 0.f, 0.f}; };
    auto act = [&](int t, int tl) {
        const int ft = 4 * (gl0 + tl) + s;
        const f32x2 b01 = {blo(bb[tl].x), bhi(bb[tl].x)}, b23 = {blo(bb[tl].y), bhi(bb[tl].y)};
        const f32x2 c01 = {cs[tl][0], cs[tl][1]}, c23 = {cs[tl][2], cs[tl][3]};
#pragma unroll
        for (int tt = 0; tt < 4; ++tt) { const f32x2 m = {-mean[tt], -mean[tt]}, rs = {rstd[tt], rstd[tt]};
            const f32x2 z01 = fma2(rs, fma2(m, c01, f32x2{acc[t][tt][0], acc[t][tt][1]}), b01), z23 = fma2(rs, fma2(m, c23, f32x2{acc[t][tt][2], acc[t][tt][3]}), b23);
            *(LAS v2u*)(smem + K4_H + ((ft >> 1) * 4 + tt) * 1024 + lane * 16 + (ft & 1) * 8) = v2u{gelu2(z01), gelu2(z23)}; } };
    const LAS char* xp = smem + K4_X + lane * 16;
    const LAS char* hp = smem + K4_H + lane * 16;
    constexpr int T0 = A ? 3 : 2;
    zero();
    ffn_pass<T0>(acc, ring, rw, 18 * 1024, xp, lane);
    TS(4);
    while (*(volatile LAS unsigned*)(smem + K4_CNT) < 8u) { }
    asm volatile("" ::: "memory");
#pragma unroll
    for (int tt = 0; tt < 4; ++tt) { const int t = 16 * tt + i;
        const float s1 = st[t * 2] + st[(64 + t) * 2], s2 = st[t * 2 + 1] + st[(64 + t) * 2 + 1];
        mean[tt] = s1 * (1.0f / D);
        rstd[tt] = __builtin_amdgcn_rsqf(__builtin_fmaxf(__builtin_fmaf(-mean[tt], mean[tt], s2 * (1.0f / D)), 0.f) + LN_EPS); }
#pragma unroll
    for (int t = 0; t < T0; ++t) act(t, t);
    TS(5);
    zero();
    ffn_pass<2>(acc, ring, rw, (18 + 18 * T0) * 1024, xp, lane);
    TS(6);
    act(0, T0); act(1, T0 + 1);
    STAMP(a.ws, 10);
    LBAR();
    TS(7);
    STAMP(a.ws, 11);
    const rsrc_t rp = mkrsrc(a.ws + OFF_P + (size_t)r * 8 * P_SRC, 8 * P_SRC);
    constexpr int G2 = A ? 90 : 72;
#pragma unroll
    for (int ps = 0; ps < 2; ++ps) {
        zero();
        ffn_pass<2>(acc, ring, rw, (G2 + 36 * ps + 18) * 1024, hp, lane);
#pragma unroll
        for (int t = 0; t < 2; ++t) {
            const int gi = (A ? 0 : 4) + 2 * ps + t, dt = 4 * (gi < j ? gi : gi + 1) + s;
#pragma unroll
            for (int tp = 0; tp < 2; ++tp) {
                const v4u o = {pk2(acc[t][2 * tp][0], acc[t][2 * tp][1]), pk2(acc[t][2 * tp][2], acc[t][2 * tp][3]),
                               pk2(acc[t][2 * tp + 1][0], acc[t][2 * tp + 1][1]), pk2(acc[t][2 * tp + 1][2], acc[t][2 * tp + 1][3])};
                __builtin_amdgcn_raw_buffer_store_b128(o, rp, lane * 16, j * (int)P_SRC + (dt * 2 + tp) * 1024, 16);
            }
        }
    }
    TS(8);
    const int dt = A ? 4 * j + s : 32 + (j >> 1);
    const int d0 = 16 * dt + 4 * g;
    v2u hv[4], b2v = {0u, 0u};
    if (A || w == 4) {
        b2v = *(const GAS v2u*)((const char*)a.b2 + d0 * 2);
#pragma unroll
        for (int tt = 0; tt < 4; ++tt) hv[tt] = *(const GAS v2u*)(a.ws + OFF_H + r * TILE_FR + ((dt >> 1) * 4 + tt) * 1024 + ((2 * (dt & 1) + (g >> 1)) * 16 + i) * 16 + (g & 1) * 8);
    }
    if (A) { zero(); ffn_pass<1>(acc, ring, rw, 180 * 1024, hp, lane); }
    STAMP(a.ws, 12);
    asm volatile("s_waitcnt vmcnt(0)\n\ts_barrier" ::: "memory");
    TS(9);
    gu32* flag = (gu32*)(a.ws + OFF_FLAG + r * 256);
    if (threadIdx.x == 0) {
        __hip_atomic_fetch_add(flag, 1u, __ATOMIC_RELAXED, __HIP_MEMORY_SCOPE_AGENT);
        for (unsigned spins = 0; __hip_atomic_load(flag, __ATOMIC_RELAXED, __HIP_MEMORY_SCOPE_AGENT) < 8u && spins < (1u << 20); ++spins) __builtin_amdgcn_s_sleep(1);
        __builtin_amdgcn_fence(__ATOMIC_ACQUIRE, "wavefront");
    }
    __syncthreads();
    STAMP(a.ws, 13);
    TS(10);
    if (A || w == 4) {
        v4u pv[2][8];
#pragma unroll
        for (int tp = 0; tp < 2; ++tp)
#pragma unroll
            for (int src = 0; src < 8; ++src) pv[tp][src] = bld128_sc1(rp, lane * 16, (A ? src != j : tp == (j & 1)) ? src * (int)P_SRC + (dt * 2 + tp) * 1024 : 0x7fff0000);
        SB();
#pragma unroll
        for (int tp = 0; tp < 2; ++tp) {
            if (!A && tp != (j & 1)) continue;
#pragma unroll
            for (int u = 0; u < 2; ++u) { const int tt = 2 * tp + u;
                f32x4 o = {blo(hv[tt].x) + blo(b2v.x), bhi(hv[tt].x) + bhi(b2v.x), blo(hv[tt].y) + blo(b2v.y), bhi(hv[tt].y) + bhi(b2v.y)};
                if (A) o += acc[0][tt];
#pragma unroll
                for (int src = 0; src < 8; ++src) {
                    o[0] += blo(pv[tp][src][2 * u]); o[1] += bhi(pv[tp][src][2 * u]); o[2] += blo(pv[tp][src][2 * u + 1]); o[3] += bhi(pv[tp][src][2 * u + 1]); }
                *(GAS v2u*)((char*)a.out + (size_t)(64 * r + 16 * tt + i) * (D * 2) + d0 * 2) = v2u{pk2(o[0], o[1]), pk2(o[2], o[3])};
            }
        }
    }
    STAMP(a.ws, 14);
    TSW(11);
    TSDUMP(a.ws, 3);
}

__global__ __launch_bounds__(512, 2) void k4_ffn(K4Args a) {
    extern __shared__ __attribute__((aligned(16))) char smem_[];
    LAS char* smem = (LAS char*)smem_;
    const unsigned lds0 = (unsigned)(size_t)smem_;
    const int tid = threadIdx.x, lane = tid & 63, w = __builtin_amdgcn_readfirstlane(tid >> 6), b = blockIdx.x;
    STAMP(a.ws, 8);
    if (w < 4) ffn_body<true>(a, smem, lds0, wg_r(b), wg_j(b), w, lane);
    else ffn_body<false>(a, smem, lds0, wg_r(b), wg_j(b), w, lane);
}

extern "C" void kernel_launch(void* const* d_in, const int* in_sizes, int n_in, void* d_out, int out_size, void* d_ws, size_t ws_size, hipStream_t stream) {
    (void)in_sizes; (void)n_in; (void)out_size; (void)ws_size;
    char* ws = (char*)d_ws;
    (void)hipFuncSetAttribute((const void*)k1_qkv, hipFuncAttributeMaxDynamicSharedMemorySize, K1_LDS);
    (void)hipFuncSetAttribute((const void*)k2_attn, hipFuncAttributeMaxDynamicSharedMemorySize, K2_LDS);
    (void)hipFuncSetAttribute((const void*)k3_oproj, hipFuncAttributeMaxDynamicSharedMemorySize, K3_LDS);
    (void)hipFuncSetAttribute((const void*)k4_ffn, hipFuncAttributeMaxDynamicSharedMemorySize, K4_LDS);
    K1Args a1{d_in[0], d_in[1], ws};
    k1_qkv<<<224, 512, K1_LDS, stream>>>(a1);
    K2Args a2{ws, d_in[2], d_in[4], d_in[6]};
    k2_attn<<<256, 512, K2_LDS, stream>>>(a2);
    k3_oproj<<<256, 512, K3_LDS, stream>>>(ws, d_in[0]);
    K4Args a4{ws, d_in[5], d_in[7], d_out};
    k4_ffn<<<256, 512, K4_LDS, stream>>>(a4);
}
```

```cpp
#include <hip/hip_runtime.h>
#include <hip/hip_bf16.h>
#include <stdint.h>

#define GAS __attribute__((address_space(1)))
#define LAS __attribute__((address_space(3)))
#define DI __device__ __forceinline__
typedef unsigned v4u __attribute__((ext_vector_type(4)));
typedef unsigned v2u __attribute__((ext_vector_type(2)));
typedef float f32x4 __attribute__((ext_vector_type(4)));
typedef float f32x16 __attribute__((ext_vector_type(16)));
typedef short bf16x8 __attribute__((ext_vector_type(8)));
typedef __bf16 bf2 __attribute__((ext_vector_type(2)));
typedef GAS unsigned gu32;
typedef __amdgpu_buffer_rsrc_t rsrc_t;

#define STAMPS 0

constexpr int S = 2048, D = 576, FF = 4608, N3 = 3 * D;
constexpr int NR = 32;
constexpr float LN_EPS = 1e-5f;
constexpr float QSCALE = 0.23570226039551584f * 1.4426950408889634f;

constexpr size_t FRAG = 1024;
constexpr size_t OFF_FLAG = 0;
constexpr size_t OFF_STAMP = 16384;
constexpr size_t OFF_QKV = 65536;
constexpr size_t QKV_G = (size_t)S * 144;
constexpr int NSL = 8;
constexpr size_t OFF_WS = OFF_QKV + 24 * QKV_G;
constexpr size_t WS_J = (size_t)1296 * FRAG;
constexpr size_t OFF_WO = OFF_WS + NSL * WS_J;
constexpr size_t OFF_O = OFF_WO + (size_t)36 * 18 * FRAG;
constexpr size_t TT_FR = (size_t)18 * FRAG;
constexpr size_t OFF_H = OFF_O + 128 * TT_FR;
constexpr size_t OFF_P = OFF_H + 128 * TT_FR;
constexpr size_t P_SRC = (size_t)36 * 2 * FRAG;
constexpr size_t OFF_CSP = OFF_P + (size_t)NR * NSL * P_SRC;
constexpr size_t OFF_CS = OFF_CSP + (size_t)18 * FF * 4;
constexpr size_t WS_END = OFF_CS + (size_t)FF * 4;

__host__ __device__ constexpr int ffn_stream_base(int wv) { return (wv < 4 ? wv * 180 : 720 + (wv - 4) * 144) * 1024; }
__host__ __device__ constexpr int ffn_pos1(bool a, int t, int ks) { return ks * (a ? 5 : 4) + t; }
__host__ __device__ constexpr int ffn_pos2(bool a, int t, int ks) { return (a ? 90 : 72) + (t < 4 ? (t >> 1) * 36 + ks * 2 + (t & 1) : 72 + ks); }
DI int wg_r(int b) { return 8 * ((b & 7) >> 1) + ((b >> 3) & 7); }
DI int wg_j(int b) { return 4 * (b & 1) + (b >> 6); }

DI f32x4 mfma16(v4u a, v4u b, f32x4 c) { return __builtin_amdgcn_mfma_f32_16x16x32_bf16(__builtin_bit_cast(bf16x8, a), __builtin_bit_cast(bf16x8, b), c, 0, 0, 0); }
DI f32x16 mfma32(v4u a, v4u b, f32x16 c) { return __builtin_amdgcn_mfma_f32_32x32x16_bf16(__builtin_bit_cast(bf16x8, a), __builtin_bit_cast(bf16x8, b), c, 0, 0, 0); }
DI unsigned pk2(float a, float b) { bf2 v = {(__bf16)a, (__bf16)b}; return __builtin_bit_cast(unsigned, v); }
DI float blo(unsigned u) { return __builtin_bit_cast(float, u << 16); }
DI float bhi(unsigned u) { return __builtin_bit_cast(float, u & 0xffff0000u); }
DI rsrc_t mkrsrc(const void* p, unsigned bytes) { return __builtin_amdgcn_make_buffer_rsrc((void*)p, 0, bytes, 0x00020000); }
DI v4u bld128(rsrc_t r, int voff, int soff) { return __builtin_amdgcn_raw_buffer_load_b128(r, voff, soff, 0); }
DI v4u bld128_sc1(rsrc_t r, int voff, int soff) { return __builtin_amdgcn_raw_buffer_load_b128(r, voff, soff, 16); }
DI v2u bld64(rsrc_t r, int voff, int soff) { return __builtin_amdgcn_raw_buffer_load_b64(r, voff, soff, 0); }
DI v4u lds128(const LAS char* p) { return *(const LAS v4u*)p; }
DI v2u lds64(const LAS char* p) { return *(const LAS v2u*)p; }
typedef short v4i16_t __attribute__((ext_vector_type(4)));
DI v2u ldstr(const LAS char* p) { return __builtin_bit_cast(v2u, __builtin_amdgcn_ds_read_tr16_b64_v4i16((LAS v4i16_t*)p)); }
DI float dot2(unsigned a, float c) { return __builtin_amdgcn_fdot2_f32_bf16(__builtin_bit_cast(bf2, a), __builtin_bit_cast(bf2, 0x3f803f80u), c, false); }
DI float dot2sq(unsigned a, float c) { const bf2 v = __builtin_bit_cast(bf2, a); return __builtin_amdgcn_fdot2_f32_bf16(v, v, c, false); }
DI void dma16(rsrc_t r, int voff, int soff, unsigned lds) {
    asm volatile("s_mov_b32 m0, %0\n\ts_nop 0\n\tbuffer_load_dwordx4 %1, %2, %3 offen lds" :: "s"(lds), "v"(voff), "s"(r), "s"(soff) : "memory");
}
#define WAITV(n) asm volatile("s_waitcnt vmcnt(" #n ")" ::: "memory")
#define BAR() asm volatile("s_barrier" ::: "memory")
#define LBAR() asm volatile("s_waitcnt lgkmcnt(0)\n\ts_barrier" ::: "memory")
#define SB() __builtin_amdgcn_sched_barrier(0)
#define PIN(x) asm volatile("" : "+v"(x))
DI float sum16(float v) { const unsigned u = __builtin_bit_cast(unsigned, v); const auto r = __builtin_amdgcn_permlane16_swap(u, u, false, false);
    return __builtin_bit_cast(float, (unsigned)r[0]) + __builtin_bit_cast(float, (unsigned)r[1]); }
DI float sum32(float v) { const unsigned u = __builtin_bit_cast(unsigned, v); const auto r = __builtin_amdgcn_permlane32_swap(u, u, false, false);
    return __builtin_bit_cast(float, (unsigned)r[0]) + __builtin_bit_cast(float, (unsigned)r[1]); }
DI float max32(float v) { const unsigned u = __builtin_bit_cast(unsigned, v); const auto r = __builtin_amdgcn_permlane32_swap(u, u, false, false);
    return __builtin_fmaxf(__builtin_bit_cast(float, (unsigned)r[0]), __builtin_bit_cast(float, (unsigned)r[1])); }
DI float lo32(float v) { const unsigned u = __builtin_bit_cast(unsigned, v); const auto r = __builtin_amdgcn_permlane32_swap(u, u, false, false);
    return __builtin_bit_cast(float, (unsigned)r[0]); }
typedef float f32x2 __attribute__((ext_vector_type(2)));
DI void ln_gelu4(f32x4 acc, f32x4 cs, float b0, float b1, float b2, float b3, float nmean, float rs, unsigned& o01, unsigned& o23) {
    float z0, z1, z2, z3, t0, t1, t2, t3;
    asm("v_fma_f32 %0, %16, %12, %8\n\tv_fma_f32 %1, %16, %13, %9\n\tv_fma_f32 %2, %16, %14, %10\n\tv_fma_f32 %3, %16, %15, %11\n\t"
        "v_fma_f32 %0, %17, %0, %18\n\tv_fma_f32 %1, %17, %1, %19\n\tv_fma_f32 %2, %17, %2, %20\n\tv_fma_f32 %3, %17, %3, %21\n\t"
        "v_mul_f32 %4, %0, %0\n\tv_mul_f32 %5, %1, %1\n\tv_mul_f32 %6, %2, %2\n\tv_mul_f32 %7, %3, %3\n\t"
        "v_fmaak_f32 %4, %22, %4, 0xc0135761\n\tv_fmaak_f32 %5, %22, %5, 0xc0135761\n\tv_fmaak_f32 %6, %22, %6, 0xc0135761\n\tv_fmaak_f32 %7, %22, %7, 0xc0135761\n\t"
        "v_mul_f32 %4, %0, %4\n\tv_mul_f32 %5, %1, %5\n\tv_mul_f32 %6, %2, %6\n\tv_mul_f32 %7, %3, %7\n\t"
        "v_exp_f32 %4, %4\n\tv_exp_f32 %5, %5\n\tv_exp_f32 %6, %6\n\tv_exp_f32 %7, %7\n\t"
        "v_add_f32 %4, 1.0, %4\n\tv_add_f32 %5, 1.0, %5\n\tv_add_f32 %6, 1.0, %6\n\tv_add_f32 %7, 1.0, %7\n\t"
        "v_rcp_f32 %4, %4\n\tv_rcp_f32 %5, %5\n\tv_rcp_f32 %6, %6\n\tv_rcp_f32 %7, %7\n\t"
        "v_mul_f32 %0, %0, %4\n\tv_mul_f32 %1, %1, %5\n\tv_mul_f32 %2, %2, %6\n\tv_mul_f32 %3, %3, %7"
        : "=&v"(z0), "=&v"(z1), "=&v"(z2), "=&v"(z3), "=&v"(t0), "=&v"(t1), "=&v"(t2), "=&v"(t3)
        : "v"(acc[0]), "v"(acc[1]), "v"(acc[2]), "v"(acc[3]), "v"(cs[0]), "v"(cs[1]), "v"(cs[2]), "v"(cs[3]), "v"(nmean), "v"(rs), "v"(b0), "v"(b1), "v"(b2), "v"(b3),
          "v"(-0.1029432397f));
    o01 = pk2(z0, z1); o23 = pk2(z2, z3);
}
#define STAMP(ws, i) do { } while (0)
#define TS(n) do { } while (0)
#define TSW(n) do { } while (0)
#define TSDECL
#define TSDUMP(ws, kidx) do { } while (0)

struct K1Args { const void* x; const void* wqkv; char* ws; };
constexpr int K1_STAGE = 32768;
constexpr int K1_TAB = 4 * K1_STAGE;
constexpr int K1_LDS = K1_TAB + 4096;

__global__ __launch_bounds__(512, 2) void k1_qkv(K1Args a) {
    extern __shared__ __attribute__((aligned(16))) char smem_[];
    LAS char* smem = (LAS char*)smem_;
    const unsigned lds0 = (unsigned)(size_t)smem_;
    const int tid = threadIdx.x, lane = tid & 63, w = __builtin_amdgcn_readfirstlane(tid >> 6), b = blockIdx.x;
    STAMP(a.ws, 0);
    TSDECL; TS(0);
    if (b == 0 && tid < 32) *(GAS unsigned*)(a.ws + OFF_FLAG + tid * 256) = 0u;
    const int xc = b & 7, idx = b >> 3;
    const int rt = 4 * (xc >> 1) + (idx & 3), ct = 7 * (xc & 1) + (idx >> 2);
    if (w >= 4) {
        const int lw = w - 4;
        const rsrc_t rx = mkrsrc(a.x, S * D * 2), rw = mkrsrc(a.wqkv, D * N3 * 2);
        const int vx0 = (128 * rt + 32 * lw + (lane >> 3)) * (D * 2) + ((lane & 7) ^ (lane >> 4)) * 16, vx1 = vx0 ^ 64;
        const int c16 = lane & 15, q = lane >> 4;
        const int vw0 = (16 * lw + q) * (N3 * 2) + 256 * ct + ((((c16 >> 1) ^ (q << 1)) << 1) | (c16 & 1)) * 16;
        const int vw1 = (16 * lw + q) * (N3 * 2) + 256 * ct + ((((c16 >> 1) ^ ((q << 1) | 1)) << 1) | (c16 & 1)) * 16;
        auto stage = [&](int s) {
            const unsigned sb = lds0 + (s & 3) * K1_STAGE;
#pragma unroll
            for (int m = 0; m < 4; ++m) {
                dma16(rw, m < 2 ? vw0 : vw1, (64 * s + 4 * m) * (N3 * 2), sb + 16384 + (4 * lw + m) * 1024);
                dma16(rx, (m & 1) ? vx1 : vx0, 128 * s + m * 8 * (D * 2), sb + (4 * lw + m) * 1024);
            }
        };
        const int g = lane >> 4, i = lane & 15, q4 = i >> 2, p = lane & 3;
        const int sxo = (32 * lw + i) * 128, swo = 16384 + (8 * g + q4) * 256 + 8 * p, key = (q4 << 1) | (g & 1);
        float sx[2] = {0.f, 0.f}, sq[2] = {0.f, 0.f}, cs[2] = {0.f, 0.f};
        auto stats = [&](int s) {
            const LAS char* sb = smem + (s & 3) * K1_STAGE;
#pragma unroll
            for (int k2 = 0; k2 < 2; ++k2)
#pragma unroll
                for (int u = 0; u < 2; ++u) {
                    const v4u xv = lds128(sb + sxo + u * 2048 + ((4 * k2 + g) ^ ((i >> 1) & 7)) * 16);
                    const LAS char* ap = sb + swo + k2 * 8192 + (((2 * lw + u) ^ key) * 32);
                    const v2u lo = ldstr(ap), hi = ldstr(ap + 1024);
#pragma unroll
                    for (int e = 0; e < 4; ++e) { sx[u] = dot2(xv[e], sx[u]); sq[u] = dot2sq(xv[e], sq[u]); }
                    cs[u] = dot2(hi.y, dot2(hi.x, dot2(lo.y, dot2(lo.x, cs[u]))));
                }
        };
        stage(0); stage(1); stage(2);
        WAITV(16); TS(1); BAR();
        WAITV(8); BAR();
#pragma unroll
        for (int s = 3; s < 9; ++s) { stage(s); stats(s - 3); WAITV(8); asm volatile("s_waitcnt lgkmcnt(0)" ::: "memory"); TS(s - 1); BAR(); }
        stats(6); WAITV(0); BAR();
        stats(7); stats(8);
        LAS float* tabx = (LAS float*)(smem + K1_TAB);
        LAS float* tabc = tabx + 256;
#pragma unroll
        for (int u = 0; u < 2; ++u) { const float s1 = sum32(sum16(sx[u])), s2 = sum32(sum16(sq[u])), s3 = sum32(sum16(cs[u]));
            if (g == 0) { const float mean = s1 * (1.0f / D), var = __builtin_fmaxf(__builtin_fmaf(-mean, mean, s2 * (1.0f / D)), 0.f);
                tabx[(32 * lw + 16 * u + i) * 2] = mean; tabx[(32 * lw + 16 * u + i) * 2 + 1] = __builtin_amdgcn_rsqf(var + LN_EPS); tabc[32 * lw + 16 * u + i] = s3; } }
        LBAR();
        TS(9);
        TSDUMP(a.ws, 0);
        return;
    }
    const int th = w & 1, ch = w >> 1, h = lane >> 5, l31 = lane & 31;
    const int q = (lane & 15) >> 2, p = lane & 3, blk = (lane >> 4) & 1;
    const int xrow = (64 * th + l31) * 128, xkey = (l31 >> 1) & 7;
    const int woff = 16384 + (8 * h + q) * 256 + (((4 * ch + blk) ^ ((q << 1) | h)) * 32) + 8 * p;
    f32x16 acc[2][2];
#pragma unroll
    for (int cx = 0; cx < 2; ++cx)
#pragma unroll
        for (int ty = 0; ty < 2; ++ty)
#pragma unroll
            for (int e = 0; e < 16; ++e) acc[cx][ty][e] = 0.f;
    int xo[4];
#pragma unroll
    for (int k4 = 0; k4 < 4; ++k4) xo[k4] = xrow + ((2 * k4 + h) ^ xkey) * 16;
    v4u bA[2][2], aA[2][2], bB[2][2], aB[2][2];
    auto rd = [&](v4u (&bf)[2][2], v4u (&af)[2][2], const LAS char* sb, int k2) {
#pragma unroll
        for (int u = 0; u < 2; ++u) {
#pragma unroll
            for (int cx = 0; cx < 2; ++cx) { const LAS char* ap = sb + ((woff ^ (cx << 6)) + (2 * k2 + u) * 4096);
                const v2u lo = ldstr(ap), hi = ldstr(ap + 1024); af[u][cx] = v4u{lo.x, lo.y, hi.x, hi.y}; }
#pragma unroll
            for (int ty = 0; ty < 2; ++ty) bf[u][ty] = lds128(sb + xo[2 * k2 + u] + ty * 4096);
        }
    };
    auto mm = [&](const v4u (&bf)[2][2], const v4u (&af)[2][2]) {
#pragma unroll
        for (int u = 0; u < 2; ++u)
#pragma unroll
            for (int cx = 0; cx < 2; ++cx)
#pragma unroll
                for (int ty = 0; ty < 2; ++ty) acc[cx][ty] = mfma32(af[u][cx], bf[u][ty], acc[cx][ty]);
    };
    int dst[2][2];
#pragma unroll
    for (int cx = 0; cx < 2; ++cx)
#pragma unroll
        for (int m = 0; m < 2; ++m) { const int n8 = 128 * ct + 64 * ch + 32 * cx + 16 * m + 8 * h, ja = n8 / 72;
            dst[cx][m] = n8 < N3 ? (int)OFF_QKV + ja * (int)QKV_G + (128 * rt + 64 * th + l31) * 144 + (n8 - 72 * ja) * 2 : 0x7fff0000; }
    BAR();
    TS(1);
    rd(bA, aA, smem, 0);
    for (int s = 0; s < 9; ++s) {
        const LAS char* sb = smem + (s & 3) * K1_STAGE;
        rd(bB, aB, sb, 1);
        SB();
        mm(bA, aA);
        SB();
        if (s == 0) BAR();
        if (s < 8) rd(bA, aA, smem + ((s + 1) & 3) * K1_STAGE, 0);
        SB();
        mm(bB, aB);
        SB();
        if (s < 7) { asm volatile("s_waitcnt lgkmcnt(12)\n\ts_barrier" ::: "memory"); }
    }
    TS(4);
    LBAR();
    const LAS float* tabx = (const LAS float*)(smem + K1_TAB);
    const LAS float* tabc = tabx + 256;
    TS(5);
    const rsrc_t rws = mkrsrc(a.ws, (unsigned)OFF_WS);
#pragma unroll
    for (int ty = 0; ty < 2; ++ty) {
        const int t = 64 * th + 32 * ty + l31;
        const float mean = tabx[t * 2], rstd = tabx[t * 2 + 1];
#pragma unroll
        for (int cx = 0; cx < 2; ++cx) {
            const float rs = rstd * (128 * ct + 64 * ch + 32 * cx < D ? QSCALE : 1.0f);
            unsigned pk[8];
#pragma unroll
            for (int qd = 0; qd < 4; ++qd) { const f32x4 c = *(const LAS f32x4*)(tabc + 64 * ch + 32 * cx + 8 * qd + 4 * h);
                pk[2 * qd] = pk2(rs * __builtin_fmaf(-mean, c[0], acc[cx][ty][4 * qd]), rs * __builtin_fmaf(-mean, c[1], acc[cx][ty][4 * qd + 1]));
                pk[2 * qd + 1] = pk2(rs * __builtin_fmaf(-mean, c[2], acc[cx][ty][4 * qd + 2]), rs * __builtin_fmaf(-mean, c[3], acc[cx][ty][4 * qd + 3])); }
#pragma unroll
            for (int m = 0; m < 2; ++m) {
                const auto s0 = __builtin_amdgcn_permlane32_swap(pk[4 * m], pk[4 * m + 2], false, false), s1 = __builtin_amdgcn_permlane32_swap(pk[4 * m + 1], pk[4 * m + 3], false, false);
                __builtin_amdgcn_raw_buffer_store_b128(v4u{(unsigned)s0[0], (unsigned)s1[0], (unsigned)s0[1], (unsigned)s1[1]}, rws, dst[cx][m], ty * 32 * 144, 0); }
        }
    }
    STAMP(a.ws, 2);
    TS(6); TSW(7);
    TSDUMP(a.ws, 0);
}

struct Unit { rsrc_t rs; int soff, ld, w2; int dst[4]; int cs; };
constexpr int NUNIT = 1296 + 1296 + 162;
struct UnitTab { int v[NUNIT + 1][8]; };
constexpr UnitTab make_units() {
    UnitTab T{};
    for (int u = 0; u <= NUNIT; ++u) {
        const bool isw1 = u < 1296, isw2 = !isw1 && u < 2592, iswo = u >= 2592 && u < NUNIT;
        const int v = isw1 ? u : isw2 ? u - 1296 : u - 2592, j = v / 162, vl = v - 162 * j;
        const int ks = vl % 18, G = vl / 18;
        T.v[u][0] = isw1 ? 0 : isw2 ? 1 : iswo ? 2 : 3;
        T.v[u][1] = isw1 ? (32 * ks * FF + 576 * j + 64 * G) * 2 : ((isw2 ? 576 * j : 0) + 32 * ks) * (D * 2) + 128 * G;
        const int base = (int)OFF_WS + j * (int)WS_J, gi = G < j ? G : G - 1;
        const bool A = isw1 ? G < 5 : (G == j || gi < 4);
        const int fp = isw1 ? ffn_pos1(A, A ? G : G - 5, ks) : ffn_pos2(A, G == j ? 4 : gi & 3, ks);
        for (int c = 0; c < 4; ++c)
            T.v[u][2 + c] = (isw1 || isw2) ? base + ffn_stream_base(A ? c : 4 + c) + fp * 1024 : iswo ? (int)OFF_WO + ((4 * G + c) * 18 + ks) * 1024 : 0x7fff0000;
        T.v[u][6] = isw1 ? (int)OFF_CSP + (ks * FF + 576 * j + 64 * G) * 4 : 0x7fff0000;
        T.v[u][7] = 0;
    }
    return T;
}
__device__ const UnitTab UNITS = make_units();
DI Unit unit_decode(const void* w1, const void* w2, const void* wo, int u) {
    const int* e = UNITS.v[u < NUNIT ? u : NUNIT];
    const int kind = e[0];
    Unit r;
    r.rs = mkrsrc(kind == 0 ? w1 : kind == 1 ? w2 : wo, kind == 3 ? 0u : kind == 2 ? D * D * 2 : D * FF * 2);
    r.ld = kind == 0 ? FF * 2 : D * 2; r.w2 = kind == 1; r.soff = e[1];
#pragma unroll
    for (int c = 0; c < 4; ++c) r.dst[c] = e[2 + c];
    r.cs = e[6];
    return r;
}
#define UST 16
DI void unit_load(const Unit& t, unsigned scr, int lane) {
    const int voff = (lane >> 3) * t.ld + (lane & 7) * 16;
#pragma unroll
    for (int m = 0; m < 4; ++m) dma16(t.rs, voff, t.soff + 8 * m * t.ld, scr + m * 1024);
}
DI void unit_store(const Unit& t, const LAS char* scr, rsrc_t rws, int lane) {
    const int g = lane >> 4, q = (lane & 15) >> 2, p = lane & 3;
    const int row0 = t.w2 ? 4 * g + q : 8 * g + q, rstep = t.w2 ? 16 : 4;
    float cs[4];
#pragma unroll
    for (int c = 0; c < 4; ++c) {
        const v2u lo = ldstr(scr + row0 * 128 + (16 * c + 4 * p) * 2);
        const v2u hi = ldstr(scr + (row0 + rstep) * 128 + (16 * c + 4 * p) * 2);
        __builtin_amdgcn_raw_buffer_store_b128(v4u{lo.x, lo.y, hi.x, hi.y}, rws, lane * 16, t.dst[c], UST);
        cs[c] = sum32(sum16(dot2(hi.y, dot2(hi.x, dot2(lo.y, dot2(lo.x, 0.f))))));
    }
    const float mine = g == 0 ? cs[0] : g == 1 ? cs[1] : g == 2 ? cs[2] : cs[3];
    __builtin_amdgcn_raw_buffer_store_b32(__builtin_bit_cast(unsigned, mine), rws, lane * 4, t.cs, 16);
}

struct K2Args { char* ws; const void* wo; const void* w1; const void* w2; };
constexpr int KV_HEAD = 320 * 40;
constexpr int K2_K = 0, K2_V = 4 * KV_HEAD, K2_O = 8 * KV_HEAD, K2_SCR = K2_O + 64 * 144, K2_LDS = K2_SCR + 8 * 4096;

template <bool EDGE> DI void attn_core(LAS char* smem, const v4u q0, const v4u q1, int r, int w, int lane, f32x16& oacc, const Unit& ua, const Unit& ub, rsrc_t rws, unsigned lds0) {
    const int hl = w & 3, qh = w >> 2, l31 = lane & 31, hh = lane >> 5;
    const int tmin = EDGE ? ((256 - 64 * r - 32 * qh) >> 5) : 0;
    const float ninf = -__builtin_inff();
    const LAS char* kp = smem + K2_K + (hl * 320 + 32 * qh + l31) * 40;
    v4u ka[9], kb[9];
#pragma unroll
    for (int t = 0; t < 9; ++t) { const v2u a0 = lds64(kp + t * 1280 + 16 * hh), a1 = lds64(kp + t * 1280 + 16 * hh + 8);
        ka[t] = v4u{a0.x, a0.y, a1.x, a1.y};
        const v2u c0 = lds64(kp + t * 1280 + 32), c1 = lds64(kp + t * 1280 + 24);
        kb[t] = v4u{c0.x, c0.y, c1.x, c1.y}; }
    auto score = [&](int t, const f32x16& c) { return mfma32(kb[t], q1, mfma32(ka[t], q0, c)); };
    bool le[16];
#pragma unroll
    for (int rg = 0; rg < 16; ++rg) le[rg] = (rg & 3) + 8 * (rg >> 2) + 4 * hh <= l31;
    auto merged = [&](const f32x16& c) { const f32x16 s8 = score(8, c); f32x16 s0;
        if (EDGE) { s0 = s8;
#pragma unroll
            for (int rg = 0; rg < 16; ++rg) s0[rg] = ninf; } else s0 = score(0, c);
        f32x16 m;
#pragma unroll
        for (int rg = 0; rg < 16; ++rg) m[rg] = le[rg] ? s8[rg] : s0[rg];
        return m; };
    const f32x16 zero = {};
    float mx;
    { const f32x16 m = merged(zero); mx = __builtin_fmaxf(m[0], m[1]);
#pragma unroll
      for (int rg = 2; rg < 16; rg += 2) mx = __builtin_fmaxf(__builtin_fmaxf(mx, m[rg]), m[rg + 1]); }
#pragma unroll
    for (int t = 1; t < 8; ++t) if (!EDGE || t >= tmin) { const f32x16 s = score(t, zero);
#pragma unroll
        for (int rg = 0; rg < 16; rg += 2) mx = __builtin_fmaxf(__builtin_fmaxf(mx, s[rg]), s[rg + 1]); }
    mx = max32(mx);
    WAITV(0); unit_store(ua, smem + K2_SCR + w * 4096, rws, lane); unit_load(ub, lds0 + K2_SCR + w * 4096, lane);
    f32x16 cm;
#pragma unroll
    for (int rg = 0; rg < 16; ++rg) cm[rg] = -mx;
    const LAS char* vp = smem + K2_V + (hl * 320 + 32 * qh + 4 * hh + ((lane & 15) >> 2)) * 40 + (((lane >> 4) & 1) ? 32 : 8 * (lane & 3));
    auto pv = [&](int t, const unsigned (&pw)[8]) {
#pragma unroll
        for (int s = 0; s < 2; ++s) {
            const v2u lo = ldstr(vp + (t * 32 + 16 * s) * 40), hi = ldstr(vp + (t * 32 + 16 * s + 8) * 40);
            oacc = mfma32(v4u{lo.x, lo.y, hi.x, hi.y}, v4u{pw[4 * s], pw[4 * s + 1], pw[4 * s + 2], pw[4 * s + 3]}, oacc);
        } };
    oacc = zero;
    { const f32x16 m = merged(cm); unsigned pw[8], p8[8], p0[8];
#pragma unroll
      for (int e = 0; e < 8; ++e) { pw[e] = pk2(__builtin_amdgcn_exp2f(m[2 * e]), __builtin_amdgcn_exp2f(m[2 * e + 1]));
          const unsigned mk = (le[2 * e] ? 0xffffu : 0u) | (le[2 * e + 1] ? 0xffff0000u : 0u); p8[e] = pw[e] & mk; p0[e] = pw[e] & ~mk; }
      pv(8, p8); if (!EDGE) pv(0, p0); }
#pragma unroll
    for (int t = 1; t < 8; ++t) if (!EDGE || t >= tmin) { const f32x16 s = score(t, cm); unsigned pw[8];
#pragma unroll
        for (int e = 0; e < 8; ++e) pw[e] = pk2(__builtin_amdgcn_exp2f(s[2 * e]), __builtin_amdgcn_exp2f(s[2 * e + 1]));
        pv(t, pw); }
}

__global__ __launch_bounds__(512, 2) void k2_attn(K2Args a) {
    extern __shared__ __attribute__((aligned(16))) char smem_[];
    LAS char* smem = (LAS char*)smem_;
    const unsigned lds0 = (unsigned)(size_t)smem_;
    char* ws = a.ws;
    const int tid = threadIdx.x, lane = tid & 63, w = __builtin_amdgcn_readfirstlane(tid >> 6), b = blockIdx.x;
    const int j = wg_j(b), r = wg_r(b);
    STAMP(ws, 3);
    TSDECL; TS(0);
    const int hl = w & 3, qh = w >> 2, l31 = lane & 31, hh = lane >> 5;
    const rsrc_t rk = mkrsrc(ws + OFF_QKV + (8 + j) * QKV_G, S * 144), rv = mkrsrc(ws + OFF_QKV + (16 + j) * QKV_G, S * 144);
    const int tok0 = tid / 9, mm = tid - 9 * tok0;
    v4u kk[6], vv[6];
#pragma unroll
    for (int m = 0; m < 6; ++m) { const int tok = tok0 + 56 * m, o_ = (64 * r - 256) * 144 + tid * 16 + m * 56 * 144;
        const int off = (tid < 504 && tok < 320 && o_ >= 0) ? o_ : 0x7ffffff0;
        kk[m] = bld128(rk, off, 0); vv[m] = bld128(rv, off, 0); }
    const char* qp = ws + OFF_QKV + j * QKV_G + (size_t)(64 * r + 32 * qh + l31) * 144 + 36 * hl;
    v4u q0, q1;
    { const GAS unsigned* qq = (const GAS unsigned*)(qp + 16 * hh); q0 = v4u{qq[0], qq[1], qq[2], qq[3]};
      const unsigned t = *(const GAS unsigned*)(qp + 32); q1 = v4u{hh == 0 ? t : 0u, 0u, 0u, 0u}; }
    const Unit ua = unit_decode(a.w1, a.w2, a.wo, b * 8 + w), ub = unit_decode(a.w1, a.w2, a.wo, 2048 + b * 8 + w);
    for (int n = tid; n < 1280; n += 512) { *(LAS unsigned*)(smem + K2_K + n * 40 + 36) = 0u; *(LAS unsigned*)(smem + K2_V + n * 40 + 36) = 0x3f80u; }
    int lo_[4];
#pragma unroll
    for (int e = 0; e < 4; ++e) { const int dw = 4 * mm + e, h = dw / 9; lo_[e] = (h * 320 + tok0) * 40 + (dw - 9 * h) * 4; }
    TS(1);
    if (tid < 504) {
#pragma unroll
        for (int m = 0; m < 6; ++m) if (m < 5 || tok0 + 280 < 320) {
#pragma unroll
            for (int e = 0; e < 4; ++e) { *(LAS unsigned*)(smem + K2_K + lo_[e] + m * 56 * 40) = kk[m][e]; *(LAS unsigned*)(smem + K2_V + lo_[e] + m * 56 * 40) = vv[m][e]; } }
    }
    unit_load(ua, lds0 + K2_SCR + w * 4096, lane);
    TS(3);
    LBAR();
    TS(4);
    STAMP(ws, 4);
    const rsrc_t rws = mkrsrc(ws, (unsigned)WS_END);
    f32x16 oacc;
    if (r < 4) attn_core<true>(smem, q0, q1, r, w, lane, oacc, ua, ub, rws, lds0);
    else attn_core<false>(smem, q0, q1, r, w, lane, oacc, ua, ub, rws, lds0);
    TS(7);
    const float inv = __builtin_amdgcn_rcpf(lo32(oacc[10]));
    LAS char* op = smem + K2_O + (32 * qh + l31) * 144 + 36 * hl + 8 * hh;
    *(LAS unsigned*)(op) = pk2(oacc[0] * inv, oacc[1] * inv); *(LAS unsigned*)(op + 4) = pk2(oacc[2] * inv, oacc[3] * inv);
    *(LAS unsigned*)(op + 16) = pk2(oacc[4] * inv, oacc[5] * inv); *(LAS unsigned*)(op + 20) = pk2(oacc[6] * inv, oacc[7] * inv);
    if (hh == 0) *(LAS unsigned*)(op + 32) = pk2(oacc[8] * inv, oacc[9] * inv);
    WAITV(0); unit_store(ub, smem + K2_SCR + w * 4096, rws, lane);
    LBAR();
    for (int n = tid; n < 576; n += 512) { const int i = n & 15, tt = (n >> 4) & 3, m = n >> 6;
        const v4u v = lds128(smem + K2_O + (16 * tt + i) * 144 + 16 * m);
        const int n8 = 72 * j + 8 * m, ks = n8 >> 5, g = (n8 & 31) >> 3;
        *(GAS v4u*)(ws + OFF_O + (4 * r + tt) * TT_FR + ks * 1024 + (g * 16 + i) * 16) = v; }
    STAMP(ws, 5);
    TS(8); TSW(9);
    TSDUMP(ws, 1);
}

constexpr int K3_LDS = 73728;
__global__ __launch_bounds__(512, 2) void k3_oproj(char* ws, const void* x) {
    extern __shared__ __attribute__((aligned(16))) char smem_[];
    LAS char* smem = (LAS char*)smem_;
    const unsigned lds0 = (unsigned)(size_t)smem_;
    const int tid = threadIdx.x, lane = tid & 63, w = __builtin_amdgcn_readfirstlane(tid >> 6), b = blockIdx.x;
    const int j = wg_j(b), r = wg_r(b), g = lane >> 4, i = lane & 15;
    STAMP(ws, 6);
    TSDECL; TS(0);
    const rsrc_t ro = mkrsrc(ws + OFF_O + 4 * r * TT_FR, 4 * (unsigned)TT_FR);
#pragma unroll
    for (int m = 0; m < 9; ++m) dma16(ro, lane * 16, (w * 9 + m) * 1024, lds0 + (w * 9 + m) * 1024);
    const int dt = w < 4 ? 4 * j + w : 32 + (j >> 1), tt0 = w < 4 ? 0 : 2 * (j & 1);
    const rsrc_t rw = mkrsrc(ws + OFF_WO, 36 * 18 * 1024), rx = mkrsrc(x, S * D * 2);
    v4u af[18]; v2u xr[4];
    if (w < 5) {
#pragma unroll
        for (int ks = 0; ks < 18; ++ks) af[ks] = bld128(rw, lane * 16 + (ks & 3) * 1024, dt * 18432 + (ks >> 2) * 4096);
#pragma unroll
        for (int u = 0; u < 4; ++u) xr[u] = bld64(rx, (64 * r + 16 * ((tt0 + u) & 3) + i) * (D * 2) + (16 * dt + 4 * g) * 2, 0);
    }
    if (w == 5) { const rsrc_t rc = mkrsrc(ws + OFF_CSP, 18 * FF * 4); const int col = 18 * b + (lane < 18 ? lane : 0); unsigned pc[18];
#pragma unroll
        for (int ks = 0; ks < 18; ++ks) pc[ks] = __builtin_amdgcn_raw_buffer_load_b32(rc, col * 4, ks * FF * 4, 0);
        SB();
        float s = 0.f;
#pragma unroll
        for (int ks = 0; ks < 18; ++ks) s += __builtin_bit_cast(float, pc[ks]);
        if (lane < 18) *(GAS float*)(ws + OFF_CS + col * 4) = s; }
    TS(1);
    WAITV(0); BAR();
    TS(2);
    auto fin = [&](f32x4 acc, v2u xv, int d_t, int tt) {
        const v2u o = {pk2(acc[0] + blo(xv.x), acc[1] + bhi(xv.x)), pk2(acc[2] + blo(xv.y), acc[3] + bhi(xv.y))};
        *(GAS v2u*)(ws + OFF_H + (4 * r + tt) * TT_FR + (d_t >> 1) * 1024 + ((2 * (d_t & 1) + (g >> 1)) * 16 + i) * 16 + (g & 1) * 8) = o; };
    if (w < 5) {
        f32x4 acc[4];
#pragma unroll
        for (int u = 0; u < 4; ++u) acc[u] = f32x4{0.f, 0.f, 0.f, 0.f};
        const LAS char* bp = smem + lane * 16;
        int to[4];
#pragma unroll
        for (int u = 0; u < 4; ++u) to[u] = ((tt0 + u) & 3) * 18432;
        v4u bf[2][2][4];
#pragma unroll
        for (int k = 0; k < 2; ++k)
#pragma unroll
            for (int u = 0; u < 4; ++u) bf[0][k][u] = lds128(bp + k * 1024 + to[u]);
#pragma unroll
        for (int gq = 0; gq < 9; ++gq) {
            if (gq < 8) {
#pragma unroll
                for (int k = 0; k < 2; ++k)
#pragma unroll
                    for (int u = 0; u < 4; ++u) bf[(gq + 1) & 1][k][u] = lds128(bp + (2 * gq + 2 + k) * 1024 + to[u]); }
            SB();
#pragma unroll
            for (int k = 0; k < 2; ++k)
#pragma unroll
                for (int u = 0; u < 4; ++u) acc[u] = mfma16(af[2 * gq + k], bf[gq & 1][k][u], acc[u]);
            SB();
        }
#pragma unroll
        for (int u = 0; u < 4; ++u) if (w < 4 || u < 2) fin(acc[u], xr[u], dt, (tt0 + u) & 3);
    }
    STAMP(ws, 7);
    TS(3); TSW(4);
    TSDUMP(ws, 2);
}

struct K4Args { char* ws; const void* b1; const void* b2; void* out; };
constexpr int K4_X = 0, K4_H = 73728;
constexpr int K4_ST = 147456, K4_CNT = K4_ST + 512, K4_LDS = K4_CNT + 64;
#define RD 16

template <int T, int OFS, class Side>
DI void ffn_pass(f32x4 (&acc)[5][4], v4u (&ring)[RD], rsrc_t rw, int pos, const LAS char* bp, int lane, Side side) {
    v4u bf[4];
#pragma unroll
    for (int tt = 0; tt < 4; ++tt) bf[tt] = lds128(bp + tt * 18432);
#pragma unroll
    for (int t = 0; t < T; ++t)
#pragma unroll
        for (int tt = 0; tt < 4; ++tt) acc[t][tt] = f32x4{0.f, 0.f, 0.f, 0.f};
#pragma unroll
    for (int u = 0; u < 18; ++u) {
        SB();
        v4u bn[4];
        if (u + 1 < 18) {
#pragma unroll
            for (int tt = 0; tt < 4; ++tt) bn[tt] = lds128(bp + tt * 18432 + (u + 1) * 1024);
        }
#pragma unroll
        for (int t = 0; t < T; ++t) {
            const int n = u * T + t, sl = (n + OFS) % RD;
#pragma unroll
            for (int tt = 0; tt < 4; ++tt) acc[t][tt] = mfma16(ring[sl], bf[tt], acc[t][tt]);
            ring[sl] = bld128(rw, lane * 16 + (n & 3) * 1024, pos + (n >> 2) * 4096);
        }
        side(u);
        if (u + 1 < 18) {
#pragma unroll
            for (int tt = 0; tt < 4; ++tt) { __builtin_amdgcn_sched_group_barrier(0x008, T, 0); __builtin_amdgcn_sched_group_barrier(0x100, 1, 0); }
        }
        SB();
        if (u + 1 < 18) {
#pragma unroll
            for (int tt = 0; tt < 4; ++tt) bf[tt] = bn[tt];
        }
    }
#pragma unroll
    for (int t = 0; t < T; ++t)
#pragma unroll
        for (int tt = 0; tt < 4; ++tt) PIN(acc[t][tt]);
    asm volatile("s_nop 7" ::: "memory");
}

template <bool A> DI void ffn_body(const K4Args& a, LAS char* smem, unsigned lds0, int r, int j, int w, int lane) {
    constexpr int NT = A ? 5 : 4;
    const int g = lane >> 4, i = lane & 15, s = w & 3;
    TSDECL; TS(0);
    f32x4 cs[NT]; v2u bb[NT];
#pragma unroll
    for (int t = 0; t < NT; ++t) { const int f = 576 * j + 16 * (4 * ((A ? 0 : 5) + t) + s) + 4 * g;
        cs[t] = *(const GAS f32x4*)(a.ws + OFF_CS + f * 4); bb[t] = *(const GAS v2u*)((const char*)a.b1 + f * 2); }
    const rsrc_t rh = mkrsrc(a.ws + OFF_H + 4 * r * TT_FR, 4 * (unsigned)TT_FR);
#pragma unroll
    for (int m = 0; m < 9; ++m) dma16(rh, lane * 16, (w + 8 * m) * 1024, lds0 + K4_X + (w + 8 * m) * 1024);
    if (threadIdx.x == 0) *(LAS unsigned*)(smem + K4_CNT) = 0u;
    BAR();
    const rsrc_t rw = mkrsrc(a.ws + OFF_WS + j * WS_J + ffn_stream_base(w), (A ? 180 : 144) * 1024);
    v4u ring[RD];
#pragma unroll
    for (int n = 0; n < RD; ++n) ring[n] = bld128(rw, lane * 16 + (n & 3) * 1024, (n >> 2) * 4096);
    TS(1);
    asm volatile("s_waitcnt vmcnt(%0) lgkmcnt(0)\n\ts_barrier" :: "n"(RD) : "memory");
    TS(2);
    LAS float* st = (LAS float*)(smem + K4_ST);
    if (!A) {
        f32x4 s1 = {0.f, 0.f, 0.f, 0.f}, s2 = s1;
        const v4u ones = {0x3f803f80u, 0x3f803f80u, 0x3f803f80u, 0x3f803f80u};
#pragma unroll
        for (int kh = 0; kh < 2; ++kh) { v4u v[9];
#pragma unroll
            for (int k = 0; k < 9; ++k) v[k] = lds128(smem + K4_X + (s * 18 + 9 * kh + k) * 1024 + lane * 16);
#pragma unroll
            for (int k = 0; k < 9; ++k) { s1 = mfma16(ones, v[k], s1); s2 = mfma16(v[k], v[k], s2); } }
        const float sq = (i & 2) ? ((i & 1) ? s2[3] : s2[2]) : ((i & 1) ? s2[1] : s2[0]);
        const float mean = s1[0] * (1.0f / D), var = __builtin_fmaxf(__builtin_fmaf(-mean, mean, sq * (1.0f / D)), 0.f);
        if (g == (i >> 2)) *(LAS f32x2*)(st + (16 * s + i) * 2) = f32x2{mean, __builtin_amdgcn_rsqf(var + LN_EPS)};
        asm volatile("s_waitcnt lgkmcnt(0)" ::: "memory");
        if (lane == 0) __hip_atomic_fetch_add((LAS unsigned*)(smem + K4_CNT), 1u, __ATOMIC_RELAXED, __HIP_MEMORY_SCOPE_WORKGROUP);
    }
    if (A) __builtin_amdgcn_s_setprio(1);
    TS(3);
    STAMP(a.ws, 9);
    f32x4 acc[5][4];
    auto none = [](int) {};
    ffn_pass<NT, 0>(acc, ring, rw, RD * 1024, smem + K4_X + lane * 16, lane, none);
    TS(4);
    while (*(volatile LAS unsigned*)(smem + K4_CNT) < 4u) { }
    asm volatile("" ::: "memory");
#pragma unroll
    for (int t = 0; t < NT; ++t) { const int ft = 4 * ((A ? 0 : 5) + t) + s;
        const float b0 = blo(bb[t].x), b1 = bhi(bb[t].x), b2 = blo(bb[t].y), b3 = bhi(bb[t].y);
#pragma unroll
        for (int tt = 0; tt < 4; ++tt) { const f32x2 mr = *(const LAS f32x2*)(st + (16 * tt + i) * 2);
            unsigned o01, o23; ln_gelu4(acc[t][tt], cs[t], b0, b1, b2, b3, -mr[0], mr[1], o01, o23);
            *(LAS v2u*)(smem + K4_H + (tt * 18 + (ft >> 1)) * 1024 + lane * 16 + (ft & 1) * 8) = v2u{o01, o23}; } }
    TS(5);
    STAMP(a.ws, 10);
    LBAR();
    TS(7);
    STAMP(a.ws, 11);
    constexpr int G2 = A ? 90 : 72;
    const LAS char* hp = smem + K4_H + lane * 16;
    const rsrc_t rp = mkrsrc(a.ws + OFF_P + (size_t)r * NSL * P_SRC, NSL * (unsigned)P_SRC);
    v4u outq[4];
    auto pack = [&]() {
#pragma unroll
        for (int t = 0; t < 2; ++t)
#pragma unroll
            for (int tp = 0; tp < 2; ++tp) {
                outq[t * 2 + tp] = v4u{pk2(acc[t][2 * tp][0], acc[t][2 * tp][1]), pk2(acc[t][2 * tp][2], acc[t][2 * tp][3]), pk2(acc[t][2 * tp + 1][0], acc[t][2 * tp + 1][1]), pk2(acc[t][2 * tp + 1][2], acc[t][2 * tp + 1][3])};
                PIN(outq[t * 2 + tp]); } };
    auto put = [&](int k, int ps) { const int gi = (A ? 0 : 4) + 2 * ps + (k >> 1), dt = 4 * (gi < j ? gi : gi + 1) + s;
        __builtin_amdgcn_raw_buffer_store_b128(outq[k], rp, lane * 16, j * (int)P_SRC + (dt * 2 + (k & 1)) * 1024, 16); };
    ffn_pass<2, G2 % RD>(acc, ring, rw, (G2 + RD) * 1024, hp, lane, none);
    pack();
    ffn_pass<2, (G2 + 36) % RD>(acc, ring, rw, (G2 + 36 + RD) * 1024, hp, lane, [&](int u) { if (u >= 1 && u <= 7 && (u & 1)) put(u >> 1, 0); });
    pack();
    if (!A) {
#pragma unroll
        for (int k = 0; k < 4; ++k) put(k, 1); }
    TS(8);
    const int dt = A ? 4 * j + s : 32 + (j >> 1);
    v2u hv[4], b2v = {0u, 0u};
    if (A || w == 4) {
        b2v = *(const GAS v2u*)((const char*)a.b2 + (16 * dt + 4 * g) * 2);
#pragma unroll
        for (int tt = 0; tt < 4; ++tt) hv[tt] = *(const GAS v2u*)(a.ws + OFF_H + (4 * r + tt) * TT_FR + (dt >> 1) * 1024 + ((2 * (dt & 1) + (g >> 1)) * 16 + i) * 16 + (g & 1) * 8);
    }
    if (A) ffn_pass<1, (G2 + 72) % RD>(acc, ring, rw, (G2 + 72 + RD) * 1024, hp, lane, [&](int u) { if (u <= 6 && !(u & 1)) put(u >> 1, 1); });
    STAMP(a.ws, 12);
    asm volatile("s_waitcnt vmcnt(0)\n\ts_barrier" ::: "memory");
    TS(9);
    gu32* flag = (gu32*)(a.ws + OFF_FLAG + r * 256);
    if (threadIdx.x == 0) {
        __hip_atomic_fetch_add(flag, 1u, __ATOMIC_RELAXED, __HIP_MEMORY_SCOPE_AGENT);
        for (unsigned spins = 0; __hip_atomic_load(flag, __ATOMIC_RELAXED, __HIP_MEMORY_SCOPE_AGENT) < (unsigned)NSL && spins < (1u << 20); ++spins) __builtin_amdgcn_s_sleep(1);
        __builtin_amdgcn_fence(__ATOMIC_ACQUIRE, "wavefront");
    }
    __syncthreads();
    STAMP(a.ws, 13);
    TS(10);
    if (A || w == 4) {
        v4u pv[2][8];
#pragma unroll
        for (int tp = 0; tp < 2; ++tp)
#pragma unroll
            for (int src = 0; src < 8; ++src) pv[tp][src] = bld128_sc1(rp, lane * 16, (A ? src != j : tp == (j & 1)) ? src * (int)P_SRC + (dt * 2 + tp) * 1024 : 0x7fff0000);
        SB();
#pragma unroll
        for (int tp = 0; tp < 2; ++tp) {
            if (!A && tp != (j & 1)) continue;
#pragma unroll
            for (int u = 0; u < 2; ++u) { const int tt = 2 * tp + u;
                f32x4 o = {blo(hv[tt].x) + blo(b2v.x), bhi(hv[tt].x) + bhi(b2v.x), blo(hv[tt].y) + blo(b2v.y), bhi(hv[tt].y) + bhi(b2v.y)};
                if (A) o += acc[0][tt];
#pragma unroll
                for (int src = 0; src < 8; ++src) o += f32x4{blo(pv[tp][src][2 * u]), bhi(pv[tp][src][2 * u]), blo(pv[tp][src][2 * u + 1]), bhi(pv[tp][src][2 * u + 1])};
                *(GAS v2u*)((char*)a.out + (size_t)(64 * r + 16 * tt + i) * (D * 2) + (16 * dt + 4 * g) * 2) = v2u{pk2(o[0], o[1]), pk2(o[2], o[3])};
            }
        }
    }
    STAMP(a.ws, 14);
    TSW(11);
    TSDUMP(a.ws, 3);
}

__global__ __launch_bounds__(512, 2) void k4_ffn(K4Args a) {
    extern __shared__ __attribute__((aligned(16))) char smem_[];
    LAS char* smem = (LAS char*)smem_;
    const unsigned lds0 = (unsigned)(size_t)smem_;
    const int tid = threadIdx.x, lane = tid & 63, w = __builtin_amdgcn_readfirstlane(tid >> 6), b = blockIdx.x;
    STAMP(a.ws, 8);
    if (w < 4) ffn_body<true>(a, smem, lds0, wg_r(b), wg_j(b), w, lane);
    else ffn_body<false>(a, smem, lds0, wg_r(b), wg_j(b), w, lane);
}

extern "C" void kernel_launch(void* const* d_in, const int* in_sizes, int n_in, void* d_out, int out_size, void* d_ws, size_t ws_size, hipStream_t stream) {
    (void)in_sizes; (void)n_in; (void)out_size; (void)ws_size;
    char* ws = (char*)d_ws;
    (void)hipFuncSetAttribute((const void*)k1_qkv, hipFuncAttributeMaxDynamicSharedMemorySize, K1_LDS);
    (void)hipFuncSetAttribute((const void*)k2_attn, hipFuncAttributeMaxDynamicSharedMemorySize, K2_LDS);
    (void)hipFuncSetAttribute((const void*)k3_oproj, hipFuncAttributeMaxDynamicSharedMemorySize, K3_LDS);
    (void)hipFuncSetAttribute((const void*)k4_ffn, hipFuncAttributeMaxDynamicSharedMemorySize, K4_LDS);
    K1Args a1{d_in[0], d_in[1], ws};
    k1_qkv<<<224, 512, K1_LDS, stream>>>(a1);
    K2Args a2{ws, d_in[2], d_in[4], d_in[6]};
    k2_attn<<<256, 512, K2_LDS, stream>>>(a2);
    k3_oproj<<<256, 512, K3_LDS, stream>>>(ws, d_in[0]);
    K4Args a4{ws, d_in[5], d_in[7], d_out};
    k4_ffn<<<256, 512, K4_LDS, stream>>>(a4);
}
```

```cpp
#include <hip/hip_runtime.h>
#include <hip/hip_bf16.h>
#include <stdint.h>

#define GAS __attribute__((address_space(1)))
#define LAS __attribute__((address_space(3)))
#define DI __device__ __forceinline__
typedef unsigned v4u __attribute__((ext_vector_type(4)));
typedef unsigned v2u __attribute__((ext_vector_type(2)));
typedef float f32x4 __attribute__((ext_vector_type(4)));
typedef float f32x16 __attribute__((ext_vector_type(16)));
typedef short bf16x8 __attribute__((ext_vector_type(8)));
typedef __bf16 bf2 __attribute__((ext_vector_type(2)));
typedef GAS unsigned gu32;
typedef __amdgpu_buffer_rsrc_t rsrc_t;

#define STAMPS 0

constexpr int S = 2048, D = 576, FF = 4608, N3 = 3 * D;
constexpr int NR = 32;
constexpr float LN_EPS = 1e-5f;
constexpr float QSCALE = 0.23570226039551584f * 1.4426950408889634f;

constexpr size_t FRAG = 1024;
constexpr size_t OFF_FLAG = 0;
constexpr int FL_O = 0, FL_H = 32, FL_P = 64, FL_N = 96;
constexpr size_t OFF_STAMP = 32768;
constexpr size_t OFF_QKV = 65536;
constexpr size_t QKV_G = (size_t)S * 144;
constexpr int NSL = 8;
constexpr size_t OFF_WS = OFF_QKV + 24 * QKV_G;
constexpr size_t WS_J = (size_t)1296 * FRAG;
constexpr size_t OFF_WO = OFF_WS + NSL * WS_J;
constexpr size_t OFF_O = OFF_WO + (size_t)36 * 18 * FRAG;
constexpr size_t TT_FR = (size_t)18 * FRAG;
constexpr size_t OFF_H = OFF_O + 128 * TT_FR;
constexpr size_t OFF_P = OFF_H + 128 * TT_FR;
constexpr size_t P_SRC = (size_t)36 * 2 * FRAG;
constexpr size_t OFF_CSP = OFF_P + (size_t)NR * NSL * P_SRC;
constexpr size_t WS_END = OFF_CSP + (size_t)18 * FF * 4;

__host__ __device__ constexpr int ffn_stream_base(int wv) { return (wv < 4 ? wv * 180 : 720 + (wv - 4) * 144) * 1024; }
__host__ __device__ constexpr int ffn_pos1(bool a, int t, int ks) { return t < 3 ? ks * 3 + t : 54 + ks * (a ? 2 : 1) + (t - 3); }
__host__ __device__ constexpr int ffn_pos2(bool a, int t, int ks) { return (a ? 90 : 72) + (t < 4 ? (t >> 1) * 36 + ks * 2 + (t & 1) : 72 + ks); }
DI int wg_r(int b) { return 8 * ((b & 7) >> 1) + ((b >> 3) & 7); }
DI int wg_j(int b) { return 4 * (b & 1) + (b >> 6); }

DI f32x4 mfma16(v4u a, v4u b, f32x4 c) { return __builtin_amdgcn_mfma_f32_16x16x32_bf16(__builtin_bit_cast(bf16x8, a), __builtin_bit_cast(bf16x8, b), c, 0, 0, 0); }
DI f32x16 mfma32(v4u a, v4u b, f32x16 c) { return __builtin_amdgcn_mfma_f32_32x32x16_bf16(__builtin_bit_cast(bf16x8, a), __builtin_bit_cast(bf16x8, b), c, 0, 0, 0); }
DI unsigned pk2(float a, float b) { bf2 v = {(__bf16)a, (__bf16)b}; return __builtin_bit_cast(unsigned, v); }
DI float blo(unsigned u) { return __builtin_bit_cast(float, u << 16); }
DI float bhi(unsigned u) { return __builtin_bit_cast(float, u & 0xffff0000u); }
DI rsrc_t mkrsrc(const void* p, unsigned bytes) { return __builtin_amdgcn_make_buffer_rsrc((void*)p, 0, bytes, 0x00020000); }
DI v4u bld128(rsrc_t r, int voff, int soff) { return __builtin_amdgcn_raw_buffer_load_b128(r, voff, soff, 0); }
DI v4u bld128_sc1(rsrc_t r, int voff, int soff) { return __builtin_amdgcn_raw_buffer_load_b128(r, voff, soff, 16); }
DI v2u bld64(rsrc_t r, int voff, int soff) { return __builtin_amdgcn_raw_buffer_load_b64(r, voff, soff, 0); }
template <int AUX> DI void bst128(v4u v, rsrc_t r, int voff, int soff) { __builtin_amdgcn_raw_buffer_store_b128(v, r, voff, soff, AUX); asm volatile("s_nop 1" :: "v"(v)); }
DI v4u lds128(const LAS char* p) { return *(const LAS v4u*)p; }
DI v2u lds64(const LAS char* p) { return *(const LAS v2u*)p; }
typedef short v4i16_t __attribute__((ext_vector_type(4)));
DI v2u ldstr(const LAS char* p) { return __builtin_bit_cast(v2u, __builtin_amdgcn_ds_read_tr16_b64_v4i16((LAS v4i16_t*)p)); }
DI float dot2(unsigned a, float c) { return __builtin_amdgcn_fdot2_f32_bf16(__builtin_bit_cast(bf2, a), __builtin_bit_cast(bf2, 0x3f803f80u), c, false); }
DI float dot2sq(unsigned a, float c) { const bf2 v = __builtin_bit_cast(bf2, a); return __builtin_amdgcn_fdot2_f32_bf16(v, v, c, false); }
DI void dma16(rsrc_t r, int voff, int soff, unsigned lds) {
    asm volatile("s_mov_b32 m0, %0\n\ts_nop 0\n\tbuffer_load_dwordx4 %1, %2, %3 offen lds" :: "s"(lds), "v"(voff), "s"(r), "s"(soff) : "memory");
}
#define WAITV(n) asm volatile("s_waitcnt vmcnt(" #n ")" ::: "memory")
#define BAR() asm volatile("s_barrier" ::: "memory")
#define LBAR() asm volatile("s_waitcnt lgkmcnt(0)\n\ts_barrier" ::: "memory")
#define SB() __builtin_amdgcn_sched_barrier(0)
#define PIN(x) asm volatile("" : "+v"(x))
DI float sum16(float v) { const unsigned u = __builtin_bit_cast(unsigned, v); const auto r = __builtin_amdgcn_permlane16_swap(u, u, false, false);
    return __builtin_bit_cast(float, (unsigned)r[0]) + __builtin_bit_cast(float, (unsigned)r[1]); }
DI float sum32(float v) { const unsigned u = __builtin_bit_cast(unsigned, v); const auto r = __builtin_amdgcn_permlane32_swap(u, u, false, false);
    return __builtin_bit_cast(float, (unsigned)r[0]) + __builtin_bit_cast(float, (unsigned)r[1]); }
DI float max32(float v) { const unsigned u = __builtin_bit_cast(unsigned, v); const auto r = __builtin_amdgcn_permlane32_swap(u, u, false, false);
    return __builtin_fmaxf(__builtin_bit_cast(float, (unsigned)r[0]), __builtin_bit_cast(float, (unsigned)r[1])); }
DI float lo32(float v) { const unsigned u = __builtin_bit_cast(unsigned, v); const auto r = __builtin_amdgcn_permlane32_swap(u, u, false, false);
    return __builtin_bit_cast(float, (unsigned)r[0]); }
typedef float f32x2 __attribute__((ext_vector_type(2)));
DI void ln_gelu4(f32x4 acc, f32x4 cs, float b0, float b1, float b2, float b3, float nmean, float rs, unsigned& o01, unsigned& o23) {
    float z0, z1, z2, z3, t0, t1, t2, t3;
    asm("v_fma_f32 %0, %16, %12, %8\n\tv_fma_f32 %1, %16, %13, %9\n\tv_fma_f32 %2, %16, %14, %10\n\tv_fma_f32 %3, %16, %15, %11\n\t"
        "v_fma_f32 %0, %17, %0, %18\n\tv_fma_f32 %1, %17, %1, %19\n\tv_fma_f32 %2, %17, %2, %20\n\tv_fma_f32 %3, %17, %3, %21\n\t"
        "v_mul_f32 %4, %0, %0\n\tv_mul_f32 %5, %1, %1\n\tv_mul_f32 %6, %2, %2\n\tv_mul_f32 %7, %3, %3\n\t"
        "v_fmaak_f32 %4, %22, %4, 0xc0135761\n\tv_fmaak_f32 %5, %22, %5, 0xc0135761\n\tv_fmaak_f32 %6, %22, %6, 0xc0135761\n\tv_fmaak_f32 %7, %22, %7, 0xc0135761\n\t"
        "v_mul_f32 %4, %0, %4\n\tv_mul_f32 %5, %1, %5\n\tv_mul_f32 %6, %2, %6\n\tv_mul_f32 %7, %3, %7\n\t"
        "v_exp_f32 %4, %4\n\tv_exp_f32 %5, %5\n\tv_exp_f32 %6, %6\n\tv_exp_f32 %7, %7\n\t"
        "v_add_f32 %4, 1.0, %4\n\tv_add_f32 %5, 1.0, %5\n\tv_add_f32 %6, 1.0, %6\n\tv_add_f32 %7, 1.0, %7\n\t"
        "v_rcp_f32 %4, %4\n\tv_rcp_f32 %5, %5\n\tv_rcp_f32 %6, %6\n\tv_rcp_f32 %7, %7\n\t"
        "v_mul_f32 %0, %0, %4\n\tv_mul_f32 %1, %1, %5\n\tv_mul_f32 %2, %2, %6\n\tv_mul_f32 %3, %3, %7"
        : "=&v"(z0), "=&v"(z1), "=&v"(z2), "=&v"(z3), "=&v"(t0), "=&v"(t1), "=&v"(t2), "=&v"(t3)
        : "v"(acc[0]), "v"(acc[1]), "v"(acc[2]), "v"(acc[3]), "v"(cs[0]), "v"(cs[1]), "v"(cs[2]), "v"(cs[3]), "v"(nmean), "v"(rs), "v"(b0), "v"(b1), "v"(b2), "v"(b3),
          "v"(-0.1029432397f));
    o01 = pk2(z0, z1); o23 = pk2(z2, z3);
}
#define STAMP(ws, i) do { } while (0)
#define FINE 0
#define TS(n) do { } while (0)
#define TSW(n) do { } while (0)
#define TSDECL
#define TSDUMP(ws, kidx) do { } while (0)

struct Unit { rsrc_t rs; int soff, ld, w2; int dst[4]; int cs; };
constexpr int NUNIT = 1296 + 1296 + 162;
struct UnitTab { int v[NUNIT + 1][8]; };
constexpr UnitTab make_units() {
    UnitTab T{};
    for (int u = 0; u <= NUNIT; ++u) {
        const bool iswo = u < 162, isw1 = !iswo && u < 1458, isw2 = u >= 1458 && u < NUNIT;
        const int v = iswo ? u : isw1 ? u - 162 : u - 1458;
        const int j = isw1 ? (v % 72) / 9 : v / 162, ks = isw1 ? v / 72 : (v % 162) % 18, G = isw1 ? v % 9 : (v % 162) / 18;
        T.v[u][0] = isw1 ? 0 : isw2 ? 1 : iswo ? 2 : 3;
        T.v[u][1] = isw1 ? (32 * ks * FF + 576 * j + 64 * G) * 2 : ((isw2 ? 576 * j : 0) + 32 * ks) * (D * 2) + 128 * G;
        const int base = (int)OFF_WS + j * (int)WS_J, gi = G < j ? G : G - 1;
        const bool A = isw1 ? G < 5 : (G == j || gi < 4);
        const int fp = isw1 ? ffn_pos1(A, A ? G : G - 5, ks) : ffn_pos2(A, G == j ? 4 : gi & 3, ks);
        for (int c = 0; c < 4; ++c)
            T.v[u][2 + c] = (isw1 || isw2) ? base + ffn_stream_base(A ? c : 4 + c) + fp * 1024 : iswo ? (int)OFF_WO + ((4 * G + c) * 18 + ks) * 1024 : 0x7fff0000;
        T.v[u][6] = isw1 ? (int)OFF_CSP + (ks * FF + 576 * j + 64 * G) * 4 : 0x7fff0000;
        T.v[u][7] = 0;
    }
    return T;
}
__device__ const UnitTab UNITS = make_units();
DI Unit unit_decode(const void* w1, const void* w2, const void* wo, int u) {
    const int* e = UNITS.v[u < NUNIT ? u : NUNIT];
    const int kind = e[0];
    Unit r;
    r.rs = mkrsrc(kind == 0 ? w1 : kind == 1 ? w2 : wo, kind == 3 ? 0u : kind == 2 ? D * D * 2 : D * FF * 2);
    r.ld = kind == 0 ? FF * 2 : D * 2; r.w2 = kind == 1; r.soff = e[1];
#pragma unroll
    for (int c = 0; c < 4; ++c) r.dst[c] = e[2 + c];
    r.cs = e[6];
    return r;
}
#define UST 16
DI void unit_load(const Unit& t, unsigned scr, int lane) {
    const int voff = (lane >> 3) * t.ld + (lane & 7) * 16;
#pragma unroll
    for (int m = 0; m < 4; ++m) dma16(t.rs, voff, t.soff + 8 * m * t.ld, scr + m * 1024);
}
DI void unit_store(const Unit& t, const LAS char* scr, rsrc_t rws, int lane) {
    const int g = lane >> 4, q = (lane & 15) >> 2, p = lane & 3;
    const int row0 = t.w2 ? 4 * g + q : 8 * g + q, rstep = t.w2 ? 16 : 4;
    float cs[4];
#pragma unroll
    for (int c = 0; c < 4; ++c) {
        const v2u lo = ldstr(scr + row0 * 128 + (16 * c + 4 * p) * 2);
        const v2u hi = ldstr(scr + (row0 + rstep) * 128 + (16 * c + 4 * p) * 2);
        bst128<UST>(v4u{lo.x, lo.y, hi.x, hi.y}, rws, lane * 16, t.dst[c]);
        cs[c] = sum32(sum16(dot2(hi.y, dot2(hi.x, dot2(lo.y, dot2(lo.x, 0.f))))));
    }
    const float mine = g == 0 ? cs[0] : g == 1 ? cs[1] : g == 2 ? cs[2] : cs[3];
    __builtin_amdgcn_raw_buffer_store_b32(__builtin_bit_cast(unsigned, mine), rws, lane * 4, t.cs, 16);
}

#define QST 0
struct K1Args { const void* x; const void* wqkv; const void* wo; const void* w1; const void* w2; char* ws; };
constexpr int K1_STAGE = 32768;
constexpr int K1_TAB = 4 * K1_STAGE;
constexpr int K1_LDS = K1_TAB + 4096;

__global__ __launch_bounds__(512, 2) void k1_qkv(K1Args a) {
    extern __shared__ __attribute__((aligned(16))) char smem_[];
    LAS char* smem = (LAS char*)smem_;
    const unsigned lds0 = (unsigned)(size_t)smem_;
    const int tid = threadIdx.x, lane = tid & 63, w = __builtin_amdgcn_readfirstlane(tid >> 6), b = blockIdx.x;
    STAMP(a.ws, 0);
    TSDECL; TS(0);
    if (b == 0 && tid < FL_N) *(GAS unsigned*)(a.ws + OFF_FLAG + tid * 256) = 0u;
    if (b >= 224) {
        const rsrc_t rws = mkrsrc(a.ws, (unsigned)WS_END);
        const int u0 = ((b - 224) * 8 + w) * 4;
        Unit t[4];
#pragma unroll
        for (int m = 0; m < 4; ++m) { t[m] = unit_decode(a.w1, a.w2, a.wo, u0 + m); unit_load(t[m], lds0 + w * 16384 + m * 4096, lane); }
        WAITV(12); unit_store(t[0], smem + w * 16384, rws, lane);
        WAITV(13); unit_store(t[1], smem + w * 16384 + 4096, rws, lane);
        WAITV(14); unit_store(t[2], smem + w * 16384 + 8192, rws, lane);
        WAITV(15); unit_store(t[3], smem + w * 16384 + 12288, rws, lane);
        STAMP(a.ws, 2);
        return;
    }
    const int xc = b & 7, idx = b >> 3;
    const int rt = 4 * (xc >> 1) + (idx & 3), ct = 7 * (xc & 1) + (idx >> 2);
    if (w >= 4) {
        const int lw = w - 4;
        const rsrc_t rx = mkrsrc(a.x, S * D * 2), rw = mkrsrc(a.wqkv, D * N3 * 2);
        const int vx0 = (128 * rt + 32 * lw + (lane >> 3)) * (D * 2) + ((lane & 7) ^ (lane >> 4)) * 16, vx1 = vx0 ^ 64;
        const int c16 = lane & 15, q = lane >> 4;
        const int vw0 = (16 * lw + q) * (N3 * 2) + 256 * ct + ((((c16 >> 1) ^ (q << 1)) << 1) | (c16 & 1)) * 16;
        const int vw1 = (16 * lw + q) * (N3 * 2) + 256 * ct + ((((c16 >> 1) ^ ((q << 1) | 1)) << 1) | (c16 & 1)) * 16;
        auto stage = [&](int s) {
            const unsigned sb = lds0 + (s & 3) * K1_STAGE;
#pragma unroll
            for (int m = 0; m < 4; ++m) {
                dma16(rw, m < 2 ? vw0 : vw1, (64 * s + 4 * m) * (N3 * 2), sb + 16384 + (4 * lw + m) * 1024);
                dma16(rx, (m & 1) ? vx1 : vx0, 128 * s + m * 8 * (D * 2), sb + (4 * lw + m) * 1024);
            }
        };
        const int g = lane >> 4, i = lane & 15, q4 = i >> 2, p = lane & 3;
        const int sxo = (32 * lw + i) * 128, swo = 16384 + (8 * g + q4) * 256 + 8 * p, key = (q4 << 1) | (g & 1);
        const v4u ones = {0x3f803f80u, 0x3f803f80u, 0x3f803f80u, 0x3f803f80u};
        f32x4 sx[2], sq[2], cs[2];
#pragma unroll
        for (int u = 0; u < 2; ++u) sx[u] = sq[u] = cs[u] = f32x4{0.f, 0.f, 0.f, 0.f};
        auto stats = [&](int s) {
            const LAS char* sb = smem + (s & 3) * K1_STAGE;
#pragma unroll
            for (int k2 = 0; k2 < 2; ++k2)
#pragma unroll
                for (int u = 0; u < 2; ++u) {
                    const v4u xv = lds128(sb + sxo + u * 2048 + ((4 * k2 + g) ^ ((i >> 1) & 7)) * 16);
                    const LAS char* ap = sb + swo + k2 * 8192 + (((2 * lw + u) ^ key) * 32);
                    const v2u lo = ldstr(ap), hi = ldstr(ap + 1024);
                    sx[u] = mfma16(ones, xv, sx[u]); sq[u] = mfma16(xv, xv, sq[u]); cs[u] = mfma16(v4u{lo.x, lo.y, hi.x, hi.y}, ones, cs[u]);
                }
        };
        stage(0); stage(1); stage(2);
        WAITV(16); TS(1); BAR();
        WAITV(8); BAR();
        stats(0);
#pragma unroll
        for (int s = 3; s < 9; ++s) { stage(s); stats(s - 2); WAITV(8); asm volatile("s_waitcnt lgkmcnt(0)" ::: "memory"); BAR(); }
        stats(7); WAITV(0); BAR();
        stats(8);
        LAS float* tabx = (LAS float*)(smem + K1_TAB);
        LAS float* tabc = tabx + 256;
#pragma unroll
        for (int u = 0; u < 2; ++u) {
            const float s2 = (i & 2) ? ((i & 1) ? sq[u][3] : sq[u][2]) : ((i & 1) ? sq[u][1] : sq[u][0]);
            const float mean = sx[u][0] * (1.0f / D), var = __builtin_fmaxf(__builtin_fmaf(-mean, mean, s2 * (1.0f / D)), 0.f);
            if (g == q4) *(LAS f32x2*)(tabx + (32 * lw + 16 * u + i) * 2) = f32x2{mean, __builtin_amdgcn_rsqf(var + LN_EPS)};
            if (i == 0) *(LAS f32x4*)(tabc + 32 * lw + 16 * u + 4 * g) = cs[u]; }
        LBAR();
        TS(9);
        TSDUMP(a.ws, 0);
        return;
    }
    const int th = w & 1, ch = w >> 1, h = lane >> 5, l31 = lane & 31;
    const int q = (lane & 15) >> 2, p = lane & 3, blk = (lane >> 4) & 1;
    const int xrow = (64 * th + l31) * 128, xkey = (l31 >> 1) & 7;
    const int woff = 16384 + (8 * h + q) * 256 + (((4 * ch + blk) ^ ((q << 1) | h)) * 32) + 8 * p;
    f32x16 acc[2][2];
#pragma unroll
    for (int cx = 0; cx < 2; ++cx)
#pragma unroll
        for (int ty = 0; ty < 2; ++ty)
#pragma unroll
            for (int e = 0; e < 16; ++e) acc[cx][ty][e] = 0.f;
    int xo[4];
#pragma unroll
    for (int k4 = 0; k4 < 4; ++k4) xo[k4] = xrow + ((2 * k4 + h) ^ xkey) * 16;
    v4u bA[2][2], aA[2][2], bB[2][2], aB[2][2];
    auto rd = [&](v4u (&bf)[2][2], v4u (&af)[2][2], const LAS char* sb, int k2) {
#pragma unroll
        for (int u = 0; u < 2; ++u) {
#pragma unroll
            for (int cx = 0; cx < 2; ++cx) { const LAS char* ap = sb + ((woff ^ (cx << 6)) + (2 * k2 + u) * 4096);
                const v2u lo = ldstr(ap), hi = ldstr(ap + 1024); af[u][cx] = v4u{lo.x, lo.y, hi.x, hi.y}; }
#pragma unroll
            for (int ty = 0; ty < 2; ++ty) bf[u][ty] = lds128(sb + xo[2 * k2 + u] + ty * 4096);
        }
    };
    auto mm = [&](const v4u (&bf)[2][2], const v4u (&af)[2][2]) {
#pragma unroll
        for (int u = 0; u < 2; ++u)
#pragma unroll
            for (int cx = 0; cx < 2; ++cx)
#pragma unroll
                for (int ty = 0; ty < 2; ++ty) acc[cx][ty] = mfma32(af[u][cx], bf[u][ty], acc[cx][ty]);
    };
    int dst[2][2];
#pragma unroll
    for (int cx = 0; cx < 2; ++cx)
#pragma unroll
        for (int m = 0; m < 2; ++m) { const int n8 = 128 * ct + 64 * ch + 32 * cx + 16 * m + 8 * h, ja = n8 / 72;
            dst[cx][m] = n8 < N3 ? (int)OFF_QKV + ja * (int)QKV_G + (128 * rt + 64 * th + l31) * 144 + (n8 - 72 * ja) * 2 : 0x7fff0000; }
    auto half = [&](v4u (&bn)[2][2], v4u (&an)[2][2], const LAS char* nb, int k2, const v4u (&bc)[2][2], const v4u (&ac)[2][2]) {
        SB();
        rd(bn, an, nb, k2);
        mm(bc, ac);
#pragma unroll
        for (int e = 0; e < 4; ++e) { __builtin_amdgcn_sched_group_barrier(0x008, 1, 0); __builtin_amdgcn_sched_group_barrier(0x100, 2, 0); }
#pragma unroll
        for (int e = 0; e < 4; ++e) { __builtin_amdgcn_sched_group_barrier(0x008, 1, 0); __builtin_amdgcn_sched_group_barrier(0x100, 1, 0); }
        SB();
    };
#define K1_SYNC() asm volatile("s_waitcnt lgkmcnt(12)\n\ts_barrier" ::: "memory")
    BAR();
    TS(1);
    unsigned pf[2];
#pragma unroll
    for (int m = 0; m < 2; ++m) {
        const int lx = (idx >> 2) * 55 + tid, lw2 = (idx & 3) * 96 + (tid - 55), wo = (192 + 192 * m + (lw2 >> 1)) * (N3 * 2) + 256 * ct + (lw2 & 1) * 128;
        const char* p = tid < 55 ? (const char*)a.x + (size_t)(128 * rt + (lx < 384 ? lx / 3 : 0)) * (D * 2) + (3 + 3 * m + lx % 3) * 128
                                 : (const char*)a.wqkv + ((tid < 151 && wo < D * N3 * 2) ? wo : 0);
        asm volatile("global_load_dword %0, %1, off" : "=v"(pf[m]) : "v"(p));
    }
    __builtin_amdgcn_s_waitcnt(0xC07F);
    rd(bA, aA, smem, 0);
    half(bB, aB, smem, 1, bA, aA);
    BAR();
    half(bA, aA, smem + K1_STAGE, 0, bB, aB);
    K1_SYNC();
#pragma nounroll
    for (int s = 1; s < 7; ++s) {
        half(bB, aB, smem + (s & 3) * K1_STAGE, 1, bA, aA);
        half(bA, aA, smem + ((s + 1) & 3) * K1_STAGE, 0, bB, aB);
        K1_SYNC();
    }
    half(bB, aB, smem + 3 * K1_STAGE, 1, bA, aA);
    half(bA, aA, smem, 0, bB, aB);
    half(bB, aB, smem, 1, bA, aA);
    SB(); mm(bB, aB); SB();
    TS(4);
    asm volatile("s_waitcnt vmcnt(0)" :: "v"(pf[0]), "v"(pf[1]));
    LBAR();
    const LAS float* tabc = (const LAS float*)(smem + K1_TAB) + 256;
    const LAS float* tabx = (const LAS float*)(smem + K1_TAB);
    TS(5);
    const rsrc_t rws = mkrsrc(a.ws, (unsigned)OFF_WS);
#pragma unroll
    for (int ty = 0; ty < 2; ++ty) {
        const int t = 64 * th + 32 * ty + l31;
        const float mean = tabx[t * 2], rstd = tabx[t * 2 + 1];
#pragma unroll
        for (int cx = 0; cx < 2; ++cx) {
            const float rs = rstd * (128 * ct + 64 * ch + 32 * cx < D ? QSCALE : 1.0f);
            unsigned pk[8];
#pragma unroll
            for (int qd = 0; qd < 4; ++qd) { const f32x4 c = *(const LAS f32x4*)(tabc + 64 * ch + 32 * cx + 8 * qd + 4 * h);
                pk[2 * qd] = pk2(rs * __builtin_fmaf(-mean, c[0], acc[cx][ty][4 * qd]), rs * __builtin_fmaf(-mean, c[1], acc[cx][ty][4 * qd + 1]));
                pk[2 * qd + 1] = pk2(rs * __builtin_fmaf(-mean, c[2], acc[cx][ty][4 * qd + 2]), rs * __builtin_fmaf(-mean, c[3], acc[cx][ty][4 * qd + 3])); }
#pragma unroll
            for (int m = 0; m < 2; ++m) {
                const auto s0 = __builtin_amdgcn_permlane32_swap(pk[4 * m], pk[4 * m + 2], false, false), s1 = __builtin_amdgcn_permlane32_swap(pk[4 * m + 1], pk[4 * m + 3], false, false);
                bst128<QST>(v4u{(unsigned)s0[0], (unsigned)s1[0], (unsigned)s0[1], (unsigned)s1[1]}, rws, dst[cx][m], ty * 32 * 144); }
        }
    }
    STAMP(a.ws, 2);
    TS(6); TSW(7);
    TSDUMP(a.ws, 0);
}

struct K2Args { char* ws; const void* x; const void* wo; const void* w1; const void* w2; const void* b1; const void* b2; void* out; };
constexpr int KV_HEAD = 320 * 40;
constexpr int K2_K = 0, K2_V = 4 * KV_HEAD, K2_O = 8 * KV_HEAD, K2_SCR = K2_O + 64 * 144;
constexpr int K4_X = 0, K4_H = 73728, K4_ST = 147456, K4_CS = K4_ST + 512, K4_CNT = K4_CS + 2304, K2_LDS = K4_CNT + 64;
static_assert(K2_SCR + 8 * 4096 <= K4_ST, "LDS map");

DI void flag_add(char* ws, int f) { __hip_atomic_fetch_add((gu32*)(ws + OFF_FLAG + f * 256), 1u, __ATOMIC_RELAXED, __HIP_MEMORY_SCOPE_AGENT); }
DI void flag_wait(char* ws, int f, unsigned n) {
    gu32* p = (gu32*)(ws + OFF_FLAG + f * 256);
    for (unsigned spins = 0; __hip_atomic_load(p, __ATOMIC_RELAXED, __HIP_MEMORY_SCOPE_AGENT) < n && spins < (1u << 20); ++spins) __builtin_amdgcn_s_sleep(1);
    __builtin_amdgcn_fence(__ATOMIC_ACQUIRE, "wavefront");
}
DI void flag_wait_all(char* ws, int f, unsigned n, int lane) {
    gu32* p = (gu32*)(ws + OFF_FLAG + (f + (lane & 31)) * 256);
    for (unsigned spins = 0; !__all(__hip_atomic_load(p, __ATOMIC_RELAXED, __HIP_MEMORY_SCOPE_AGENT) >= n) && spins < (1u << 20); ++spins) __builtin_amdgcn_s_sleep(1);
    __builtin_amdgcn_fence(__ATOMIC_ACQUIRE, "wavefront");
}
DI void tile_in(const char* src, LAS char* smem, int tid) {
    const rsrc_t rs = mkrsrc(src, 4 * (unsigned)TT_FR);
    v4u t[9];
#pragma unroll
    for (int m = 0; m < 9; ++m) t[m] = bld128_sc1(rs, tid * 16, m * 8192);
#pragma unroll
    for (int m = 0; m < 9; ++m) *(LAS v4u*)(smem + tid * 16 + m * 8192) = t[m];
}

template <bool EDGE> DI void attn_core(LAS char* smem, const v4u q0, const v4u q1, int r, int w, int lane, f32x16& oacc, const Unit& ua, rsrc_t rws) {
    const int hl = w & 3, qh = w >> 2, l31 = lane & 31, hh = lane >> 5;
    const int tmin = EDGE ? ((256 - 64 * r - 32 * qh) >> 5) : 0;
    const float ninf = -__builtin_inff();
    const LAS char* kp = smem + K2_K + (hl * 320 + 32 * qh + l31) * 40;
    v4u ka[9], kb[9];
#pragma unroll
    for (int t = 0; t < 9; ++t) { const v2u a0 = lds64(kp + t * 1280 + 16 * hh), a1 = lds64(kp + t * 1280 + 16 * hh + 8);
        ka[t] = v4u{a0.x, a0.y, a1.x, a1.y};
        const v2u c0 = lds64(kp + t * 1280 + 32), c1 = lds64(kp + t * 1280 + 24);
        kb[t] = v4u{c0.x, c0.y, c1.x, c1.y}; }
    auto score = [&](int t, const f32x16& c) { return mfma32(kb[t], q1, mfma32(ka[t], q0, c)); };
    bool le[16];
#pragma unroll
    for (int rg = 0; rg < 16; ++rg) le[rg] = (rg & 3) + 8 * (rg >> 2) + 4 * hh <= l31;
    auto merged = [&](const f32x16& c) { const f32x16 s8 = score(8, c); f32x16 s0;
        if (EDGE) { s0 = s8;
#pragma unroll
            for (int rg = 0; rg < 16; ++rg) s0[rg] = ninf; } else s0 = score(0, c);
        f32x16 m;
#pragma unroll
        for (int rg = 0; rg < 16; ++rg) m[rg] = le[rg] ? s8[rg] : s0[rg];
        return m; };
    const f32x16 zero = {};
    float mx;
    { const f32x16 m = merged(zero); mx = __builtin_fmaxf(m[0], m[1]);
#pragma unroll
      for (int rg = 2; rg < 16; rg += 2) mx = __builtin_fmaxf(__builtin_fmaxf(mx, m[rg]), m[rg + 1]); }
#pragma unroll
    for (int t = 1; t < 8; ++t) if (!EDGE || t >= tmin) { const f32x16 s = score(t, zero);
#pragma unroll
        for (int rg = 0; rg < 16; rg += 2) mx = __builtin_fmaxf(__builtin_fmaxf(mx, s[rg]), s[rg + 1]); }
    mx = max32(mx);
    WAITV(0); unit_store(ua, smem + K2_SCR + w * 4096, rws, lane);
    f32x16 cm;
#pragma unroll
    for (int rg = 0; rg < 16; ++rg) cm[rg] = -mx;
    const LAS char* vp = smem + K2_V + (hl * 320 + 32 * qh + 4 * hh + ((lane & 15) >> 2)) * 40 + (((lane >> 4) & 1) ? 32 : 8 * (lane & 3));
    auto pv = [&](int t, const unsigned (&pw)[8]) {
#pragma unroll
        for (int s = 0; s < 2; ++s) {
            const v2u lo = ldstr(vp + (t * 32 + 16 * s) * 40), hi = ldstr(vp + (t * 32 + 16 * s + 8) * 40);
            oacc = mfma32(v4u{lo.x, lo.y, hi.x, hi.y}, v4u{pw[4 * s], pw[4 * s + 1], pw[4 * s + 2], pw[4 * s + 3]}, oacc);
        } };
    oacc = zero;
    { const f32x16 m = merged(cm); unsigned pw[8], p8[8], p0[8];
#pragma unroll
      for (int e = 0; e < 8; ++e) { pw[e] = pk2(__builtin_amdgcn_exp2f(m[2 * e]), __builtin_amdgcn_exp2f(m[2 * e + 1]));
          const unsigned mk = (le[2 * e] ? 0xffffu : 0u) | (le[2 * e + 1] ? 0xffff0000u : 0u); p8[e] = pw[e] & mk; p0[e] = pw[e] & ~mk; }
      pv(8, p8); if (!EDGE) pv(0, p0); }
#pragma unroll
    for (int t = 1; t < 8; ++t) if (!EDGE || t >= tmin) { const f32x16 s = score(t, cm); unsigned pw[8];
#pragma unroll
        for (int e = 0; e < 8; ++e) pw[e] = pk2(__builtin_amdgcn_exp2f(s[2 * e]), __builtin_amdgcn_exp2f(s[2 * e + 1]));
        pv(t, pw); }
}

#define RD 24

template <int T, int OFS, class Side>
DI void ffn_pass(f32x4 (&acc)[3][4], v4u (&ring)[RD], rsrc_t rw, int pos, const LAS char* bp, int lane, Side side) {
    v4u bf[4];
#pragma unroll
    for (int tt = 0; tt < 4; ++tt) bf[tt] = lds128(bp + tt * 18432);
#pragma unroll
    for (int t = 0; t < T; ++t)
#pragma unroll
        for (int tt = 0; tt < 4; ++tt) acc[t][tt] = f32x4{0.f, 0.f, 0.f, 0.f};
#pragma unroll
    for (int u = 0; u < 18; ++u) {
        SB();
        v4u bn[4];
        if (u + 1 < 18) {
#pragma unroll
            for (int tt = 0; tt < 4; ++tt) bn[tt] = lds128(bp + tt * 18432 + (u + 1) * 1024);
        }
#pragma unroll
        for (int t = 0; t < T; ++t) {
            const int n = u * T + t, sl = (n + OFS) % RD;
#pragma unroll
            for (int tt = 0; tt < 4; ++tt) acc[t][tt] = mfma16(ring[sl], bf[tt], acc[t][tt]);
            ring[sl] = bld128(rw, lane * 16 + (n & 3) * 1024, pos + (n >> 2) * 4096);
        }
        side(u);
#pragma unroll
        for (int t = 0; t < T; ++t) {
            if (T == 1) {
#pragma unroll
                for (int e = 0; e < 4; ++e) { __builtin_amdgcn_sched_group_barrier(0x008, 1, 0); __builtin_amdgcn_sched_group_barrier(0x100, 1, 0); }
            } else if (T == 2) {
#pragma unroll
                for (int e = 0; e < 2; ++e) { __builtin_amdgcn_sched_group_barrier(0x008, 2, 0); __builtin_amdgcn_sched_group_barrier(0x100, 1, 0); }
            } else { __builtin_amdgcn_sched_group_barrier(0x008, 4, 0); if (t < 4) __builtin_amdgcn_sched_group_barrier(0x100, 1, 0); }
            __builtin_amdgcn_sched_group_barrier(0x020, 1, 0);
        }
        SB();
        if (u + 1 < 18) {
#pragma unroll
            for (int tt = 0; tt < 4; ++tt) bf[tt] = bn[tt];
        }
    }
#pragma unroll
    for (int t = 0; t < T; ++t)
#pragma unroll
        for (int tt = 0; tt < 4; ++tt) PIN(acc[t][tt]);
    asm volatile("s_nop 7" ::: "memory");
}

template <bool A> DI void ffn_body(const K2Args& a, LAS char* smem, int r, int j, int w, int lane
) {
    constexpr int NT = A ? 5 : 4;
    const int tid = threadIdx.x, g = lane >> 4, i = lane & 15, s = w & 3;
    char* ws = a.ws;
    const rsrc_t rws = mkrsrc(ws, (unsigned)WS_END);
    v4u ring[RD];
    {
        if (tid == 448) { flag_add(ws, FL_O + r); flag_wait(ws, FL_O + r, 8); }
        const int dt = A ? 4 * j + w : 32 + (j >> 1), tt0 = A ? 0 : 2 * (j & 1);
        const rsrc_t rw = mkrsrc(ws + OFF_WO, 36 * 18 * 1024), rx = mkrsrc(a.x, S * D * 2);
        v4u af[18]; v2u xr[4];
        if (A || w == 4) {
#pragma unroll
            for (int ks = 0; ks < 18; ++ks) af[ks] = bld128(rw, lane * 16 + (ks & 3) * 1024, dt * 18432 + (ks >> 2) * 4096);
#pragma unroll
            for (int u = 0; u < 4; ++u) xr[u] = bld64(rx, (64 * r + 16 * ((tt0 + u) & 3) + i) * (D * 2) + (16 * dt + 4 * g) * 2, 0);
        }
        BAR();
        TS(7);
        STAMP(ws, 6);
        tile_in(ws + OFF_O + 4 * r * TT_FR, smem, tid);
        SB();
        { const rsrc_t rf = mkrsrc(ws + OFF_WS + j * WS_J + ffn_stream_base(w), (A ? 180 : 144) * 1024);
#pragma unroll
          for (int n = 0; n < RD; ++n) ring[n] = bld128_sc1(rf, lane * 16 + (n & 3) * 1024, (n >> 2) * 4096); }
        SB();
        LBAR();
        TS(8);
        if (A || w == 4) {
            f32x4 acc[4];
#pragma unroll
            for (int u = 0; u < 4; ++u) acc[u] = f32x4{0.f, 0.f, 0.f, 0.f};
            const LAS char* bp = smem + lane * 16;
            int to[4];
#pragma unroll
            for (int u = 0; u < 4; ++u) to[u] = ((tt0 + u) & 3) * 18432;
            v4u bf[2][4];
#pragma unroll
            for (int u = 0; u < 4; ++u) bf[0][u] = lds128(bp + to[u]);
#pragma unroll
            for (int ks = 0; ks < 18; ++ks) {
                SB();
                if (ks < 17) {
#pragma unroll
                    for (int u = 0; u < 4; ++u) bf[(ks + 1) & 1][u] = lds128(bp + (ks + 1) * 1024 + to[u]); }
#pragma unroll
                for (int u = 0; u < 4; ++u) acc[u] = mfma16(af[ks], bf[ks & 1][u], acc[u]);
#pragma unroll
                for (int u = 0; u < 4; ++u) { __builtin_amdgcn_sched_group_barrier(0x008, 1, 0); __builtin_amdgcn_sched_group_barrier(0x100, 1, 0); }
                SB();
            }
#pragma unroll
            for (int u = 0; u < 4; ++u) if (A || u < 2) { const int tt = (tt0 + u) & 3;
                const v2u o = {pk2(acc[u][0] + blo(xr[u].x), acc[u][1] + bhi(xr[u].x)), pk2(acc[u][2] + blo(xr[u].y), acc[u][3] + bhi(xr[u].y))};
                __builtin_amdgcn_raw_buffer_store_b64(o, rws, (int)OFF_H + (4 * r + tt) * (int)TT_FR + (dt >> 1) * 1024 + ((2 * (dt & 1) + (g >> 1)) * 16 + i) * 16 + (g & 1) * 8, 0, 16); }
        }
        TS(9);
        asm volatile("s_waitcnt vmcnt(0) lgkmcnt(0)\n\ts_barrier" ::: "memory");
        STAMP(ws, 7);
        TS(10);
    }
    const rsrc_t rw = mkrsrc(ws + OFF_WS + j * WS_J + ffn_stream_base(w), (A ? 180 : 144) * 1024);
    if (w == 7) { if (lane == 0) flag_add(ws, FL_H + r); flag_wait_all(ws, FL_O, 8, lane); }
    BAR();
    TS(12);
    if (tid == 448) flag_wait(ws, FL_H + r, 8);
    v2u bb[NT];
#pragma unroll
    for (int t = 0; t < NT; ++t) bb[t] = *(const GAS v2u*)((const char*)a.b1 + (576 * j + 16 * (4 * ((A ? 0 : 5) + t) + s) + 4 * g) * 2);
    unsigned pc[18], pc2[18];
    const rsrc_t rc = mkrsrc(ws + OFF_CSP, 18 * FF * 4);
#pragma unroll
    for (int ks = 0; ks < 18; ++ks) pc[ks] = __builtin_amdgcn_raw_buffer_load_b32(rc, (576 * j + tid) * 4, ks * FF * 4, 16);
    if (w == 0) {
#pragma unroll
        for (int ks = 0; ks < 18; ++ks) pc2[ks] = __builtin_amdgcn_raw_buffer_load_b32(rc, (576 * j + 512 + tid) * 4, ks * FF * 4, 16); }
    TS(13);
    BAR();
    TS(14);
    STAMP(ws, 8);
    tile_in(ws + OFF_H + 4 * r * TT_FR, smem + K4_X, tid);
    LBAR();
    TS(15);
    LAS float* st = (LAS float*)(smem + K4_ST);
    if (!A) {
        f32x4 s1 = {0.f, 0.f, 0.f, 0.f}, s2 = s1;
        const v4u ones = {0x3f803f80u, 0x3f803f80u, 0x3f803f80u, 0x3f803f80u};
#pragma unroll
        for (int kh = 0; kh < 2; ++kh) { v4u v[9];
#pragma unroll
            for (int k = 0; k < 9; ++k) v[k] = lds128(smem + K4_X + (s * 18 + 9 * kh + k) * 1024 + lane * 16);
#pragma unroll
            for (int k = 0; k < 9; ++k) { s1 = mfma16(ones, v[k], s1); s2 = mfma16(v[k], v[k], s2); } }
        const float sq = (i & 2) ? ((i & 1) ? s2[3] : s2[2]) : ((i & 1) ? s2[1] : s2[0]);
        const float mean = s1[0] * (1.0f / D), var = __builtin_fmaxf(__builtin_fmaf(-mean, mean, sq * (1.0f / D)), 0.f);
        if (g == (i >> 2)) *(LAS f32x2*)(st + (16 * s + i) * 2) = f32x2{mean, __builtin_amdgcn_rsqf(var + LN_EPS)};
    }
    { float sum = 0.f;
#pragma unroll
      for (int ks = 0; ks < 18; ++ks) sum += __builtin_bit_cast(float, pc[ks]);
      *(LAS float*)(smem + K4_CS + tid * 4) = sum;
      if (w == 0) { sum = 0.f;
#pragma unroll
          for (int ks = 0; ks < 18; ++ks) sum += __builtin_bit_cast(float, pc2[ks]);
          *(LAS float*)(smem + K4_CS + (512 + tid) * 4) = sum; }
      asm volatile("s_waitcnt lgkmcnt(0)" ::: "memory");
      if (lane == 0) __hip_atomic_fetch_add((LAS unsigned*)(smem + K4_CNT), 1u, __ATOMIC_RELAXED, __HIP_MEMORY_SCOPE_WORKGROUP); }
    if (A) __builtin_amdgcn_s_setprio(1);
    STAMP(ws, 9);
    f32x4 acc[3][4];
    auto none = [](int) {};
    auto act = [&](int slot, int t) { const int ft = 4 * ((A ? 0 : 5) + t) + s;
        const float b0 = blo(bb[t].x), b1 = bhi(bb[t].x), b2 = blo(bb[t].y), b3 = bhi(bb[t].y);
        const f32x4 cs = *(const LAS f32x4*)(smem + K4_CS + (16 * ft + 4 * g) * 4);
#pragma unroll
        for (int tt = 0; tt < 4; ++tt) { const f32x2 mr = *(const LAS f32x2*)(st + (16 * tt + i) * 2);
            unsigned o01, o23; ln_gelu4(acc[slot][tt], cs, b0, b1, b2, b3, -mr[0], mr[1], o01, o23);
            *(LAS v2u*)(smem + K4_H + (tt * 18 + (ft >> 1)) * 1024 + lane * 16 + (ft & 1) * 8) = v2u{o01, o23}; } };
    ffn_pass<3, 0>(acc, ring, rw, RD * 1024, smem + K4_X + lane * 16, lane, none);
    TS(16);
    while (*(volatile LAS unsigned*)(smem + K4_CNT) < 8u) { }
    asm volatile("" ::: "memory");
    act(0, 0); act(1, 1); act(2, 2);
    TS(11);
    ffn_pass<NT - 3, 54 % RD>(acc, ring, rw, (54 + RD) * 1024, smem + K4_X + lane * 16, lane, none);
    TS(23);
    act(0, 3); if (A) act(1, 4);
    TS(17);
    STAMP(ws, 10);
    LBAR();
    TS(18);
    STAMP(ws, 11);
    constexpr int G2 = A ? 90 : 72;
    const LAS char* hp = smem + K4_H + lane * 16;
    const rsrc_t rp = mkrsrc(ws + OFF_P + (size_t)r * NSL * P_SRC, NSL * (unsigned)P_SRC);
    v4u outq[4];
    auto pack = [&]() {
#pragma unroll
        for (int t = 0; t < 2; ++t)
#pragma unroll
            for (int tp = 0; tp < 2; ++tp) {
                outq[t * 2 + tp] = v4u{pk2(acc[t][2 * tp][0], acc[t][2 * tp][1]), pk2(acc[t][2 * tp][2], acc[t][2 * tp][3]), pk2(acc[t][2 * tp + 1][0], acc[t][2 * tp + 1][1]), pk2(acc[t][2 * tp + 1][2], acc[t][2 * tp + 1][3])};
                PIN(outq[t * 2 + tp]); } };
    auto put = [&](int k, int ps) { const int gi = (A ? 0 : 4) + 2 * ps + (k >> 1), dt = 4 * (gi < j ? gi : gi + 1) + s;
        bst128<16>(outq[k], rp, lane * 16, j * (int)P_SRC + (dt * 2 + (k & 1)) * 1024); };
    ffn_pass<2, G2 % RD>(acc, ring, rw, (G2 + RD) * 1024, hp, lane, none);
    pack();
    ffn_pass<2, (G2 + 36) % RD>(acc, ring, rw, (G2 + 36 + RD) * 1024, hp, lane, [&](int u) { if (u >= 1 && u <= 7 && (u & 1)) put(u >> 1, 0); });
    pack();
    if (!A) {
#pragma unroll
        for (int k = 0; k < 4; ++k) put(k, 1); }
    TS(19);
    if (A) ffn_pass<1, (G2 + 72) % RD>(acc, ring, rw, (G2 + 72 + RD) * 1024, hp, lane, [&](int u) { if (u <= 6 && !(u & 1)) put(u >> 1, 1); });
    STAMP(ws, 12);
    asm volatile("s_waitcnt vmcnt(0)\n\ts_barrier" ::: "memory");
    TS(20);
    if (tid == 448) { flag_add(ws, FL_P + r); flag_wait(ws, FL_P + r, 8); }
    BAR();
    STAMP(ws, 13);
    TS(21);
    if (A || w == 4) {
        const int dt = A ? 4 * j + s : 32 + (j >> 1);
        v4u pv[2][8];
#pragma unroll
        for (int tp = 0; tp < 2; ++tp)
#pragma unroll
            for (int src = 0; src < 8; ++src) pv[tp][src] = bld128_sc1(rp, lane * 16, (A ? src != j : tp == (j & 1)) ? src * (int)P_SRC + (dt * 2 + tp) * 1024 : 0x7fff0000);
        const v2u b2v = *(const GAS v2u*)((const char*)a.b2 + (16 * dt + 4 * g) * 2);
        SB();
#pragma unroll
        for (int tp = 0; tp < 2; ++tp) {
            if (!A && tp != (j & 1)) continue;
#pragma unroll
            for (int u = 0; u < 2; ++u) { const int tt = 2 * tp + u;
                const v2u hv = lds64(smem + K4_X + (tt * 18 + (dt >> 1)) * 1024 + ((2 * (dt & 1) + (g >> 1)) * 16 + i) * 16 + (g & 1) * 8);
                f32x4 o = {blo(hv.x) + blo(b2v.x), bhi(hv.x) + bhi(b2v.x), blo(hv.y) + blo(b2v.y), bhi(hv.y) + bhi(b2v.y)};
                if (A) o += acc[0][tt];
#pragma unroll
                for (int src = 0; src < 8; ++src) o += f32x4{blo(pv[tp][src][2 * u]), bhi(pv[tp][src][2 * u]), blo(pv[tp][src][2 * u + 1]), bhi(pv[tp][src][2 * u + 1])};
                *(GAS v2u*)((char*)a.out + (size_t)(64 * r + 16 * tt + i) * (D * 2) + (16 * dt + 4 * g) * 2) = v2u{pk2(o[0], o[1]), pk2(o[2], o[3])};
            }
        }
    }
    STAMP(ws, 14);
    TSW(22);
    TSDUMP(ws, 1);
}

__global__ __launch_bounds__(512, 2) void k2_rest(K2Args a) {
    extern __shared__ __attribute__((aligned(16))) char smem_[];
    LAS char* smem = (LAS char*)smem_;
    const unsigned lds0 = (unsigned)(size_t)smem_;
    char* ws = a.ws;
    const int tid = threadIdx.x, lane = tid & 63, w = __builtin_amdgcn_readfirstlane(tid >> 6), b = blockIdx.x;
    const int j = wg_j(b), r = wg_r(b);
    STAMP(ws, 3);
    TSDECL; TS(0);
    const rsrc_t rws = mkrsrc(ws, (unsigned)WS_END);
    {
        const int hl = w & 3, qh = w >> 2, l31 = lane & 31, hh = lane >> 5;
        const rsrc_t rk = mkrsrc(ws + OFF_QKV + (8 + j) * QKV_G, S * 144), rv = mkrsrc(ws + OFF_QKV + (16 + j) * QKV_G, S * 144);
        const int tok0 = tid / 9, mm = tid - 9 * tok0;
        v4u kk[6], vv[6];
#pragma unroll
        for (int m = 0; m < 6; ++m) { const int tok = tok0 + 56 * m, o_ = (64 * r - 256) * 144 + tid * 16 + m * 56 * 144;
            const int off = (tid < 504 && tok < 320 && o_ >= 0) ? o_ : 0x7ffffff0;
            kk[m] = bld128(rk, off, 0); vv[m] = bld128(rv, off, 0); }
        const char* qp = ws + OFF_QKV + j * QKV_G + (size_t)(64 * r + 32 * qh + l31) * 144 + 36 * hl;
        v4u q0, q1;
        { const GAS unsigned* qq = (const GAS unsigned*)(qp + 16 * hh); q0 = v4u{qq[0], qq[1], qq[2], qq[3]};
          const unsigned t = *(const GAS unsigned*)(qp + 32); q1 = v4u{hh == 0 ? t : 0u, 0u, 0u, 0u}; }
        const Unit ua = unit_decode(a.w1, a.w2, a.wo, 1024 + b * 8 + w);
        for (int n = tid; n < 1280; n += 512) { *(LAS unsigned*)(smem + K2_K + n * 40 + 36) = 0u; *(LAS unsigned*)(smem + K2_V + n * 40 + 36) = 0x3f80u; }
        if (tid == 0) *(LAS unsigned*)(smem + K4_CNT) = 0u;
        int lo_[4];
#pragma unroll
        for (int e = 0; e < 4; ++e) { const int dw = 4 * mm + e, h = dw / 9; lo_[e] = (h * 320 + tok0) * 40 + (dw - 9 * h) * 4; }
        TS(1);
        if (tid < 504) {
#pragma unroll
            for (int m = 0; m < 6; ++m) if (m < 5 || tok0 + 280 < 320) {
#pragma unroll
                for (int e = 0; e < 4; ++e) { *(LAS unsigned*)(smem + K2_K + lo_[e] + m * 56 * 40) = kk[m][e]; *(LAS unsigned*)(smem + K2_V + lo_[e] + m * 56 * 40) = vv[m][e]; } }
        }
        unit_load(ua, lds0 + K2_SCR + w * 4096, lane);
        TS(2);
        LBAR();
        TS(3);
        STAMP(ws, 4);
        f32x16 oacc;
        if (r < 4) attn_core<true>(smem, q0, q1, r, w, lane, oacc, ua, rws);
        else attn_core<false>(smem, q0, q1, r, w, lane, oacc, ua, rws);
        TS(4);
        const float inv = __builtin_amdgcn_rcpf(lo32(oacc[10]));
        LAS char* op = smem + K2_O + (32 * qh + l31) * 144 + 36 * hl + 8 * hh;
        *(LAS unsigned*)(op) = pk2(oacc[0] * inv, oacc[1] * inv); *(LAS unsigned*)(op + 4) = pk2(oacc[2] * inv, oacc[3] * inv);
        *(LAS unsigned*)(op + 16) = pk2(oacc[4] * inv, oacc[5] * inv); *(LAS unsigned*)(op + 20) = pk2(oacc[6] * inv, oacc[7] * inv);
        if (hh == 0) *(LAS unsigned*)(op + 32) = pk2(oacc[8] * inv, oacc[9] * inv);
        LBAR();
        TS(5);
#pragma unroll
        for (int m2 = 0; m2 < 2; ++m2) { const int n = tid + 512 * m2;
            if (n < 576) { const int i = n & 15, tt = (n >> 4) & 3, m = n >> 6;
                const v4u v = lds128(smem + K2_O + (16 * tt + i) * 144 + 16 * m);
                const int n8 = 72 * j + 8 * m, ks = n8 >> 5, g = (n8 & 31) >> 3;
                bst128<16>(v, rws, (int)OFF_O + (4 * r + tt) * (int)TT_FR + ks * 1024 + (g * 16 + i) * 16, 0); } }
        asm volatile("s_waitcnt vmcnt(0) lgkmcnt(0)\n\ts_barrier" ::: "memory");
        STAMP(ws, 5);
        TS(6);
    }
    if (w < 4) ffn_body<true>(a, smem, r, j, w, lane); else ffn_body<false>(a, smem, r, j, w, lane);
}

extern "C" void kernel_launch(void* const* d_in, const int* in_sizes, int n_in, void* d_out, int out_size, void* d_ws, size_t ws_size, hipStream_t stream) {
    (void)in_sizes; (void)n_in; (void)out_size; (void)ws_size;
    char* ws = (char*)d_ws;
    (void)hipFuncSetAttribute((const void*)k1_qkv, hipFuncAttributeMaxDynamicSharedMemorySize, K1_LDS);
    (void)hipFuncSetAttribute((const void*)k2_rest, hipFuncAttributeMaxDynamicSharedMemorySize, K2_LDS);
    K1Args a1{d_in[0], d_in[1], d_in[2], d_in[4], d_in[6], ws};
    k1_qkv<<<256, 512, K1_LDS, stream>>>(a1);
    K2Args a2{ws, d_in[0], d_in[2], d_in[4], d_in[6], d_in[5], d_in[7], d_out};
    k2_rest<<<256, 512, K2_LDS, stream>>>(a2);
}
```

```cpp
#include <hip/hip_runtime.h>
#include <hip/hip_bf16.h>
#include <stdint.h>

#define GAS __attribute__((address_space(1)))
#define LAS __attribute__((address_space(3)))
#define DI __device__ __forceinline__
typedef unsigned v4u __attribute__((ext_vector_type(4)));
typedef unsigned v2u __attribute__((ext_vector_type(2)));
typedef float f32x4 __attribute__((ext_vector_type(4)));
typedef float f32x16 __attribute__((ext_vector_type(16)));
typedef short bf16x8 __attribute__((ext_vector_type(8)));
typedef __bf16 bf2 __attribute__((ext_vector_type(2)));
typedef GAS unsigned gu32;
typedef __amdgpu_buffer_rsrc_t rsrc_t;

#define STAMPS 0

constexpr int S = 2048, D = 576, FF = 4608, N3 = 3 * D;
constexpr int NR = 32;
constexpr float LN_EPS = 1e-5f;
constexpr float QSCALE = 0.23570226039551584f * 1.4426950408889634f;

constexpr size_t FRAG = 1024;
constexpr size_t OFF_FLAG = 0;
constexpr int FL_O = 0, FL_H = 32, FL_P = 64, FL_N = 96;
constexpr size_t OFF_STAMP = 32768;
constexpr size_t OFF_QKV = 65536;
constexpr size_t QKV_G = (size_t)S * 144;
constexpr int NSL = 8;
constexpr size_t OFF_WS = OFF_QKV + 24 * QKV_G;
constexpr size_t WS_J = (size_t)1296 * FRAG;
constexpr size_t OFF_WO = OFF_WS + NSL * WS_J;
constexpr size_t OFF_O = OFF_WO + (size_t)36 * 18 * FRAG;
constexpr size_t TT_FR = (size_t)18 * FRAG;
constexpr size_t OFF_H = OFF_O + 128 * TT_FR;
constexpr size_t OFF_P = OFF_H + 128 * TT_FR;
constexpr size_t P_SRC = (size_t)36 * 2 * FRAG;
constexpr size_t OFF_CSP = OFF_P + (size_t)NR * NSL * P_SRC;
constexpr size_t WS_END = OFF_CSP + (size_t)18 * FF * 4;

__host__ __device__ constexpr int ffn_stream_base(int wv) { return (wv < 4 ? wv * 180 : 720 + (wv - 4) * 144) * 1024; }
__host__ __device__ constexpr int ffn_pos1(bool a, int t, int ks) { return t < 3 ? ks * 3 + t : 54 + ks * (a ? 2 : 1) + (t - 3); }
__host__ __device__ constexpr int ffn_pos2(bool a, int t, int ks) { return (a ? 90 : 72) + (t < 4 ? (t >> 1) * 36 + ks * 2 + (t & 1) : 72 + ks); }
DI int wg_r(int b) { return 8 * ((b & 7) >> 1) + ((b >> 3) & 7); }
DI int wg_j(int b) { return 4 * (b & 1) + (b >> 6); }

DI f32x4 mfma16(v4u a, v4u b, f32x4 c) { return __builtin_amdgcn_mfma_f32_16x16x32_bf16(__builtin_bit_cast(bf16x8, a), __builtin_bit_cast(bf16x8, b), c, 0, 0, 0); }
DI f32x16 mfma32(v4u a, v4u b, f32x16 c) { return __builtin_amdgcn_mfma_f32_32x32x16_bf16(__builtin_bit_cast(bf16x8, a), __builtin_bit_cast(bf16x8, b), c, 0, 0, 0); }
DI unsigned pk2(float a, float b) { bf2 v = {(__bf16)a, (__bf16)b}; return __builtin_bit_cast(unsigned, v); }
DI float blo(unsigned u) { return __builtin_bit_cast(float, u << 16); }
DI float bhi(unsigned u) { return __builtin_bit_cast(float, u & 0xffff0000u); }
DI rsrc_t mkrsrc(const void* p, unsigned bytes) { return __builtin_amdgcn_make_buffer_rsrc((void*)p, 0, bytes, 0x00020000); }
DI v4u bld128(rsrc_t r, int voff, int soff) { return __builtin_amdgcn_raw_buffer_load_b128(r, voff, soff, 0); }
DI v4u bld128_sc1(rsrc_t r, int voff, int soff) { return __builtin_amdgcn_raw_buffer_load_b128(r, voff, soff, 16); }
DI v2u bld64(rsrc_t r, int voff, int soff) { return __builtin_amdgcn_raw_buffer_load_b64(r, voff, soff, 0); }
template <int AUX> DI void bst128(v4u v, rsrc_t r, int voff, int soff) { __builtin_amdgcn_raw_buffer_store_b128(v, r, voff, soff, AUX); asm volatile("s_nop 1" :: "v"(v)); }
DI v4u lds128(const LAS char* p) { return *(const LAS v4u*)p; }
DI v2u lds64(const LAS char* p) { return *(const LAS v2u*)p; }
typedef short v4i16_t __attribute__((ext_vector_type(4)));
DI v2u ldstr(const LAS char* p) { return __builtin_bit_cast(v2u, __builtin_amdgcn_ds_read_tr16_b64_v4i16((LAS v4i16_t*)p)); }
DI float dot2(unsigned a, float c) { return __builtin_amdgcn_fdot2_f32_bf16(__builtin_bit_cast(bf2, a), __builtin_bit_cast(bf2, 0x3f803f80u), c, false); }
DI float dot2sq(unsigned a, float c) { const bf2 v = __builtin_bit_cast(bf2, a); return __builtin_amdgcn_fdot2_f32_bf16(v, v, c, false); }
DI void dma16(rsrc_t r, int voff, int soff, unsigned lds) {
    asm volatile("s_mov_b32 m0, %0\n\ts_nop 0\n\tbuffer_load_dwordx4 %1, %2, %3 offen lds" :: "s"(lds), "v"(voff), "s"(r), "s"(soff) : "memory");
}
#define WAITV(n) asm volatile("s_waitcnt vmcnt(" #n ")" ::: "memory")
#define BAR() asm volatile("s_barrier" ::: "memory")
#define LBAR() asm volatile("s_waitcnt lgkmcnt(0)\n\ts_barrier" ::: "memory")
#define SB() __builtin_amdgcn_sched_barrier(0)
#define PIN(x) asm volatile("" : "+v"(x))
DI float sum16(float v) { const unsigned u = __builtin_bit_cast(unsigned, v); const auto r = __builtin_amdgcn_permlane16_swap(u, u, false, false);
    return __builtin_bit_cast(float, (unsigned)r[0]) + __builtin_bit_cast(float, (unsigned)r[1]); }
DI float sum32(float v) { const unsigned u = __builtin_bit_cast(unsigned, v); const auto r = __builtin_amdgcn_permlane32_swap(u, u, false, false);
    return __builtin_bit_cast(float, (unsigned)r[0]) + __builtin_bit_cast(float, (unsigned)r[1]); }
DI float max32(float v) { const unsigned u = __builtin_bit_cast(unsigned, v); const auto r = __builtin_amdgcn_permlane32_swap(u, u, false, false);
    return __builtin_fmaxf(__builtin_bit_cast(float, (unsigned)r[0]), __builtin_bit_cast(float, (unsigned)r[1])); }
DI float lo32(float v) { const unsigned u = __builtin_bit_cast(unsigned, v); const auto r = __builtin_amdgcn_permlane32_swap(u, u, false, false);
    return __builtin_bit_cast(float, (unsigned)r[0]); }
typedef float f32x2 __attribute__((ext_vector_type(2)));
DI void ln_gelu4(f32x4 acc, f32x4 cs, float b0, float b1, float b2, float b3, float nmean, float rs, unsigned& o01, unsigned& o23) {
    float z0, z1, z2, z3, t0, t1, t2, t3;
    asm("v_fma_f32 %0, %16, %12, %8\n\tv_fma_f32 %1, %16, %13, %9\n\tv_fma_f32 %2, %16, %14, %10\n\tv_fma_f32 %3, %16, %15, %11\n\t"
        "v_fma_f32 %0, %17, %0, %18\n\tv_fma_f32 %1, %17, %1, %19\n\tv_fma_f32 %2, %17, %2, %20\n\tv_fma_f32 %3, %17, %3, %21\n\t"
        "v_mul_f32 %4, %0, %0\n\tv_mul_f32 %5, %1, %1\n\tv_mul_f32 %6, %2, %2\n\tv_mul_f32 %7, %3, %3\n\t"
        "v_fmaak_f32 %4, %22, %4, 0xc0135761\n\tv_fmaak_f32 %5, %22, %5, 0xc0135761\n\tv_fmaak_f32 %6, %22, %6, 0xc0135761\n\tv_fmaak_f32 %7, %22, %7, 0xc0135761\n\t"
        "v_mul_f32 %4, %0, %4\n\tv_mul_f32 %5, %1, %5\n\tv_mul_f32 %6, %2, %6\n\tv_mul_f32 %7, %3, %7\n\t"
        "v_exp_f32 %4, %4\n\tv_exp_f32 %5, %5\n\tv_exp_f32 %6, %6\n\tv_exp_f32 %7, %7\n\t"
        "v_add_f32 %4, 1.0, %4\n\tv_add_f32 %5, 1.0, %5\n\tv_add_f32 %6, 1.0, %6\n\tv_add_f32 %7, 1.0, %7\n\t"
        "v_rcp_f32 %4, %4\n\tv_rcp_f32 %5, %5\n\tv_rcp_f32 %6, %6\n\tv_rcp_f32 %7, %7\n\t"
        "v_mul_f32 %0, %0, %4\n\tv_mul_f32 %1, %1, %5\n\tv_mul_f32 %2, %2, %6\n\tv_mul_f32 %3, %3, %7"
        : "=&v"(z0), "=&v"(z1), "=&v"(z2), "=&v"(z3), "=&v"(t0), "=&v"(t1), "=&v"(t2), "=&v"(t3)
        : "v"(acc[0]), "v"(acc[1]), "v"(acc[2]), "v"(acc[3]), "v"(cs[0]), "v"(cs[1]), "v"(cs[2]), "v"(cs[3]), "v"(nmean), "v"(rs), "v"(b0), "v"(b1), "v"(b2), "v"(b3),
          "v"(-0.1029432397f));
    o01 = pk2(z0, z1); o23 = pk2(z2, z3);
}
#define STAMP(ws, i) do { } while (0)
#define FINE 0
#define TS(n) do { } while (0)
#define TSW(n) do { } while (0)
#define TSDECL
#define TSDUMP(ws, kidx) do { } while (0)

struct Unit { rsrc_t rs; int soff, ld, w2; int dst[4]; int cs; };
constexpr int NUNIT = 1296 + 1296 + 162;
struct UnitTab { int v[NUNIT + 1][8]; };
constexpr UnitTab make_units() {
    UnitTab T{};
    for (int u = 0; u <= NUNIT; ++u) {
        const bool iswo = u < 162, isw1 = !iswo && u < 1458, isw2 = u >= 1458 && u < NUNIT;
        const int v = iswo ? u : isw1 ? u - 162 : u - 1458;
        const int j = isw1 ? (v % 72) / 9 : v / 162, ks = isw1 ? v / 72 : (v % 162) % 18, G = isw1 ? v % 9 : (v % 162) / 18;
        T.v[u][0] = isw1 ? 0 : isw2 ? 1 : iswo ? 2 : 3;
        T.v[u][1] = isw1 ? (32 * ks * FF + 576 * j + 64 * G) * 2 : ((isw2 ? 576 * j : 0) + 32 * ks) * (D * 2) + 128 * G;
        const int base = (int)OFF_WS + j * (int)WS_J, gi = G < j ? G : G - 1;
        const bool A = isw1 ? G < 5 : (G == j || gi < 4);
        const int fp = isw1 ? ffn_pos1(A, A ? G : G - 5, ks) : ffn_pos2(A, G == j ? 4 : gi & 3, ks);
        for (int c = 0; c < 4; ++c)
            T.v[u][2 + c] = (isw1 || isw2) ? base + ffn_stream_base(A ? c : 4 + c) + fp * 1024 : iswo ? (int)OFF_WO + ((4 * G + c) * 18 + ks) * 1024 : 0x7fff0000;
        T.v[u][6] = isw1 ? (int)OFF_CSP + (ks * FF + 576 * j + 64 * G) * 4 : 0x7fff0000;
        T.v[u][7] = 0;
    }
    return T;
}
__device__ const UnitTab UNITS = make_units();
DI Unit unit_decode(const void* w1, const void* w2, const void* wo, int u) {
    const int* e = UNITS.v[u < NUNIT ? u : NUNIT];
    const int kind = e[0];
    Unit r;
    r.rs = mkrsrc(kind == 0 ? w1 : kind == 1 ? w2 : wo, kind == 3 ? 0u : kind == 2 ? D * D * 2 : D * FF * 2);
    r.ld = kind == 0 ? FF * 2 : D * 2; r.w2 = kind == 1; r.soff = e[1];
#pragma unroll
    for (int c = 0; c < 4; ++c) r.dst[c] = e[2 + c];
    r.cs = e[6];
    return r;
}
#define UST 16
DI void unit_load(const Unit& t, unsigned scr, int lane) {
    const int voff = (lane >> 3) * t.ld + (lane & 7) * 16;
#pragma unroll
    for (int m = 0; m < 4; ++m) dma16(t.rs, voff, t.soff + 8 * m * t.ld, scr + m * 1024);
}
DI void unit_store(const Unit& t, const LAS char* scr, rsrc_t rws, int lane) {
    const int g = lane >> 4, q = (lane & 15) >> 2, p = lane & 3;
    const int row0 = t.w2 ? 4 * g + q : 8 * g + q, rstep = t.w2 ? 16 : 4;
    float cs[4];
#pragma unroll
    for (int c = 0; c < 4; ++c) {
        const v2u lo = ldstr(scr + row0 * 128 + (16 * c + 4 * p) * 2);
        const v2u hi = ldstr(scr + (row0 + rstep) * 128 + (16 * c + 4 * p) * 2);
        bst128<UST>(v4u{lo.x, lo.y, hi.x, hi.y}, rws, lane * 16, t.dst[c]);
        cs[c] = sum32(sum16(dot2(hi.y, dot2(hi.x, dot2(lo.y, dot2(lo.x, 0.f))))));
    }
    const float mine = g == 0 ? cs[0] : g == 1 ? cs[1] : g == 2 ? cs[2] : cs[3];
    __builtin_amdgcn_raw_buffer_store_b32(__builtin_bit_cast(unsigned, mine), rws, lane * 4, t.cs, 16);
}

#define QST 0
struct K1Args { const void* x; const void* wqkv; const void* wo; const void* w1; const void* w2; char* ws; };
constexpr int K1_STAGE = 32768;
constexpr int K1_TAB = 4 * K1_STAGE;
constexpr int K1_LDS = K1_TAB + 4096;

__global__ __launch_bounds__(512, 2) void k1_qkv(K1Args a) {
    extern __shared__ __attribute__((aligned(16))) char smem_[];
    LAS char* smem = (LAS char*)smem_;
    const unsigned lds0 = (unsigned)(size_t)smem_;
    const int tid = threadIdx.x, lane = tid & 63, w = __builtin_amdgcn_readfirstlane(tid >> 6), b = blockIdx.x;
    STAMP(a.ws, 0);
    TSDECL; TS(0);
    if (b == 0 && tid < FL_N) *(GAS unsigned*)(a.ws + OFF_FLAG + tid * 256) = 0u;
    if (b >= 224) {
        const rsrc_t rws = mkrsrc(a.ws, (unsigned)WS_END);
        const int u0 = ((b - 224) * 8 + w) * 4;
        Unit t[4];
#pragma unroll
        for (int m = 0; m < 4; ++m) { t[m] = unit_decode(a.w1, a.w2, a.wo, u0 + m); unit_load(t[m], lds0 + w * 16384 + m * 4096, lane); }
        WAITV(12); unit_store(t[0], smem + w * 16384, rws, lane);
        WAITV(13); unit_store(t[1], smem + w * 16384 + 4096, rws, lane);
        WAITV(14); unit_store(t[2], smem + w * 16384 + 8192, rws, lane);
        WAITV(15); unit_store(t[3], smem + w * 16384 + 12288, rws, lane);
        STAMP(a.ws, 2);
        return;
    }
    const int xc = b & 7, idx = b >> 3;
    const int rt = 4 * (xc >> 1) + (idx & 3), ct = 7 * (xc & 1) + (idx >> 2);
    if (w >= 4) {
        const int lw = w - 4;
        const rsrc_t rx = mkrsrc(a.x, S * D * 2), rw = mkrsrc(a.wqkv, D * N3 * 2);
        const int vx0 = (128 * rt + 32 * lw + (lane >> 3)) * (D * 2) + ((lane & 7) ^ (lane >> 4)) * 16, vx1 = vx0 ^ 64;
        const int c16 = lane & 15, q = lane >> 4;
        const int vw0 = (16 * lw + q) * (N3 * 2) + 256 * ct + ((((c16 >> 1) ^ (q << 1)) << 1) | (c16 & 1)) * 16;
        const int vw1 = (16 * lw + q) * (N3 * 2) + 256 * ct + ((((c16 >> 1) ^ ((q << 1) | 1)) << 1) | (c16 & 1)) * 16;
        auto stage = [&](int s) {
            const unsigned sb = lds0 + (s & 3) * K1_STAGE;
#pragma unroll
            for (int m = 0; m < 4; ++m) {
                dma16(rw, m < 2 ? vw0 : vw1, (64 * s + 4 * m) * (N3 * 2), sb + 16384 + (4 * lw + m) * 1024);
                dma16(rx, (m & 1) ? vx1 : vx0, 128 * s + m * 8 * (D * 2), sb + (4 * lw + m) * 1024);
            }
        };
        const int g = lane >> 4, i = lane & 15, q4 = i >> 2, p = lane & 3;
        const int sxo = (32 * lw + i) * 128, swo = 16384 + (8 * g + q4) * 256 + 8 * p, key = (q4 << 1) | (g & 1);
        const v4u ones = {0x3f803f80u, 0x3f803f80u, 0x3f803f80u, 0x3f803f80u};
        f32x4 sx[2], sq[2], cs[2];
#pragma unroll
        for (int u = 0; u < 2; ++u) sx[u] = sq[u] = cs[u] = f32x4{0.f, 0.f, 0.f, 0.f};
        auto stats = [&](int s) {
            const LAS char* sb = smem + (s & 3) * K1_STAGE;
#pragma unroll
            for (int k2 = 0; k2 < 2; ++k2)
#pragma unroll
                for (int u = 0; u < 2; ++u) {
                    const v4u xv = lds128(sb + sxo + u * 2048 + ((4 * k2 + g) ^ ((i >> 1) & 7)) * 16);
                    const LAS char* ap = sb + swo + k2 * 8192 + (((2 * lw + u) ^ key) * 32);
                    const v2u lo = ldstr(ap), hi = ldstr(ap + 1024);
                    sx[u] = mfma16(ones, xv, sx[u]); sq[u] = mfma16(xv, xv, sq[u]); cs[u] = mfma16(v4u{lo.x, lo.y, hi.x, hi.y}, ones, cs[u]);
                }
        };
        stage(0); stage(1); stage(2);
        WAITV(16); TS(1); BAR();
        WAITV(8); BAR();
        stats(0);
#pragma unroll
        for (int s = 3; s < 9; ++s) { stage(s); stats(s - 2); WAITV(8); asm volatile("s_waitcnt lgkmcnt(0)" ::: "memory"); BAR(); }
        stats(7); WAITV(0); BAR();
        stats(8);
        LAS float* tabx = (LAS float*)(smem + K1_TAB);
        LAS float* tabc = tabx + 256;
#pragma unroll
        for (int u = 0; u < 2; ++u) {
            const float s2 = (i & 2) ? ((i & 1) ? sq[u][3] : sq[u][2]) : ((i & 1) ? sq[u][1] : sq[u][0]);
            const float mean = sx[u][0] * (1.0f / D), var = __builtin_fmaxf(__builtin_fmaf(-mean, mean, s2 * (1.0f / D)), 0.f);
            if (g == q4) *(LAS f32x2*)(tabx + (32 * lw + 16 * u + i) * 2) = f32x2{mean, __builtin_amdgcn_rsqf(var + LN_EPS)};
            if (i == 0) *(LAS f32x4*)(tabc + 32 * lw + 16 * u + 4 * g) = cs[u]; }
        LBAR();
        TS(9);
        TSDUMP(a.ws, 0);
        return;
    }
    const int th = w & 1, ch = w >> 1, h = lane >> 5, l31 = lane & 31;
    const int q = (lane & 15) >> 2, p = lane & 3, blk = (lane >> 4) & 1;
    const int xrow = (64 * th + l31) * 128, xkey = (l31 >> 1) & 7;
    const int woff = 16384 + (8 * h + q) * 256 + (((4 * ch + blk) ^ ((q << 1) | h)) * 32) + 8 * p;
    f32x16 acc[2][2];
#pragma unroll
    for (int cx = 0; cx < 2; ++cx)
#pragma unroll
        for (int ty = 0; ty < 2; ++ty)
#pragma unroll
            for (int e = 0; e < 16; ++e) acc[cx][ty][e] = 0.f;
    int xo[4];
#pragma unroll
    for (int k4 = 0; k4 < 4; ++k4) xo[k4] = xrow + ((2 * k4 + h) ^ xkey) * 16;
    v4u bA[2][2], aA[2][2], bB[2][2], aB[2][2];
    auto rd = [&](v4u (&bf)[2][2], v4u (&af)[2][2], const LAS char* sb, int k2) {
#pragma unroll
        for (int u = 0; u < 2; ++u) {
#pragma unroll
            for (int cx = 0; cx < 2; ++cx) { const LAS char* ap = sb + ((woff ^ (cx << 6)) + (2 * k2 + u) * 4096);
                const v2u lo = ldstr(ap), hi = ldstr(ap + 1024); af[u][cx] = v4u{lo.x, lo.y, hi.x, hi.y}; }
#pragma unroll
            for (int ty = 0; ty < 2; ++ty) bf[u][ty] = lds128(sb + xo[2 * k2 + u] + ty * 4096);
        }
    };
    auto mm = [&](const v4u (&bf)[2][2], const v4u (&af)[2][2]) {
#pragma unroll
        for (int u = 0; u < 2; ++u)
#pragma unroll
            for (int cx = 0; cx < 2; ++cx)
#pragma unroll
                for (int ty = 0; ty < 2; ++ty) acc[cx][ty] = mfma32(af[u][cx], bf[u][ty], acc[cx][ty]);
    };
    int dst[2][2];
#pragma unroll
    for (int cx = 0; cx < 2; ++cx)
#pragma unroll
        for (int m = 0; m < 2; ++m) { const int n8 = 128 * ct + 64 * ch + 32 * cx + 16 * m + 8 * h, ja = n8 / 72;
            dst[cx][m] = n8 < N3 ? (int)OFF_QKV + ja * (int)QKV_G + (128 * rt + 64 * th + l31) * 144 + (n8 - 72 * ja) * 2 : 0x7fff0000; }
    auto half = [&](v4u (&bn)[2][2], v4u (&an)[2][2], const LAS char* nb, int k2, const v4u (&bc)[2][2], const v4u (&ac)[2][2]) {
        SB();
        rd(bn, an, nb, k2);
        mm(bc, ac);
#pragma unroll
        for (int e = 0; e < 4; ++e) { __builtin_amdgcn_sched_group_barrier(0x008, 1, 0); __builtin_amdgcn_sched_group_barrier(0x100, 2, 0); }
#pragma unroll
        for (int e = 0; e < 4; ++e) { __builtin_amdgcn_sched_group_barrier(0x008, 1, 0); __builtin_amdgcn_sched_group_barrier(0x100, 1, 0); }
        SB();
    };
#define K1_SYNC() asm volatile("s_waitcnt lgkmcnt(12)\n\ts_barrier" ::: "memory")
    BAR();
    TS(1);
    unsigned pf[2];
#pragma unroll
    for (int m = 0; m < 2; ++m) {
        const int lx = (idx >> 2) * 55 + tid, lw2 = (idx & 3) * 96 + (tid - 55), wo = (192 + 192 * m + (lw2 >> 1)) * (N3 * 2) + 256 * ct + (lw2 & 1) * 128;
        const char* p = tid < 55 ? (const char*)a.x + (size_t)(128 * rt + (lx < 384 ? lx / 3 : 0)) * (D * 2) + (3 + 3 * m + lx % 3) * 128
                                 : (const char*)a.wqkv + ((tid < 151 && wo < D * N3 * 2) ? wo : 0);
        asm volatile("global_load_dword %0, %1, off" : "=v"(pf[m]) : "v"(p));
    }
    __builtin_amdgcn_s_waitcnt(0xC07F);
    rd(bA, aA, smem, 0);
    half(bB, aB, smem, 1, bA, aA);
    BAR();
    half(bA, aA, smem + K1_STAGE, 0, bB, aB);
    K1_SYNC();
#pragma nounroll
    for (int s = 1; s < 7; ++s) {
        half(bB, aB, smem + (s & 3) * K1_STAGE, 1, bA, aA);
        half(bA, aA, smem + ((s + 1) & 3) * K1_STAGE, 0, bB, aB);
        K1_SYNC();
    }
    half(bB, aB, smem + 3 * K1_STAGE, 1, bA, aA);
    half(bA, aA, smem, 0, bB, aB);
    half(bB, aB, smem, 1, bA, aA);
    SB(); mm(bB, aB); SB();
    TS(4);
    asm volatile("s_waitcnt vmcnt(0)" :: "v"(pf[0]), "v"(pf[1]));
    LBAR();
    const LAS float* tabc = (const LAS float*)(smem + K1_TAB) + 256;
    const LAS float* tabx = (const LAS float*)(smem + K1_TAB);
    TS(5);
    const rsrc_t rws = mkrsrc(a.ws, (unsigned)OFF_WS);
#pragma unroll
    for (int ty = 0; ty < 2; ++ty) {
        const int t = 64 * th + 32 * ty + l31;
        const float mean = tabx[t * 2], rstd = tabx[t * 2 + 1];
#pragma unroll
        for (int cx = 0; cx < 2; ++cx) {
            const float rs = rstd * (128 * ct + 64 * ch + 32 * cx < D ? QSCALE : 1.0f);
            unsigned pk[8];
#pragma unroll
            for (int qd = 0; qd < 4; ++qd) { const f32x4 c = *(const LAS f32x4*)(tabc + 64 * ch + 32 * cx + 8 * qd + 4 * h);
                pk[2 * qd] = pk2(rs * __builtin_fmaf(-mean, c[0], acc[cx][ty][4 * qd]), rs * __builtin_fmaf(-mean, c[1], acc[cx][ty][4 * qd + 1]));
                pk[2 * qd + 1] = pk2(rs * __builtin_fmaf(-mean, c[2], acc[cx][ty][4 * qd + 2]), rs * __builtin_fmaf(-mean, c[3], acc[cx][ty][4 * qd + 3])); }
#pragma unroll
            for (int m = 0; m < 2; ++m) {
                const auto s0 = __builtin_amdgcn_permlane32_swap(pk[4 * m], pk[4 * m + 2], false, false), s1 = __builtin_amdgcn_permlane32_swap(pk[4 * m + 1], pk[4 * m + 3], false, false);
                bst128<QST>(v4u{(unsigned)s0[0], (unsigned)s1[0], (unsigned)s0[1], (unsigned)s1[1]}, rws, dst[cx][m], ty * 32 * 144); }
        }
    }
    STAMP(a.ws, 2);
    TS(6); TSW(7);
    TSDUMP(a.ws, 0);
}

struct K2Args { char* ws; const void* x; const void* wo; const void* w1; const void* w2; const void* b1; const void* b2; void* out; };
constexpr int KV_HEAD = 320 * 40;
constexpr int K2_K = 0, K2_V = 4 * KV_HEAD, K2_O = 8 * KV_HEAD, K2_SCR = K2_O + 64 * 144;
constexpr int K4_X = 0, K4_H = 73728, K4_ST = 147456, K4_CS = K4_ST + 512, K4_CNT = K4_CS + 2304, K2_LDS = K4_CNT + 64;
static_assert(K2_SCR + 8 * 4096 <= K4_ST, "LDS map");

DI void flag_add(char* ws, int f) { __hip_atomic_fetch_add((gu32*)(ws + OFF_FLAG + f * 256), 1u, __ATOMIC_RELAXED, __HIP_MEMORY_SCOPE_AGENT); }
DI void flag_wait(char* ws, int f, unsigned n) {
    gu32* p = (gu32*)(ws + OFF_FLAG + f * 256);
    for (unsigned spins = 0; __hip_atomic_load(p, __ATOMIC_RELAXED, __HIP_MEMORY_SCOPE_AGENT) < n && spins < (1u << 20); ++spins) __builtin_amdgcn_s_sleep(1);
    __builtin_amdgcn_fence(__ATOMIC_ACQUIRE, "wavefront");
}
DI void flag_wait_all(char* ws, int f, unsigned n, int lane) {
    gu32* p = (gu32*)(ws + OFF_FLAG + (f + (lane & 31)) * 256);
    for (unsigned spins = 0; !__all(__hip_atomic_load(p, __ATOMIC_RELAXED, __HIP_MEMORY_SCOPE_AGENT) >= n) && spins < (1u << 20); ++spins) __builtin_amdgcn_s_sleep(1);
    __builtin_amdgcn_fence(__ATOMIC_ACQUIRE, "wavefront");
}
DI void tile_in(const char* src, LAS char* smem, int tid) {
    const rsrc_t rs = mkrsrc(src, 4 * (unsigned)TT_FR);
    v4u t[9];
#pragma unroll
    for (int m = 0; m < 9; ++m) t[m] = bld128_sc1(rs, tid * 16, m * 8192);
#pragma unroll
    for (int m = 0; m < 9; ++m) *(LAS v4u*)(smem + tid * 16 + m * 8192) = t[m];
}

template <bool EDGE> DI void attn_core(LAS char* smem, const v4u q0, const v4u q1, int r, int w, int lane, f32x16& oacc, const Unit& ua, rsrc_t rws) {
    const int hl = w & 3, qh = w >> 2, l31 = lane & 31, hh = lane >> 5;
    const int tmin = EDGE ? ((256 - 64 * r - 32 * qh) >> 5) : 0;
    const float ninf = -__builtin_inff();
    const LAS char* kp = smem + K2_K + (hl * 320 + 32 * qh + l31) * 40;
    v4u ka[9], kb[9];
#pragma unroll
    for (int t = 0; t < 9; ++t) { const v2u a0 = lds64(kp + t * 1280 + 16 * hh), a1 = lds64(kp + t * 1280 + 16 * hh + 8);
        ka[t] = v4u{a0.x, a0.y, a1.x, a1.y};
        const v2u c0 = lds64(kp + t * 1280 + 32), c1 = lds64(kp + t * 1280 + 24);
        kb[t] = v4u{c0.x, c0.y, c1.x, c1.y}; }
    auto score = [&](int t, const f32x16& c) { return mfma32(kb[t], q1, mfma32(ka[t], q0, c)); };
    bool le[16];
#pragma unroll
    for (int rg = 0; rg < 16; ++rg) le[rg] = (rg & 3) + 8 * (rg >> 2) + 4 * hh <= l31;
    auto merged = [&](const f32x16& c) { const f32x16 s8 = score(8, c); f32x16 s0;
        if (EDGE) { s0 = s8;
#pragma unroll
            for (int rg = 0; rg < 16; ++rg) s0[rg] = ninf; } else s0 = score(0, c);
        f32x16 m;
#pragma unroll
        for (int rg = 0; rg < 16; ++rg) m[rg] = le[rg] ? s8[rg] : s0[rg];
        return m; };
    const f32x16 zero = {};
    float mx;
    { const f32x16 m = merged(zero); mx = __builtin_fmaxf(m[0], m[1]);
#pragma unroll
      for (int rg = 2; rg < 16; rg += 2) mx = __builtin_fmaxf(__builtin_fmaxf(mx, m[rg]), m[rg + 1]); }
#pragma unroll
    for (int t = 1; t < 8; ++t) if (!EDGE || t >= tmin) { const f32x16 s = score(t, zero);
#pragma unroll
        for (int rg = 0; rg < 16; rg += 2) mx = __builtin_fmaxf(__builtin_fmaxf(mx, s[rg]), s[rg + 1]); }
    mx = max32(mx);
    WAITV(0); unit_store(ua, smem + K2_SCR + w * 4096, rws, lane);
    f32x16 cm;
#pragma unroll
    for (int rg = 0; rg < 16; ++rg) cm[rg] = -mx;
    const LAS char* vp = smem + K2_V + (hl * 320 + 32 * qh + 4 * hh + ((lane & 15) >> 2)) * 40 + (((lane >> 4) & 1) ? 32 : 8 * (lane & 3));
    auto pv = [&](int t, const unsigned (&pw)[8]) {
#pragma unroll
        for (int s = 0; s < 2; ++s) {
            const v2u lo = ldstr(vp + (t * 32 + 16 * s) * 40), hi = ldstr(vp + (t * 32 + 16 * s + 8) * 40);
            oacc = mfma32(v4u{lo.x, lo.y, hi.x, hi.y}, v4u{pw[4 * s], pw[4 * s + 1], pw[4 * s + 2], pw[4 * s + 3]}, oacc);
        } };
    oacc = zero;
    { const f32x16 m = merged(cm); unsigned pw[8], p8[8], p0[8];
#pragma unroll
      for (int e = 0; e < 8; ++e) { pw[e] = pk2(__builtin_amdgcn_exp2f(m[2 * e]), __builtin_amdgcn_exp2f(m[2 * e + 1]));
          const unsigned mk = (le[2 * e] ? 0xffffu : 0u) | (le[2 * e + 1] ? 0xffff0000u : 0u); p8[e] = pw[e] & mk; p0[e] = pw[e] & ~mk; }
      pv(8, p8); if (!EDGE) pv(0, p0); }
#pragma unroll
    for (int t = 1; t < 8; ++t) if (!EDGE || t >= tmin) { const f32x16 s = score(t, cm); unsigned pw[8];
#pragma unroll
        for (int e = 0; e < 8; ++e) pw[e] = pk2(__builtin_amdgcn_exp2f(s[2 * e]), __builtin_amdgcn_exp2f(s[2 * e + 1]));
        pv(t, pw); }
}

#define RD 24

template <int T, int OFS, class Side>
DI void ffn_pass(f32x4 (&acc)[3][4], v4u (&ring)[RD], rsrc_t rw, int pos, const LAS char* bp, int lane, Side side) {
    v4u bf[4];
#pragma unroll
    for (int tt = 0; tt < 4; ++tt) bf[tt] = lds128(bp + tt * 18432);
#pragma unroll
    for (int t = 0; t < T; ++t)
#pragma unroll
        for (int tt = 0; tt < 4; ++tt) acc[t][tt] = f32x4{0.f, 0.f, 0.f, 0.f};
#pragma unroll
    for (int u = 0; u < 18; ++u) {
        SB();
        v4u bn[4];
        if (u + 1 < 18) {
#pragma unroll
            for (int tt = 0; tt < 4; ++tt) bn[tt] = lds128(bp + tt * 18432 + (u + 1) * 1024);
        }
#pragma unroll
        for (int t = 0; t < T; ++t) {
            const int n = u * T + t, sl = (n + OFS) % RD;
#pragma unroll
            for (int tt = 0; tt < 4; ++tt) acc[t][tt] = mfma16(ring[sl], bf[tt], acc[t][tt]);
            ring[sl] = bld128(rw, lane * 16 + (n & 3) * 1024, pos + (n >> 2) * 4096);
        }
        side(u);
#pragma unroll
        for (int t = 0; t < T; ++t) {
            if (T == 1) {
#pragma unroll
                for (int e = 0; e < 4; ++e) { __builtin_amdgcn_sched_group_barrier(0x008, 1, 0); __builtin_amdgcn_sched_group_barrier(0x100, 1, 0); }
            } else if (T == 2) {
#pragma unroll
                for (int e = 0; e < 2; ++e) { __builtin_amdgcn_sched_group_barrier(0x008, 2, 0); __builtin_amdgcn_sched_group_barrier(0x100, 1, 0); }
            } else { __builtin_amdgcn_sched_group_barrier(0x008, 4, 0); if (t < 4) __builtin_amdgcn_sched_group_barrier(0x100, 1, 0); }
            __builtin_amdgcn_sched_group_barrier(0x020, 1, 0);
        }
        SB();
        if (u + 1 < 18) {
#pragma unroll
            for (int tt = 0; tt < 4; ++tt) bf[tt] = bn[tt];
        }
    }
#pragma unroll
    for (int t = 0; t < T; ++t)
#pragma unroll
        for (int tt = 0; tt < 4; ++tt) PIN(acc[t][tt]);
    asm volatile("s_nop 7" ::: "memory");
}

template <bool A> DI void ffn_body(const K2Args& a, LAS char* smem, int r, int j, int w, int lane
) {
    constexpr int NT = A ? 5 : 4;
    const int tid = threadIdx.x, g = lane >> 4, i = lane & 15, s = w & 3;
    char* ws = a.ws;
    const rsrc_t rws = mkrsrc(ws, (unsigned)WS_END);
    v4u ring[RD];
    {
        if (tid == 448) { flag_add(ws, FL_O + r); flag_wait(ws, FL_O + r, 8); }
        const int dt = A ? 4 * j + w : 32 + (j >> 1), tt0 = A ? 0 : 2 * (j & 1);
        const rsrc_t rw = mkrsrc(ws + OFF_WO, 36 * 18 * 1024), rx = mkrsrc(a.x, S * D * 2);
        v4u af[18]; v2u xr[4];
        if (A || w == 4) {
#pragma unroll
            for (int ks = 0; ks < 18; ++ks) af[ks] = bld128(rw, lane * 16 + (ks & 3) * 1024, dt * 18432 + (ks >> 2) * 4096);
#pragma unroll
            for (int u = 0; u < 4; ++u) xr[u] = bld64(rx, (64 * r + 16 * ((tt0 + u) & 3) + i) * (D * 2) + (16 * dt + 4 * g) * 2, 0);
        }
        BAR();
        TS(7);
        STAMP(ws, 6);
        tile_in(ws + OFF_O + 4 * r * TT_FR, smem, tid);
        LBAR();
        TS(8);
        const rsrc_t rf = mkrsrc(ws + OFF_WS + j * WS_J + ffn_stream_base(w), (A ? 180 : 144) * 1024);
        auto fill = [&](int n) { ring[n] = bld128_sc1(rf, lane * 16 + (n & 3) * 1024, (n >> 2) * 4096); };
        if (!(A || w == 4)) {
#pragma unroll
            for (int n = 0; n < RD / 2; ++n) fill(n);
            if (w == 7) flag_wait_all(ws, FL_O, 8, lane);
        }
        if (A || w == 4) {
            f32x4 acc[4];
#pragma unroll
            for (int u = 0; u < 4; ++u) acc[u] = f32x4{0.f, 0.f, 0.f, 0.f};
            const LAS char* bp = smem + lane * 16;
            int to[4];
#pragma unroll
            for (int u = 0; u < 4; ++u) to[u] = ((tt0 + u) & 3) * 18432;
            v4u bf[2][4];
#pragma unroll
            for (int u = 0; u < 4; ++u) bf[0][u] = lds128(bp + to[u]);
#pragma unroll
            for (int ks = 0; ks < 18; ++ks) {
                SB();
                if (ks < 17) {
#pragma unroll
                    for (int u = 0; u < 4; ++u) bf[(ks + 1) & 1][u] = lds128(bp + (ks + 1) * 1024 + to[u]); }
#pragma unroll
                for (int u = 0; u < 4; ++u) acc[u] = mfma16(af[ks], bf[ks & 1][u], acc[u]);
                if (ks < RD / 2) fill(ks);
#pragma unroll
                for (int u = 0; u < 4; ++u) { __builtin_amdgcn_sched_group_barrier(0x008, 1, 0); __builtin_amdgcn_sched_group_barrier(0x100, 1, 0); }
                SB();
            }
#pragma unroll
            for (int u = 0; u < 4; ++u) if (A || u < 2) { const int tt = (tt0 + u) & 3;
                const v2u o = {pk2(acc[u][0] + blo(xr[u].x), acc[u][1] + bhi(xr[u].x)), pk2(acc[u][2] + blo(xr[u].y), acc[u][3] + bhi(xr[u].y))};
                __builtin_amdgcn_raw_buffer_store_b64(o, rws, (int)OFF_H + (4 * r + tt) * (int)TT_FR + (dt >> 1) * 1024 + ((2 * (dt & 1) + (g >> 1)) * 16 + i) * 16 + (g & 1) * 8, 0, 16); }
        }
        TS(9);
        SB();
        if (w != 7) {
#pragma unroll
            for (int n = RD / 2; n < RD; ++n) fill(n); }
        SB();
        asm volatile("s_waitcnt vmcnt(%0) lgkmcnt(0)\n\ts_barrier" :: "n"(RD - RD / 2) : "memory");
        STAMP(ws, 7);
        TS(10);
        if (w == 7) {
            if (lane == 0) { flag_add(ws, FL_H + r); flag_wait(ws, FL_H + r, 8); }
            SB();
#pragma unroll
            for (int n = RD / 2; n < RD; ++n) fill(n); }
    }
    const rsrc_t rw = mkrsrc(ws + OFF_WS + j * WS_J + ffn_stream_base(w), (A ? 180 : 144) * 1024);
    BAR();
    TS(14);
    STAMP(ws, 8);
    v4u ht[9];
    { const rsrc_t rs = mkrsrc(ws + OFF_H + 4 * r * TT_FR, 4 * (unsigned)TT_FR);
#pragma unroll
      for (int m = 0; m < 9; ++m) ht[m] = bld128_sc1(rs, tid * 16, m * 8192); }
    SB();
    v2u bb[NT];
#pragma unroll
    for (int t = 0; t < NT; ++t) bb[t] = *(const GAS v2u*)((const char*)a.b1 + (576 * j + 16 * (4 * ((A ? 0 : 5) + t) + s) + 4 * g) * 2);
    unsigned pc[18], pc2[18];
    const rsrc_t rc = mkrsrc(ws + OFF_CSP, 18 * FF * 4);
#pragma unroll
    for (int ks = 0; ks < 18; ++ks) pc[ks] = __builtin_amdgcn_raw_buffer_load_b32(rc, (576 * j + tid) * 4, ks * FF * 4, 16);
    if (w == 4) {
#pragma unroll
        for (int ks = 0; ks < 18; ++ks) pc2[ks] = __builtin_amdgcn_raw_buffer_load_b32(rc, (576 * j + 512 + lane) * 4, ks * FF * 4, 16); }
    SB();
#pragma unroll
    for (int m = 0; m < 9; ++m) *(LAS v4u*)(smem + K4_X + tid * 16 + m * 8192) = ht[m];
    LBAR();
    TS(15);
    LAS float* st = (LAS float*)(smem + K4_ST);
    if (!A) {
        f32x4 s1 = {0.f, 0.f, 0.f, 0.f}, s2 = s1;
        const v4u ones = {0x3f803f80u, 0x3f803f80u, 0x3f803f80u, 0x3f803f80u};
#pragma unroll
        for (int kh = 0; kh < 2; ++kh) { v4u v[9];
#pragma unroll
            for (int k = 0; k < 9; ++k) v[k] = lds128(smem + K4_X + (s * 18 + 9 * kh + k) * 1024 + lane * 16);
#pragma unroll
            for (int k = 0; k < 9; ++k) { s1 = mfma16(ones, v[k], s1); s2 = mfma16(v[k], v[k], s2); } }
        const float sq = (i & 2) ? ((i & 1) ? s2[3] : s2[2]) : ((i & 1) ? s2[1] : s2[0]);
        const float mean = s1[0] * (1.0f / D), var = __builtin_fmaxf(__builtin_fmaf(-mean, mean, sq * (1.0f / D)), 0.f);
        if (g == (i >> 2)) *(LAS f32x2*)(st + (16 * s + i) * 2) = f32x2{mean, __builtin_amdgcn_rsqf(var + LN_EPS)};
    }
    { float sum = 0.f;
#pragma unroll
      for (int ks = 0; ks < 18; ++ks) sum += __builtin_bit_cast(float, pc[ks]);
      *(LAS float*)(smem + K4_CS + tid * 4) = sum;
      if (w == 4) { sum = 0.f;
#pragma unroll
          for (int ks = 0; ks < 18; ++ks) sum += __builtin_bit_cast(float, pc2[ks]);
          *(LAS float*)(smem + K4_CS + (512 + lane) * 4) = sum; }
      asm volatile("s_waitcnt lgkmcnt(0)" ::: "memory");
      if (lane == 0) __hip_atomic_fetch_add((LAS unsigned*)(smem + K4_CNT), 1u, __ATOMIC_RELAXED, __HIP_MEMORY_SCOPE_WORKGROUP); }
    if (A) __builtin_amdgcn_s_setprio(1);
    STAMP(ws, 9);
    f32x4 acc[3][4];
    auto none = [](int) {};
    auto act = [&](int slot, int t) { const int ft = 4 * ((A ? 0 : 5) + t) + s;
        const float b0 = blo(bb[t].x), b1 = bhi(bb[t].x), b2 = blo(bb[t].y), b3 = bhi(bb[t].y);
        const f32x4 cs = *(const LAS f32x4*)(smem + K4_CS + (16 * ft + 4 * g) * 4);
#pragma unroll
        for (int tt = 0; tt < 4; ++tt) { const f32x2 mr = *(const LAS f32x2*)(st + (16 * tt + i) * 2);
            unsigned o01, o23; ln_gelu4(acc[slot][tt], cs, b0, b1, b2, b3, -mr[0], mr[1], o01, o23);
            *(LAS v2u*)(smem + K4_H + (tt * 18 + (ft >> 1)) * 1024 + lane * 16 + (ft & 1) * 8) = v2u{o01, o23}; } };
    ffn_pass<3, 0>(acc, ring, rw, RD * 1024, smem + K4_X + lane * 16, lane, none);
    TS(16);
    while (*(volatile LAS unsigned*)(smem + K4_CNT) < 8u) { }
    asm volatile("" ::: "memory");
    act(0, 0); act(1, 1); act(2, 2);
    TS(11);
    ffn_pass<NT - 3, 54 % RD>(acc, ring, rw, (54 + RD) * 1024, smem + K4_X + lane * 16, lane, none);
    TS(23);
    act(0, 3); if (A) act(1, 4);
    TS(17);
    STAMP(ws, 10);
    LBAR();
    TS(18);
    STAMP(ws, 11);
    constexpr int G2 = A ? 90 : 72;
    const LAS char* hp = smem + K4_H + lane * 16;
    const rsrc_t rp = mkrsrc(ws + OFF_P + (size_t)r * NSL * P_SRC, NSL * (unsigned)P_SRC);
    v4u outq[4];
    auto pack = [&]() {
#pragma unroll
        for (int t = 0; t < 2; ++t)
#pragma unroll
            for (int tp = 0; tp < 2; ++tp) {
                outq[t * 2 + tp] = v4u{pk2(acc[t][2 * tp][0], acc[t][2 * tp][1]), pk2(acc[t][2 * tp][2], acc[t][2 * tp][3]), pk2(acc[t][2 * tp + 1][0], acc[t][2 * tp + 1][1]), pk2(acc[t][2 * tp + 1][2], acc[t][2 * tp + 1][3])};
                PIN(outq[t * 2 + tp]); } };
    auto put = [&](int k, int ps) { const int gi = (A ? 0 : 4) + 2 * ps + (k >> 1), dt = 4 * (gi < j ? gi : gi + 1) + s;
        bst128<16>(outq[k], rp, lane * 16, j * (int)P_SRC + (dt * 2 + (k & 1)) * 1024); };
    ffn_pass<2, G2 % RD>(acc, ring, rw, (G2 + RD) * 1024, hp, lane, none);
    pack();
    ffn_pass<2, (G2 + 36) % RD>(acc, ring, rw, (G2 + 36 + RD) * 1024, hp, lane, [&](int u) { if (u >= 1 && u <= 7 && (u & 1)) put(u >> 1, 0); });
    pack();
    if (!A) {
#pragma unroll
        for (int k = 0; k < 4; ++k) put(k, 1); }
    TS(19);
    if (A) ffn_pass<1, (G2 + 72) % RD>(acc, ring, rw, (G2 + 72 + RD) * 1024, hp, lane, [&](int u) { if (u <= 6 && !(u & 1)) put(u >> 1, 1); });
    STAMP(ws, 12);
    asm volatile("s_waitcnt vmcnt(0)\n\ts_barrier" ::: "memory");
    TS(20);
    if (tid == 448) { flag_add(ws, FL_P + r); flag_wait(ws, FL_P + r, 8); }
    BAR();
    STAMP(ws, 13);
    TS(21);
    if (A || w == 4) {
        const int dt = A ? 4 * j + s : 32 + (j >> 1);
        v4u pv[2][8];
#pragma unroll
        for (int tp = 0; tp < 2; ++tp)
#pragma unroll
            for (int src = 0; src < 8; ++src) pv[tp][src] = bld128_sc1(rp, lane * 16, (A ? src != j : tp == (j & 1)) ? src * (int)P_SRC + (dt * 2 + tp) * 1024 : 0x7fff0000);
        const v2u b2v = *(const GAS v2u*)((const char*)a.b2 + (16 * dt + 4 * g) * 2);
        SB();
#pragma unroll
        for (int tp = 0; tp < 2; ++tp) {
            if (!A && tp != (j & 1)) continue;
#pragma unroll
            for (int u = 0; u < 2; ++u) { const int tt = 2 * tp + u;
                const v2u hv = lds64(smem + K4_X + (tt * 18 + (dt >> 1)) * 1024 + ((2 * (dt & 1) + (g >> 1)) * 16 + i) * 16 + (g & 1) * 8);
                f32x4 o = {blo(hv.x) + blo(b2v.x), bhi(hv.x) + bhi(b2v.x), blo(hv.y) + blo(b2v.y), bhi(hv.y) + bhi(b2v.y)};
                if (A) o += acc[0][tt];
#pragma unroll
                for (int src = 0; src < 8; ++src) o += f32x4{blo(pv[tp][src][2 * u]), bhi(pv[tp][src][2 * u]), blo(pv[tp][src][2 * u + 1]), bhi(pv[tp][src][2 * u + 1])};
                *(GAS v2u*)((char*)a.out + (size_t)(64 * r + 16 * tt + i) * (D * 2) + (16 * dt + 4 * g) * 2) = v2u{pk2(o[0], o[1]), pk2(o[2], o[3])};
            }
        }
    }
    STAMP(ws, 14);
    TSW(22);
    TSDUMP(ws, 1);
}

__global__ __launch_bounds__(512, 2) void k2_rest(K2Args a) {
    extern __shared__ __attribute__((aligned(16))) char smem_[];
    LAS char* smem = (LAS char*)smem_;
    const unsigned lds0 = (unsigned)(size_t)smem_;
    char* ws = a.ws;
    const int tid = threadIdx.x, lane = tid & 63, w = __builtin_amdgcn_readfirstlane(tid >> 6), b = blockIdx.x;
    const int j = wg_j(b), r = wg_r(b);
    STAMP(ws, 3);
    TSDECL; TS(0);
    const rsrc_t rws = mkrsrc(ws, (unsigned)WS_END);
    {
        const int hl = w & 3, qh = w >> 2, l31 = lane & 31, hh = lane >> 5;
        const rsrc_t rk = mkrsrc(ws + OFF_QKV + (8 + j) * QKV_G, S * 144), rv = mkrsrc(ws + OFF_QKV + (16 + j) * QKV_G, S * 144);
        const int tok0 = tid / 9, mm = tid - 9 * tok0;
        v4u kk[6], vv[6];
#pragma unroll
        for (int m = 0; m < 6; ++m) { const int tok = tok0 + 56 * m, o_ = (64 * r - 256) * 144 + tid * 16 + m * 56 * 144;
            const int off = (tid < 504 && tok < 320 && o_ >= 0) ? o_ : 0x7ffffff0;
            kk[m] = bld128(rk, off, 0); vv[m] = bld128(rv, off, 0); }
        const char* qp = ws + OFF_QKV + j * QKV_G + (size_t)(64 * r + 32 * qh + l31) * 144 + 36 * hl;
        v4u q0, q1;
        { const GAS unsigned* qq = (const GAS unsigned*)(qp + 16 * hh); q0 = v4u{qq[0], qq[1], qq[2], qq[3]};
          const unsigned t = *(const GAS unsigned*)(qp + 32); q1 = v4u{hh == 0 ? t : 0u, 0u, 0u, 0u}; }
        const Unit ua = unit_decode(a.w1, a.w2, a.wo, 1024 + b * 8 + w);
        for (int n = tid; n < 1280; n += 512) { *(LAS unsigned*)(smem + K2_K + n * 40 + 36) = 0u; *(LAS unsigned*)(smem + K2_V + n * 40 + 36) = 0x3f80u; }
        if (tid == 0) *(LAS unsigned*)(smem + K4_CNT) = 0u;
        int lo_[4];
#pragma unroll
        for (int e = 0; e < 4; ++e) { const int dw = 4 * mm + e, h = dw / 9; lo_[e] = (h * 320 + tok0) * 40 + (dw - 9 * h) * 4; }
        TS(1);
        if (tid < 504) {
#pragma unroll
            for (int m = 0; m < 6; ++m) if (m < 5 || tok0 + 280 < 320) {
#pragma unroll
                for (int e = 0; e < 4; ++e) { *(LAS unsigned*)(smem + K2_K + lo_[e] + m * 56 * 40) = kk[m][e]; *(LAS unsigned*)(smem + K2_V + lo_[e] + m * 56 * 40) = vv[m][e]; } }
        }
        unit_load(ua, lds0 + K2_SCR + w * 4096, lane);
        TS(2);
        LBAR();
        TS(3);
        STAMP(ws, 4);
        f32x16 oacc;
        if (r < 4) attn_core<true>(smem, q0, q1, r, w, lane, oacc, ua, rws);
        else attn_core<false>(smem, q0, q1, r, w, lane, oacc, ua, rws);
        TS(4);
        const float inv = __builtin_amdgcn_rcpf(lo32(oacc[10]));
        LAS char* op = smem + K2_O + (32 * qh + l31) * 144 + 36 * hl + 8 * hh;
        *(LAS unsigned*)(op) = pk2(oacc[0] * inv, oacc[1] * inv); *(LAS unsigned*)(op + 4) = pk2(oacc[2] * inv, oacc[3] * inv);
        *(LAS unsigned*)(op + 16) = pk2(oacc[4] * inv, oacc[5] * inv); *(LAS unsigned*)(op + 20) = pk2(oacc[6] * inv, oacc[7] * inv);
        if (hh == 0) *(LAS unsigned*)(op + 32) = pk2(oacc[8] * inv, oacc[9] * inv);
        LBAR();
        TS(5);
#pragma unroll
        for (int m2 = 0; m2 < 2; ++m2) { const int n = tid + 512 * m2;
            if (n < 576) { const int i = n & 15, tt = (n >> 4) & 3, m = n >> 6;
                const v4u v = lds128(smem + K2_O + (16 * tt + i) * 144 + 16 * m);
                const int n8 = 72 * j + 8 * m, ks = n8 >> 5, g = (n8 & 31) >> 3;
                bst128<16>(v, rws, (int)OFF_O + (4 * r + tt) * (int)TT_FR + ks * 1024 + (g * 16 + i) * 16, 0); } }
        asm volatile("s_waitcnt vmcnt(0) lgkmcnt(0)\n\ts_barrier" ::: "memory");
        STAMP(ws, 5);
        TS(6);
    }
    if (w < 4) ffn_body<true>(a, smem, r, j, w, lane); else ffn_body<false>(a, smem, r, j, w, lane);
}

extern "C" void kernel_launch(void* const* d_in, const int* in_sizes, int n_in, void* d_out, int out_size, void* d_ws, size_t ws_size, hipStream_t stream) {
    (void)in_sizes; (void)n_in; (void)out_size; (void)ws_size;
    char* ws = (char*)d_ws;
    (void)hipFuncSetAttribute((const void*)k1_qkv, hipFuncAttributeMaxDynamicSharedMemorySize, K1_LDS);
    (void)hipFuncSetAttribute((const void*)k2_rest, hipFuncAttributeMaxDynamicSharedMemorySize, K2_LDS);
    K1Args a1{d_in[0], d_in[1], d_in[2], d_in[4], d_in[6], ws};
    k1_qkv<<<256, 512, K1_LDS, stream>>>(a1);
    K2Args a2{ws, d_in[0], d_in[2], d_in[4], d_in[6], d_in[5], d_in[7], d_out};
    k2_rest<<<256, 512, K2_LDS, stream>>>(a2);
}
```

```cpp
#include <hip/hip_runtime.h>
#include <hip/hip_bf16.h>
#include <stdint.h>

#define GAS __attribute__((address_space(1)))
#define LAS __attribute__((address_space(3)))
#define DI __device__ __forceinline__
typedef unsigned v4u __attribute__((ext_vector_type(4)));
typedef unsigned v2u __attribute__((ext_vector_type(2)));
typedef float f32x4 __attribute__((ext_vector_type(4)));
typedef float f32x16 __attribute__((ext_vector_type(16)));
typedef short bf16x8 __attribute__((ext_vector_type(8)));
typedef __bf16 bf2 __attribute__((ext_vector_type(2)));
typedef GAS unsigned gu32;
typedef __amdgpu_buffer_rsrc_t rsrc_t;

#define STAMPS 0

constexpr int S = 2048, D = 576, FF = 4608, N3 = 3 * D;
constexpr int NR = 32;
constexpr float LN_EPS = 1e-5f;
constexpr float QSCALE = 0.23570226039551584f * 1.4426950408889634f;

constexpr size_t FRAG = 1024;
constexpr size_t OFF_FLAG = 0;
constexpr int FL_O = 0, FL_H = 32, FL_P = 64, FL_N = 96;
constexpr size_t OFF_STAMP = 32768;
constexpr size_t OFF_QKV = 65536;
constexpr size_t QKV_G = (size_t)S * 144;
constexpr int NSL = 8;
constexpr size_t OFF_WS = OFF_QKV + 24 * QKV_G;
constexpr size_t WS_J = (size_t)1296 * FRAG;
constexpr size_t OFF_WO = OFF_WS + NSL * WS_J;
constexpr size_t OFF_O = OFF_WO + (size_t)36 * 18 * FRAG;
constexpr size_t TT_FR = (size_t)18 * FRAG;
constexpr size_t OFF_H = OFF_O + 128 * TT_FR;
constexpr size_t OFF_P = OFF_H + 128 * TT_FR;
constexpr size_t P_SRC = (size_t)36 * 2 * FRAG;
constexpr size_t OFF_CSP = OFF_P + (size_t)NR * NSL * P_SRC;
constexpr size_t WS_END = OFF_CSP + (size_t)18 * FF * 4;

__host__ __device__ constexpr int ffn_stream_base(int wv) { return (wv < 4 ? wv * 180 : 720 + (wv - 4) * 144) * 1024; }
__host__ __device__ constexpr int ffn_pos32(bool a, int c, int k16) { return a ? k16 * 2 + c : c * 36 + k16; }
__host__ __device__ constexpr int ffn_pos2(bool a, int t, int ks) { return (a ? 90 : 72) + (t < 4 ? (t >> 1) * 36 + ks * 2 + (t & 1) : 72 + ks); }
DI int wg_r(int b) { return 8 * ((b & 7) >> 1) + ((b >> 3) & 7); }
DI int wg_j(int b) { return 4 * (b & 1) + (b >> 6); }

DI f32x4 mfma16(v4u a, v4u b, f32x4 c) { return __builtin_amdgcn_mfma_f32_16x16x32_bf16(__builtin_bit_cast(bf16x8, a), __builtin_bit_cast(bf16x8, b), c, 0, 0, 0); }
DI f32x16 mfma32(v4u a, v4u b, f32x16 c) { return __builtin_amdgcn_mfma_f32_32x32x16_bf16(__builtin_bit_cast(bf16x8, a), __builtin_bit_cast(bf16x8, b), c, 0, 0, 0); }
DI unsigned pk2(float a, float b) { bf2 v = {(__bf16)a, (__bf16)b}; return __builtin_bit_cast(unsigned, v); }
DI float blo(unsigned u) { return __builtin_bit_cast(float, u << 16); }
DI float bhi(unsigned u) { return __builtin_bit_cast(float, u & 0xffff0000u); }
DI rsrc_t mkrsrc(const void* p, unsigned bytes) { return __builtin_amdgcn_make_buffer_rsrc((void*)p, 0, bytes, 0x00020000); }
DI v4u bld128(rsrc_t r, int voff, int soff) { return __builtin_amdgcn_raw_buffer_load_b128(r, voff, soff, 0); }
DI v4u bld128_sc1(rsrc_t r, int voff, int soff) { return __builtin_amdgcn_raw_buffer_load_b128(r, voff, soff, 16); }
DI v2u bld64(rsrc_t r, int voff, int soff) { return __builtin_amdgcn_raw_buffer_load_b64(r, voff, soff, 0); }
template <int AUX> DI void bst128(v4u v, rsrc_t r, int voff, int soff) { __builtin_amdgcn_raw_buffer_store_b128(v, r, voff, soff, AUX); asm volatile("s_nop 1" :: "v"(v)); }
DI v4u lds128(const LAS char* p) { return *(const LAS v4u*)p; }
DI v2u lds64(const LAS char* p) { return *(const LAS v2u*)p; }
typedef short v4i16_t __attribute__((ext_vector_type(4)));
DI v2u ldstr(const LAS char* p) { return __builtin_bit_cast(v2u, __builtin_amdgcn_ds_read_tr16_b64_v4i16((LAS v4i16_t*)p)); }
DI float dot2(unsigned a, float c) { return __builtin_amdgcn_fdot2_f32_bf16(__builtin_bit_cast(bf2, a), __builtin_bit_cast(bf2, 0x3f803f80u), c, false); }
DI float dot2sq(unsigned a, float c) { const bf2 v = __builtin_bit_cast(bf2, a); return __builtin_amdgcn_fdot2_f32_bf16(v, v, c, false); }
DI void dma16(rsrc_t r, int voff, int soff, unsigned lds) {
    asm volatile("s_mov_b32 m0, %0\n\ts_nop 0\n\tbuffer_load_dwordx4 %1, %2, %3 offen lds" :: "s"(lds), "v"(voff), "s"(r), "s"(soff) : "memory");
}
#define WAITV(n) asm volatile("s_waitcnt vmcnt(" #n ")" ::: "memory")
#define BAR() asm volatile("s_barrier" ::: "memory")
#define LBAR() asm volatile("s_waitcnt lgkmcnt(0)\n\ts_barrier" ::: "memory")
#define SB() __builtin_amdgcn_sched_barrier(0)
#define PIN(x) asm volatile("" : "+v"(x))
DI float sum16(float v) { const unsigned u = __builtin_bit_cast(unsigned, v); const auto r = __builtin_amdgcn_permlane16_swap(u, u, false, false);
    return __builtin_bit_cast(float, (unsigned)r[0]) + __builtin_bit_cast(float, (unsigned)r[1]); }
DI float sum32(float v) { const unsigned u = __builtin_bit_cast(unsigned, v); const auto r = __builtin_amdgcn_permlane32_swap(u, u, false, false);
    return __builtin_bit_cast(float, (unsigned)r[0]) + __builtin_bit_cast(float, (unsigned)r[1]); }
DI float max32(float v) { const unsigned u = __builtin_bit_cast(unsigned, v); const auto r = __builtin_amdgcn_permlane32_swap(u, u, false, false);
    return __builtin_fmaxf(__builtin_bit_cast(float, (unsigned)r[0]), __builtin_bit_cast(float, (unsigned)r[1])); }
DI float lo32(float v) { const unsigned u = __builtin_bit_cast(unsigned, v); const auto r = __builtin_amdgcn_permlane32_swap(u, u, false, false);
    return __builtin_bit_cast(float, (unsigned)r[0]); }
typedef float f32x2 __attribute__((ext_vector_type(2)));
DI void ln_gelu4(f32x4 acc, f32x4 cs, float b0, float b1, float b2, float b3, float nmean, float rs, unsigned& o01, unsigned& o23) {
    float z0, z1, z2, z3, t0, t1, t2, t3;
    asm("v_fma_f32 %0, %16, %12, %8\n\tv_fma_f32 %1, %16, %13, %9\n\tv_fma_f32 %2, %16, %14, %10\n\tv_fma_f32 %3, %16, %15, %11\n\t"
        "v_fma_f32 %0, %17, %0, %18\n\tv_fma_f32 %1, %17, %1, %19\n\tv_fma_f32 %2, %17, %2, %20\n\tv_fma_f32 %3, %17, %3, %21\n\t"
        "v_mul_f32 %4, %0, %0\n\tv_mul_f32 %5, %1, %1\n\tv_mul_f32 %6, %2, %2\n\tv_mul_f32 %7, %3, %3\n\t"
        "v_fmaak_f32 %4, %22, %4, 0xc0135761\n\tv_fmaak_f32 %5, %22, %5, 0xc0135761\n\tv_fmaak_f32 %6, %22, %6, 0xc0135761\n\tv_fmaak_f32 %7, %22, %7, 0xc0135761\n\t"
        "v_mul_f32 %4, %0, %4\n\tv_mul_f32 %5, %1, %5\n\tv_mul_f32 %6, %2, %6\n\tv_mul_f32 %7, %3, %7\n\t"
        "v_exp_f32 %4, %4\n\tv_exp_f32 %5, %5\n\tv_exp_f32 %6, %6\n\tv_exp_f32 %7, %7\n\t"
        "v_add_f32 %4, 1.0, %4\n\tv_add_f32 %5, 1.0, %5\n\tv_add_f32 %6, 1.0, %6\n\tv_add_f32 %7, 1.0, %7\n\t"
        "v_rcp_f32 %4, %4\n\tv_rcp_f32 %5, %5\n\tv_rcp_f32 %6, %6\n\tv_rcp_f32 %7, %7\n\t"
        "v_mul_f32 %0, %0, %4\n\tv_mul_f32 %1, %1, %5\n\tv_mul_f32 %2, %2, %6\n\tv_mul_f32 %3, %3, %7"
        : "=&v"(z0), "=&v"(z1), "=&v"(z2), "=&v"(z3), "=&v"(t0), "=&v"(t1), "=&v"(t2), "=&v"(t3)
        : "v"(acc[0]), "v"(acc[1]), "v"(acc[2]), "v"(acc[3]), "v"(cs[0]), "v"(cs[1]), "v"(cs[2]), "v"(cs[3]), "v"(nmean), "v"(rs), "v"(b0), "v"(b1), "v"(b2), "v"(b3),
          "v"(-0.1029432397f));
    o01 = pk2(z0, z1); o23 = pk2(z2, z3);
}
#define STAMP(ws, i) do { } while (0)
#define FINE 0
#define TS(n) do { } while (0)
#define TSW(n) do { } while (0)
#define TSDECL
#define TSDUMP(ws, kidx) do { } while (0)

struct Unit { rsrc_t rs; int soff, ld, kind; int dst[4]; int cs; };
constexpr int NUNIT = 1296 + 1296 + 162;
struct UnitTab { int v[NUNIT + 1][8]; };
constexpr UnitTab make_units() {
    UnitTab T{};
    for (int u = 0; u <= NUNIT; ++u) {
        const bool iswo = u < 162, isw1 = !iswo && u < 1458, isw2 = u >= 1458 && u < NUNIT;
        const int v = iswo ? u : isw1 ? u - 162 : u - 1458;
        const int j = isw1 ? (v % 72) / 9 : v / 162, ks = isw1 ? v / 72 : (v % 162) % 18, G = isw1 ? v % 9 : (v % 162) / 18;
        T.v[u][0] = isw1 ? (G < 8 ? 0 : 4) : isw2 ? (ks < 16 ? 1 : 5) : iswo ? 2 : 3;
        T.v[u][1] = isw1 ? (32 * ks * FF + 576 * j + 64 * G) * 2 : ((isw2 ? 576 * j : 0) + 32 * ks) * (D * 2) + 128 * G;
        const int base = (int)OFF_WS + j * (int)WS_J, gi = G < j ? G : G - 1;
        for (int c = 0; c < 4; ++c) {
            int d = 0x7fff0000;
            if (isw1 && G < 8) { const bool A = !(G & 1); d = base + ffn_stream_base((G >> 1) + (A ? 0 : 4)) + ffn_pos32(A, c >> 1, 2 * ks + (c & 1)) * 1024; }
            else if (isw1) d = base + ffn_stream_base(c) + (72 + ks) * 1024;
            else if (isw2) { const bool A = G == j || gi < 4; d = base + ffn_stream_base(A ? c : 4 + c) + ffn_pos2(A, G == j ? 4 : gi & 3, ks) * 1024; }
            else if (iswo) d = (int)OFF_WO + ((4 * G + c) * 18 + ks) * 1024;
            T.v[u][2 + c] = d;
        }
        T.v[u][6] = isw1 ? (int)OFF_CSP + (ks * FF + 576 * j + 64 * G) * 4 : 0x7fff0000;
        T.v[u][7] = 0;
    }
    return T;
}
__device__ const UnitTab UNITS = make_units();
DI Unit unit_decode(const void* w1, const void* w2, const void* wo, int u) {
    const int* e = UNITS.v[u < NUNIT ? u : NUNIT];
    const int kind = e[0];
    Unit r;
    const bool w1k = kind == 0 || kind == 4, w2k = kind == 1 || kind == 5;
    r.rs = mkrsrc(w1k ? w1 : w2k ? w2 : wo, kind == 3 ? 0u : kind == 2 ? D * D * 2 : D * FF * 2);
    r.ld = w1k ? FF * 2 : D * 2; r.kind = kind; r.soff = e[1];
#pragma unroll
    for (int c = 0; c < 4; ++c) r.dst[c] = e[2 + c];
    r.cs = e[6];
    return r;
}
#define UST 16
DI void unit_load(const Unit& t, unsigned scr, int lane) {
    const int voff = (lane >> 3) * t.ld + (lane & 7) * 16;
#pragma unroll
    for (int m = 0; m < 4; ++m) dma16(t.rs, voff, t.soff + 8 * m * t.ld, scr + m * 1024);
}
DI void unit_store(const Unit& t, const LAS char* scr, rsrc_t rws, int lane) {
    const int g = lane >> 4, q = (lane & 15) >> 2, p = lane & 3;
    float mine;
    if (t.kind == 0) {
        const int o = (8 * (lane >> 5) + q) * 128 + (g & 1) * 32 + 8 * p;
        float cs[2] = {0.f, 0.f};
#pragma unroll
        for (int c = 0; c < 4; ++c) {
            const v2u lo = ldstr(scr + o + (c & 1) * 2048 + (c >> 1) * 64), hi = ldstr(scr + o + (c & 1) * 2048 + (c >> 1) * 64 + 512);
            bst128<UST>(v4u{lo.x, lo.y, hi.x, hi.y}, rws, lane * 16, t.dst[c]);
            cs[c >> 1] = dot2(hi.y, dot2(hi.x, dot2(lo.y, dot2(lo.x, cs[c >> 1]))));
        }
        const float s0 = sum32(cs[0]), s1 = sum32(cs[1]);
        mine = lane < 32 ? s0 : s1;
    } else {
        const int row0 = t.kind == 1 ? 16 * (g >> 1) + 4 * (g & 1) + q : t.kind == 5 ? 4 * g + q : 8 * g + q, rstep = t.kind == 1 ? 8 : t.kind == 5 ? 16 : 4;
        float cs[4];
#pragma unroll
        for (int c = 0; c < 4; ++c) {
            const v2u lo = ldstr(scr + row0 * 128 + (16 * c + 4 * p) * 2);
            const v2u hi = ldstr(scr + (row0 + rstep) * 128 + (16 * c + 4 * p) * 2);
            bst128<UST>(v4u{lo.x, lo.y, hi.x, hi.y}, rws, lane * 16, t.dst[c]);
            cs[c] = sum32(sum16(dot2(hi.y, dot2(hi.x, dot2(lo.y, dot2(lo.x, 0.f))))));
        }
        mine = g == 0 ? cs[0] : g == 1 ? cs[1] : g == 2 ? cs[2] : cs[3];
    }
    __builtin_amdgcn_raw_buffer_store_b32(__builtin_bit_cast(unsigned, mine), rws, lane * 4, t.cs, 16);
}

#define QST 0
struct K1Args { const void* x; const void* wqkv; const void* wo; const void* w1; const void* w2; char* ws; };
constexpr int K1_STAGE = 32768;
constexpr int K1_TAB = 4 * K1_STAGE;
constexpr int K1_LDS = K1_TAB + 4096;

__global__ __launch_bounds__(512, 2) void k1_qkv(K1Args a) {
    extern __shared__ __attribute__((aligned(16))) char smem_[];
    LAS char* smem = (LAS char*)smem_;
    const unsigned lds0 = (unsigned)(size_t)smem_;
    const int tid = threadIdx.x, lane = tid & 63, w = __builtin_amdgcn_readfirstlane(tid >> 6), b = blockIdx.x;
    STAMP(a.ws, 0);
    TSDECL; TS(0);
    if (b == 0 && tid < FL_N) *(GAS unsigned*)(a.ws + OFF_FLAG + tid * 256) = 0u;
    if (b >= 224) {
        const rsrc_t rws = mkrsrc(a.ws, (unsigned)WS_END);
#pragma unroll
        for (int rd = 0; rd < 2; ++rd) {
            const int u0 = rd * 768 + ((b - 224) * 8 + w) * 3;
            Unit t[3];
#pragma unroll
            for (int m = 0; m < 3; ++m) { t[m] = unit_decode(a.w1, a.w2, a.wo, u0 + m < 1458 ? u0 + m : NUNIT); unit_load(t[m], lds0 + w * 16384 + m * 4096, lane); }
            WAITV(8); unit_store(t[0], smem + w * 16384, rws, lane);
            WAITV(9); unit_store(t[1], smem + w * 16384 + 4096, rws, lane);
            WAITV(10); unit_store(t[2], smem + w * 16384 + 8192, rws, lane);
            asm volatile("s_waitcnt lgkmcnt(0)" ::: "memory");
        }
        STAMP(a.ws, 2);
        return;
    }
    const int xc = b & 7, idx = b >> 3;
    const int rt = 4 * (xc >> 1) + (idx & 3), ct = 7 * (xc & 1) + (idx >> 2);
    if (w >= 4) {
        const int lw = w - 4;
        const rsrc_t rx = mkrsrc(a.x, S * D * 2), rw = mkrsrc(a.wqkv, D * N3 * 2);
        const int vx0 = (128 * rt + 32 * lw + (lane >> 3)) * (D * 2) + ((lane & 7) ^ (lane >> 4)) * 16, vx1 = vx0 ^ 64;
        const int c16 = lane & 15, q = lane >> 4;
        const int vw0 = (16 * lw + q) * (N3 * 2) + 256 * ct + ((((c16 >> 1) ^ (q << 1)) << 1) | (c16 & 1)) * 16;
        const int vw1 = (16 * lw + q) * (N3 * 2) + 256 * ct + ((((c16 >> 1) ^ ((q << 1) | 1)) << 1) | (c16 & 1)) * 16;
        auto stage = [&](int s) {
            const unsigned sb = lds0 + (s & 3) * K1_STAGE;
#pragma unroll
            for (int m = 0; m < 4; ++m) {
                dma16(rw, m < 2 ? vw0 : vw1, (64 * s + 4 * m) * (N3 * 2), sb + 16384 + (4 * lw + m) * 1024);
                dma16(rx, (m & 1) ? vx1 : vx0, 128 * s + m * 8 * (D * 2), sb + (4 * lw + m) * 1024);
            }
        };
        const int g = lane >> 4, i = lane & 15, q4 = i >> 2, p = lane & 3;
        const int sxo = (32 * lw + i) * 128, swo = 16384 + (8 * g + q4) * 256 + 8 * p, key = (q4 << 1) | (g & 1);
        const v4u ones = {0x3f803f80u, 0x3f803f80u, 0x3f803f80u, 0x3f803f80u};
        f32x4 sx[2], sq[2], cs[2];
#pragma unroll
        for (int u = 0; u < 2; ++u) sx[u] = sq[u] = cs[u] = f32x4{0.f, 0.f, 0.f, 0.f};
        auto stats = [&](int s) {
            const LAS char* sb = smem + (s & 3) * K1_STAGE;
#pragma unroll
            for (int k2 = 0; k2 < 2; ++k2)
#pragma unroll
                for (int u = 0; u < 2; ++u) {
                    const v4u xv = lds128(sb + sxo + u * 2048 + ((4 * k2 + g) ^ ((i >> 1) & 7)) * 16);
                    const LAS char* ap = sb + swo + k2 * 8192 + (((2 * lw + u) ^ key) * 32);
                    const v2u lo = ldstr(ap), hi = ldstr(ap + 1024);
                    sx[u] = mfma16(ones, xv, sx[u]); sq[u] = mfma16(xv, xv, sq[u]); cs[u] = mfma16(v4u{lo.x, lo.y, hi.x, hi.y}, ones, cs[u]);
                }
        };
        stage(0); stage(1); stage(2);
        WAITV(16); TS(1); BAR();
        WAITV(8); BAR();
        stats(0);
#pragma unroll
        for (int s = 3; s < 9; ++s) { stage(s); stats(s - 2); WAITV(8); asm volatile("s_waitcnt lgkmcnt(0)" ::: "memory"); BAR(); }
        stats(7); WAITV(0); BAR();
        stats(8);
        LAS float* tabx = (LAS float*)(smem + K1_TAB);
        LAS float* tabc = tabx + 256;
#pragma unroll
        for (int u = 0; u < 2; ++u) {
            const float s2 = (i & 2) ? ((i & 1) ? sq[u][3] : sq[u][2]) : ((i & 1) ? sq[u][1] : sq[u][0]);
            const float mean = sx[u][0] * (1.0f / D), var = __builtin_fmaxf(__builtin_fmaf(-mean, mean, s2 * (1.0f / D)), 0.f);
            if (g == q4) *(LAS f32x2*)(tabx + (32 * lw + 16 * u + i) * 2) = f32x2{mean, __builtin_amdgcn_rsqf(var + LN_EPS)};
            if (i == 0) *(LAS f32x4*)(tabc + 32 * lw + 16 * u + 4 * g) = cs[u]; }
        LBAR();
        TS(9);
        TSDUMP(a.ws, 0);
        return;
    }
    const int th = w & 1, ch = w >> 1, h = lane >> 5, l31 = lane & 31;
    const int q = (lane & 15) >> 2, p = lane & 3, blk = (lane >> 4) & 1;
    const int xrow = (64 * th + l31) * 128, xkey = (l31 >> 1) & 7;
    const int woff = 16384 + (8 * h + q) * 256 + (((4 * ch + blk) ^ ((q << 1) | h)) * 32) + 8 * p;
    f32x16 acc[2][2];
#pragma unroll
    for (int cx = 0; cx < 2; ++cx)
#pragma unroll
        for (int ty = 0; ty < 2; ++ty)
#pragma unroll
            for (int e = 0; e < 16; ++e) acc[cx][ty][e] = 0.f;
    int xo[4];
#pragma unroll
    for (int k4 = 0; k4 < 4; ++k4) xo[k4] = xrow + ((2 * k4 + h) ^ xkey) * 16;
    v4u bA[2][2], aA[2][2], bB[2][2], aB[2][2];
    auto rd = [&](v4u (&bf)[2][2], v4u (&af)[2][2], const LAS char* sb, int k2) {
#pragma unroll
        for (int u = 0; u < 2; ++u) {
#pragma unroll
            for (int cx = 0; cx < 2; ++cx) { const LAS char* ap = sb + ((woff ^ (cx << 6)) + (2 * k2 + u) * 4096);
                const v2u lo = ldstr(ap), hi = ldstr(ap + 1024); af[u][cx] = v4u{lo.x, lo.y, hi.x, hi.y}; }
#pragma unroll
            for (int ty = 0; ty < 2; ++ty) bf[u][ty] = lds128(sb + xo[2 * k2 + u] + ty * 4096);
        }
    };
    auto mm = [&](const v4u (&bf)[2][2], const v4u (&af)[2][2]) {
#pragma unroll
        for (int u = 0; u < 2; ++u)
#pragma unroll
            for (int cx = 0; cx < 2; ++cx)
#pragma unroll
                for (int ty = 0; ty < 2; ++ty) acc[cx][ty] = mfma32(af[u][cx], bf[u][ty], acc[cx][ty]);
    };
    int dst[2][2];
#pragma unroll
    for (int cx = 0; cx < 2; ++cx)
#pragma unroll
        for (int m = 0; m < 2; ++m) { const int n8 = 128 * ct + 64 * ch + 32 * cx + 16 * m + 8 * h, ja = n8 / 72;
            dst[cx][m] = n8 < N3 ? (int)OFF_QKV + ja * (int)QKV_G + (128 * rt + 64 * th + l31) * 144 + (n8 - 72 * ja) * 2 : 0x7fff0000; }
    auto half = [&](v4u (&bn)[2][2], v4u (&an)[2][2], const LAS char* nb, int k2, const v4u (&bc)[2][2], const v4u (&ac)[2][2]) {
        SB();
        rd(bn, an, nb, k2);
        mm(bc, ac);
#pragma unroll
        for (int e = 0; e < 4; ++e) { __builtin_amdgcn_sched_group_barrier(0x008, 1, 0); __builtin_amdgcn_sched_group_barrier(0x100, 2, 0); }
#pragma unroll
        for (int e = 0; e < 4; ++e) { __builtin_amdgcn_sched_group_barrier(0x008, 1, 0); __builtin_amdgcn_sched_group_barrier(0x100, 1, 0); }
        SB();
    };
#define K1_SYNC() asm volatile("s_waitcnt lgkmcnt(12)\n\ts_barrier" ::: "memory")
    BAR();
    TS(1);
    unsigned pf[2];
#pragma unroll
    for (int m = 0; m < 2; ++m) {
        const int lx = (idx >> 2) * 55 + tid, lw2 = (idx & 3) * 96 + (tid - 55), wo = (192 + 192 * m + (lw2 >> 1)) * (N3 * 2) + 256 * ct + (lw2 & 1) * 128;
        const char* p = tid < 55 ? (const char*)a.x + (size_t)(128 * rt + (lx < 384 ? lx / 3 : 0)) * (D * 2) + (3 + 3 * m + lx % 3) * 128
                                 : (const char*)a.wqkv + ((tid < 151 && wo < D * N3 * 2) ? wo : 0);
        asm volatile("global_load_dword %0, %1, off" : "=v"(pf[m]) : "v"(p));
    }
    __builtin_amdgcn_s_waitcnt(0xC07F);
    rd(bA, aA, smem, 0);
    half(bB, aB, smem, 1, bA, aA);
    BAR();
    half(bA, aA, smem + K1_STAGE, 0, bB, aB);
    K1_SYNC();
#pragma nounroll
    for (int s = 1; s < 7; ++s) {
        half(bB, aB, smem + (s & 3) * K1_STAGE, 1, bA, aA);
        half(bA, aA, smem + ((s + 1) & 3) * K1_STAGE, 0, bB, aB);
        K1_SYNC();
    }
    half(bB, aB, smem + 3 * K1_STAGE, 1, bA, aA);
    half(bA, aA, smem, 0, bB, aB);
    half(bB, aB, smem, 1, bA, aA);
    SB(); mm(bB, aB); SB();
    TS(4);
    asm volatile("s_waitcnt vmcnt(0)" :: "v"(pf[0]), "v"(pf[1]));
    LBAR();
    const LAS float* tabc = (const LAS float*)(smem + K1_TAB) + 256;
    const LAS float* tabx = (const LAS float*)(smem + K1_TAB);
    TS(5);
    const rsrc_t rws = mkrsrc(a.ws, (unsigned)OFF_WS);
#pragma unroll
    for (int ty = 0; ty < 2; ++ty) {
        const int t = 64 * th + 32 * ty + l31;
        const float mean = tabx[t * 2], rstd = tabx[t * 2 + 1];
#pragma unroll
        for (int cx = 0; cx < 2; ++cx) {
            const float rs = rstd * (128 * ct + 64 * ch + 32 * cx < D ? QSCALE : 1.0f);
            unsigned pk[8];
#pragma unroll
            for (int qd = 0; qd < 4; ++qd) { const f32x4 c = *(const LAS f32x4*)(tabc + 64 * ch + 32 * cx + 8 * qd + 4 * h);
                pk[2 * qd] = pk2(rs * __builtin_fmaf(-mean, c[0], acc[cx][ty][4 * qd]), rs * __builtin_fmaf(-mean, c[1], acc[cx][ty][4 * qd + 1]));
                pk[2 * qd + 1] = pk2(rs * __builtin_fmaf(-mean, c[2], acc[cx][ty][4 * qd + 2]), rs * __builtin_fmaf(-mean, c[3], acc[cx][ty][4 * qd + 3])); }
#pragma unroll
            for (int m = 0; m < 2; ++m) {
                const auto s0 = __builtin_amdgcn_permlane32_swap(pk[4 * m], pk[4 * m + 2], false, false), s1 = __builtin_amdgcn_permlane32_swap(pk[4 * m + 1], pk[4 * m + 3], false, false);
                bst128<QST>(v4u{(unsigned)s0[0], (unsigned)s1[0], (unsigned)s0[1], (unsigned)s1[1]}, rws, dst[cx][m], ty * 32 * 144); }
        }
    }
    STAMP(a.ws, 2);
    TS(6); TSW(7);
    TSDUMP(a.ws, 0);
}

struct K2Args { char* ws; const void* x; const void* wo; const void* w1; const void* w2; const void* b1; const void* b2; void* out; };
constexpr int KV_HEAD = 320 * 40;
constexpr int K2_K = 0, K2_V = 4 * KV_HEAD, K2_O = 8 * KV_HEAD, K2_SCR = K2_O + 64 * 144;
constexpr int K4_X = 0, K4_H = 73728, K4_ST = 147456, K4_CS = K4_ST + 512, K4_B1 = K4_CS + 2304, K4_CNT = K4_B1 + 2304, K4_OWN = K4_CNT + 256, K2_LDS = K4_OWN + 8192;
static_assert(K2_SCR + 8 * 4096 <= K4_ST, "LDS map");

DI void flag_add(char* ws, int f) { __hip_atomic_fetch_add((gu32*)(ws + OFF_FLAG + f * 256), 1u, __ATOMIC_RELAXED, __HIP_MEMORY_SCOPE_AGENT); }
DI void flag_wait(char* ws, int f, unsigned n) {
    gu32* p = (gu32*)(ws + OFF_FLAG + f * 256);
    for (unsigned spins = 0; __hip_atomic_load(p, __ATOMIC_RELAXED, __HIP_MEMORY_SCOPE_AGENT) < n && spins < (1u << 20); ++spins) __builtin_amdgcn_s_sleep(1);
    __builtin_amdgcn_fence(__ATOMIC_ACQUIRE, "wavefront");
}
DI void flag_wait_all(char* ws, int f, unsigned n, int lane) {
    gu32* p = (gu32*)(ws + OFF_FLAG + (f + (lane & 31)) * 256);
    for (unsigned spins = 0; !__all(__hip_atomic_load(p, __ATOMIC_RELAXED, __HIP_MEMORY_SCOPE_AGENT) >= n) && spins < (1u << 20); ++spins) __builtin_amdgcn_s_sleep(1);
    __builtin_amdgcn_fence(__ATOMIC_ACQUIRE, "wavefront");
}
DI void tile_in(const char* src, LAS char* smem, int tid) {
    const rsrc_t rs = mkrsrc(src, 4 * (unsigned)TT_FR);
    v4u t[9];
#pragma unroll
    for (int m = 0; m < 9; ++m) t[m] = bld128_sc1(rs, tid * 16, m * 8192);
#pragma unroll
    for (int m = 0; m < 9; ++m) *(LAS v4u*)(smem + tid * 16 + m * 8192) = t[m];
}

template <bool EDGE> DI void attn_core(LAS char* smem, const v4u q0, const v4u q1, int r, int w, int lane, f32x16& oacc, const Unit& ua, rsrc_t rws) {
    const int hl = w & 3, qh = w >> 2, l31 = lane & 31, hh = lane >> 5;
    const int tmin = EDGE ? ((256 - 64 * r - 32 * qh) >> 5) : 0;
    const float ninf = -__builtin_inff();
    const LAS char* kp = smem + K2_K + (hl * 320 + 32 * qh + l31) * 40;
    v4u ka[9], kb[9];
#pragma unroll
    for (int t = 0; t < 9; ++t) { const v2u a0 = lds64(kp + t * 1280 + 16 * hh), a1 = lds64(kp + t * 1280 + 16 * hh + 8);
        ka[t] = v4u{a0.x, a0.y, a1.x, a1.y};
        const v2u c0 = lds64(kp + t * 1280 + 32), c1 = lds64(kp + t * 1280 + 24);
        kb[t] = v4u{c0.x, c0.y, c1.x, c1.y}; }
    auto score = [&](int t, const f32x16& c) { return mfma32(kb[t], q1, mfma32(ka[t], q0, c)); };
    bool le[16];
#pragma unroll
    for (int rg = 0; rg < 16; ++rg) le[rg] = (rg & 3) + 8 * (rg >> 2) + 4 * hh <= l31;
    auto merged = [&](const f32x16& c) { const f32x16 s8 = score(8, c); f32x16 s0;
        if (EDGE) { s0 = s8;
#pragma unroll
            for (int rg = 0; rg < 16; ++rg) s0[rg] = ninf; } else s0 = score(0, c);
        f32x16 m;
#pragma unroll
        for (int rg = 0; rg < 16; ++rg) m[rg] = le[rg] ? s8[rg] : s0[rg];
        return m; };
    const f32x16 zero = {};
    float mx;
    { const f32x16 m = merged(zero); mx = __builtin_fmaxf(m[0], m[1]);
#pragma unroll
      for (int rg = 2; rg < 16; rg += 2) mx = __builtin_fmaxf(__builtin_fmaxf(mx, m[rg]), m[rg + 1]); }
#pragma unroll
    for (int t = 1; t < 8; ++t) if (!EDGE || t >= tmin) { const f32x16 s = score(t, zero);
#pragma unroll
        for (int rg = 0; rg < 16; rg += 2) mx = __builtin_fmaxf(__builtin_fmaxf(mx, s[rg]), s[rg + 1]); }
    mx = max32(mx);
    WAITV(0); unit_store(ua, smem + K2_SCR + w * 4096, rws, lane);
    f32x16 cm;
#pragma unroll
    for (int rg = 0; rg < 16; ++rg) cm[rg] = -mx;
    const LAS char* vp = smem + K2_V + (hl * 320 + 32 * qh + 4 * hh + ((lane & 15) >> 2)) * 40 + (((lane >> 4) & 1) ? 32 : 8 * (lane & 3));
    auto pv = [&](int t, const unsigned (&pw)[8]) {
#pragma unroll
        for (int s = 0; s < 2; ++s) {
            const v2u lo = ldstr(vp + (t * 32 + 16 * s) * 40), hi = ldstr(vp + (t * 32 + 16 * s + 8) * 40);
            oacc = mfma32(v4u{lo.x, lo.y, hi.x, hi.y}, v4u{pw[4 * s], pw[4 * s + 1], pw[4 * s + 2], pw[4 * s + 3]}, oacc);
        } };
    oacc = zero;
    { const f32x16 m = merged(cm); unsigned pw[8], p8[8], p0[8];
#pragma unroll
      for (int e = 0; e < 8; ++e) { pw[e] = pk2(__builtin_amdgcn_exp2f(m[2 * e]), __builtin_amdgcn_exp2f(m[2 * e + 1]));
          const unsigned mk = (le[2 * e] ? 0xffffu : 0u) | (le[2 * e + 1] ? 0xffff0000u : 0u); p8[e] = pw[e] & mk; p0[e] = pw[e] & ~mk; }
      pv(8, p8); if (!EDGE) pv(0, p0); }
#pragma unroll
    for (int t = 1; t < 8; ++t) if (!EDGE || t >= tmin) { const f32x16 s = score(t, cm); unsigned pw[8];
#pragma unroll
        for (int e = 0; e < 8; ++e) pw[e] = pk2(__builtin_amdgcn_exp2f(s[2 * e]), __builtin_amdgcn_exp2f(s[2 * e + 1]));
        pv(t, pw); }
}

#define PRA 2
#define PRB 1
#define PRV 0
#define PRG 1
#define RD 24

template <int T, int OFS, int PRIO>
DI void ffn_pass32(f32x16 (&acc)[2][2], v4u (&ring)[RD], rsrc_t rw, int pos, const LAS char* bp, int lane) {
    __builtin_amdgcn_s_setprio(PRIO);
    v4u bf[2];
#pragma unroll
    for (int ty = 0; ty < 2; ++ty) bf[ty] = lds128(bp + ty * 36864);
#pragma unroll
    for (int t = 0; t < T; ++t)
#pragma unroll
        for (int ty = 0; ty < 2; ++ty)
#pragma unroll
            for (int e = 0; e < 16; ++e) acc[t][ty][e] = 0.f;
#pragma unroll
    for (int u = 0; u < 36; ++u) {
        SB();
        v4u bn[2];
        if (u + 1 < 36) {
#pragma unroll
            for (int ty = 0; ty < 2; ++ty) bn[ty] = lds128(bp + ty * 36864 + (u + 1) * 512);
        }
#pragma unroll
        for (int t = 0; t < T; ++t) {
            const int n = u * T + t, sl = (n + OFS) % RD;
#pragma unroll
            for (int ty = 0; ty < 2; ++ty) acc[t][ty] = mfma32(ring[sl], bf[ty], acc[t][ty]);
            ring[sl] = bld128_sc1(rw, lane * 16 + (n & 3) * 1024, pos + (n >> 2) * 4096);
        }
#pragma unroll
        for (int t = 0; t < T; ++t) {
            if (T == 1) {
#pragma unroll
                for (int e = 0; e < 2; ++e) { __builtin_amdgcn_sched_group_barrier(0x008, 1, 0); __builtin_amdgcn_sched_group_barrier(0x100, 1, 0); }
            } else { __builtin_amdgcn_sched_group_barrier(0x008, 1, 0); __builtin_amdgcn_sched_group_barrier(0x100, 1, 0); __builtin_amdgcn_sched_group_barrier(0x008, 1, 0); }
            __builtin_amdgcn_sched_group_barrier(0x020, 1, 0);
        }
        SB();
        if (u + 1 < 36) {
#pragma unroll
            for (int ty = 0; ty < 2; ++ty) bf[ty] = bn[ty];
        }
    }
#pragma unroll
    for (int t = 0; t < T; ++t)
#pragma unroll
        for (int ty = 0; ty < 2; ++ty) PIN(acc[t][ty]);
    asm volatile("s_nop 11" ::: "memory");
    __builtin_amdgcn_s_setprio(PRV);
}
template <int T, int OFS, class Side>
DI void ffn_pass(f32x4 (&acc)[2][4], v4u (&ring)[RD], rsrc_t rw, int pos, const LAS char* bp, int lane, Side side) {
    v4u bf[4];
#pragma unroll
    for (int tt = 0; tt < 4; ++tt) bf[tt] = lds128(bp + tt * 18432);
#pragma unroll
    for (int t = 0; t < T; ++t)
#pragma unroll
        for (int tt = 0; tt < 4; ++tt) acc[t][tt] = f32x4{0.f, 0.f, 0.f, 0.f};
#pragma unroll
    for (int u = 0; u < 18; ++u) {
        SB();
        v4u bn[4];
        if (u + 1 < 18) {
#pragma unroll
            for (int tt = 0; tt < 4; ++tt) bn[tt] = lds128(bp + tt * 18432 + (u + 1) * 1024);
        }
#pragma unroll
        for (int t = 0; t < T; ++t) {
            const int n = u * T + t, sl = (n + OFS) % RD;
#pragma unroll
            for (int tt = 0; tt < 4; ++tt) acc[t][tt] = mfma16(ring[sl], bf[tt], acc[t][tt]);
            ring[sl] = bld128_sc1(rw, lane * 16 + (n & 3) * 1024, pos + (n >> 2) * 4096);
        }
        side(u);
#pragma unroll
        for (int t = 0; t < T; ++t) {
            if (T == 1) {
#pragma unroll
                for (int e = 0; e < 4; ++e) { __builtin_amdgcn_sched_group_barrier(0x008, 1, 0); __builtin_amdgcn_sched_group_barrier(0x100, 1, 0); }
            } else if (T == 2) {
#pragma unroll
                for (int e = 0; e < 2; ++e) { __builtin_amdgcn_sched_group_barrier(0x008, 2, 0); __builtin_amdgcn_sched_group_barrier(0x100, 1, 0); }
            } else { __builtin_amdgcn_sched_group_barrier(0x008, 4, 0); if (t < 4) __builtin_amdgcn_sched_group_barrier(0x100, 1, 0); }
            __builtin_amdgcn_sched_group_barrier(0x020, 1, 0);
        }
        SB();
        if (u + 1 < 18) {
#pragma unroll
            for (int tt = 0; tt < 4; ++tt) bf[tt] = bn[tt];
        }
    }
#pragma unroll
    for (int t = 0; t < T; ++t)
#pragma unroll
        for (int tt = 0; tt < 4; ++tt) PIN(acc[t][tt]);
    asm volatile("s_nop 7" ::: "memory");
}

template <bool A> DI void ffn_body(const K2Args& a, LAS char* smem, int r, int j, int w, int lane
) {
    constexpr int NT = A ? 5 : 4;
    const int tid = threadIdx.x, g = lane >> 4, i = lane & 15, s = w & 3;
    char* ws = a.ws;
    const rsrc_t rws = mkrsrc(ws, (unsigned)WS_END);
    v4u ring[RD];
    {
        if (tid == 448) { flag_add(ws, FL_O + r); flag_wait(ws, FL_O + r, 8); }
        const int dt = A ? 4 * j + w : 32 + (j >> 1), tt0 = A ? 0 : 2 * (j & 1);
        const rsrc_t rw = mkrsrc(ws + OFF_WO, 36 * 18 * 1024), rx = mkrsrc(a.x, S * D * 2);
        v4u af[18]; v2u xr[4];
        if (A || w == 4) {
#pragma unroll
            for (int ks = 0; ks < 18; ++ks) af[ks] = bld128(rw, lane * 16 + (ks & 3) * 1024, dt * 18432 + (ks >> 2) * 4096);
#pragma unroll
            for (int u = 0; u < 4; ++u) xr[u] = bld64(rx, (64 * r + 16 * ((tt0 + u) & 3) + i) * (D * 2) + (16 * dt + 4 * g) * 2, 0);
        }
        BAR();
        TS(7);
        STAMP(ws, 6);
        tile_in(ws + OFF_O + 4 * r * TT_FR, smem, tid);
        LBAR();
        TS(8);
        const rsrc_t rf = mkrsrc(ws + OFF_WS + j * WS_J + ffn_stream_base(w), (A ? 180 : 144) * 1024);
        auto fill = [&](int n) { ring[n] = bld128_sc1(rf, lane * 16 + (n & 3) * 1024, (n >> 2) * 4096); };
        if (!(A || w == 4)) {
#pragma unroll
            for (int n = 0; n < RD / 2; ++n) fill(n);
            if (w == 7) flag_wait_all(ws, FL_O, 8, lane);
        }
        if (A || w == 4) {
            f32x4 acc[4];
#pragma unroll
            for (int u = 0; u < 4; ++u) acc[u] = f32x4{0.f, 0.f, 0.f, 0.f};
            const LAS char* bp = smem + lane * 16;
            int to[4];
#pragma unroll
            for (int u = 0; u < 4; ++u) to[u] = ((tt0 + u) & 3) * 18432;
            v4u bf[2][4];
#pragma unroll
            for (int u = 0; u < 4; ++u) bf[0][u] = lds128(bp + to[u]);
#pragma unroll
            for (int ks = 0; ks < 18; ++ks) {
                SB();
                if (ks < 17) {
#pragma unroll
                    for (int u = 0; u < 4; ++u) bf[(ks + 1) & 1][u] = lds128(bp + (ks + 1) * 1024 + to[u]); }
#pragma unroll
                for (int u = 0; u < 4; ++u) acc[u] = mfma16(af[ks], bf[ks & 1][u], acc[u]);
                if (ks < RD / 2) fill(ks);
#pragma unroll
                for (int u = 0; u < 4; ++u) { __builtin_amdgcn_sched_group_barrier(0x008, 1, 0); __builtin_amdgcn_sched_group_barrier(0x100, 1, 0); }
                SB();
            }
#pragma unroll
            for (int u = 0; u < 4; ++u) if (A || u < 2) { const int tt = (tt0 + u) & 3;
                const v2u o = {pk2(acc[u][0] + blo(xr[u].x), acc[u][1] + bhi(xr[u].x)), pk2(acc[u][2] + blo(xr[u].y), acc[u][3] + bhi(xr[u].y))};
                __builtin_amdgcn_raw_buffer_store_b64(o, rws, (int)OFF_H + (4 * r + tt) * (int)TT_FR + (dt >> 1) * 1024 + ((2 * (dt & 1) + (g >> 1)) * 16 + i) * 16 + (g & 1) * 8, 0, 16); }
        }
        TS(9);
        SB();
        if (w != 7) {
#pragma unroll
            for (int n = RD / 2; n < RD; ++n) fill(n); }
        SB();
        asm volatile("s_waitcnt vmcnt(%0) lgkmcnt(0)\n\ts_barrier" :: "n"(RD - RD / 2) : "memory");
        STAMP(ws, 7);
        TS(10);
        if (w == 7) {
            if (lane == 0) { flag_add(ws, FL_H + r); flag_wait(ws, FL_H + r, 8); }
            SB();
#pragma unroll
            for (int n = RD / 2; n < RD; ++n) fill(n); }
    }
    const rsrc_t rw = mkrsrc(ws + OFF_WS + j * WS_J + ffn_stream_base(w), (A ? 180 : 144) * 1024);
    BAR();
    TS(14);
    STAMP(ws, 8);
    tile_in(ws + OFF_H + 4 * r * TT_FR, smem + K4_X, tid);
    LBAR();
    TS(15);
    unsigned pc[3][18];
    const rsrc_t rc = mkrsrc(ws + OFF_CSP, 18 * FF * 4);
    if (!A) {
#pragma unroll
        for (int m = 0; m < 3; ++m) if (m < 2 || w == 4) {
#pragma unroll
            for (int ks = 0; ks < 18; ++ks) pc[m][ks] = __builtin_amdgcn_raw_buffer_load_b32(rc, (576 * j + 256 * m + tid - 256) * 4, ks * FF * 4, 16); } }
    unsigned short b1v[3];
    if (!A) {
#pragma unroll
        for (int m = 0; m < 3; ++m) if (m < 2 || w == 4) b1v[m] = *(const GAS unsigned short*)((const char*)a.b1 + (576 * j + 256 * m + tid - 256) * 2); }
    SB();
    LAS float* st = (LAS float*)(smem + K4_ST);
    if (!A) {
        f32x4 s1 = {0.f, 0.f, 0.f, 0.f}, s2 = s1;
        const v4u ones = {0x3f803f80u, 0x3f803f80u, 0x3f803f80u, 0x3f803f80u};
#pragma unroll
        for (int kh = 0; kh < 2; ++kh) { v4u v[9];
#pragma unroll
            for (int k = 0; k < 9; ++k) v[k] = lds128(smem + K4_X + (s * 18 + 9 * kh + k) * 1024 + lane * 16);
#pragma unroll
            for (int k = 0; k < 9; ++k) { s1 = mfma16(ones, v[k], s1); s2 = mfma16(v[k], v[k], s2); } }
        const float sq = (i & 2) ? ((i & 1) ? s2[3] : s2[2]) : ((i & 1) ? s2[1] : s2[0]);
        const float mean = s1[0] * (1.0f / D), var = __builtin_fmaxf(__builtin_fmaf(-mean, mean, sq * (1.0f / D)), 0.f);
        if (g == (i >> 2)) *(LAS f32x2*)(st + (16 * s + i) * 2) = f32x2{mean, __builtin_amdgcn_rsqf(var + LN_EPS)};
    }
    if (!A) {
#pragma unroll
        for (int m = 0; m < 3; ++m) if (m < 2 || w == 4) { float sum = 0.f;
#pragma unroll
            for (int ks = 0; ks < 18; ++ks) sum += __builtin_bit_cast(float, pc[m][ks]);
            *(LAS float*)(smem + K4_CS + (256 * m + tid - 256) * 4) = sum; *(LAS float*)(smem + K4_B1 + (256 * m + tid - 256) * 4) = blo(b1v[m]); }
        asm volatile("s_waitcnt lgkmcnt(0)" ::: "memory");
        if (lane == 0) __hip_atomic_fetch_add((LAS unsigned*)(smem + K4_CNT), 1u, __ATOMIC_RELAXED, __HIP_MEMORY_SCOPE_WORKGROUP); }
    STAMP(ws, 9);
    f32x4 acc[2][4];
    auto none = [](int) {};
    {
        const int hh = lane >> 5, l31 = lane & 31, G = 2 * s + (A ? 0 : 1);
        const LAS char* xp = smem + K4_X + (l31 >> 4) * 18432 + (hh * 16 + (lane & 15)) * 16;
        f32x16 ax[2][2];
        auto act32 = [&](int slot, int c) {
            const f32x2 mr0 = *(const LAS f32x2*)(st + l31 * 2), mr1 = *(const LAS f32x2*)(st + (32 + l31) * 2);
            f32x4 cv[4], bv[4];
#pragma unroll
            for (int qd = 0; qd < 4; ++qd) { const int f = 64 * G + 32 * c + 8 * qd + 4 * hh; cv[qd] = *(const LAS f32x4*)(smem + K4_CS + f * 4); bv[qd] = *(const LAS f32x4*)(smem + K4_B1 + f * 4); }
#pragma unroll
            for (int ty = 0; ty < 2; ++ty)
#pragma unroll
                for (int p = 0; p < 2; ++p) { unsigned o[4];
#pragma unroll
                    for (int x = 0; x < 2; ++x) { const int qd = 2 * p + x;
                        ln_gelu4(f32x4{ax[slot][ty][4 * qd], ax[slot][ty][4 * qd + 1], ax[slot][ty][4 * qd + 2], ax[slot][ty][4 * qd + 3]}, cv[qd], bv[qd][0], bv[qd][1], bv[qd][2], bv[qd][3],
                                 -(ty ? mr1 : mr0)[0], (ty ? mr1 : mr0)[1], o[2 * x], o[2 * x + 1]); }
                    *(LAS v4u*)(smem + K4_H + ((2 * ty + (l31 >> 4)) * 18 + 2 * G + c) * 1024 + ((2 * p + hh) * 16 + (lane & 15)) * 16) = v4u{o[0], o[1], o[2], o[3]}; } };
        auto tables = [&]() { while (*(volatile LAS unsigned*)(smem + K4_CNT) < 4u) { }
            asm volatile("" ::: "memory"); };
        if (A) {
            ffn_pass32<2, 0, PRA>(ax, ring, rw, RD * 1024, xp, lane);
            TS(16);
            tables();
            act32(0, 0); act32(1, 1);
            TS(11);
            __builtin_amdgcn_s_setprio(PRA);
            ffn_pass<1, 72 % RD>(acc, ring, rw, (72 + RD) * 1024, smem + K4_X + lane * 16, lane, none);
            TS(23);
            __builtin_amdgcn_s_setprio(PRV);
            const int f = 512 + 16 * s + 4 * g;
            const f32x4 cv = *(const LAS f32x4*)(smem + K4_CS + f * 4), bv = *(const LAS f32x4*)(smem + K4_B1 + f * 4);
#pragma unroll
            for (int tt = 0; tt < 4; ++tt) { const f32x2 mr = *(const LAS f32x2*)(st + (16 * tt + i) * 2);
                unsigned o01, o23; ln_gelu4(acc[0][tt], cv, bv[0], bv[1], bv[2], bv[3], -mr[0], mr[1], o01, o23);
                *(LAS v2u*)(smem + K4_H + (tt * 18 + 16 + (s >> 1)) * 1024 + lane * 16 + (s & 1) * 8) = v2u{o01, o23}; }
        } else {
            ffn_pass32<1, 0, PRB>(ax, ring, rw, RD * 1024, xp, lane);
            TS(16);
            tables();
            act32(0, 0);
            TS(11);
            ffn_pass32<1, 36 % RD, PRB>(ax, ring, rw, (36 + RD) * 1024, xp, lane);
            TS(23);
            act32(0, 1);
        }
    }
    TS(17);
    STAMP(ws, 10);
    LBAR();
    TS(18);
    STAMP(ws, 11);
    __builtin_amdgcn_s_setprio(A ? PRG : 0);
    constexpr int G2 = A ? 90 : 72;
    const LAS char* hp = smem + K4_H + lane * 16;
    const rsrc_t rp = mkrsrc(ws + OFF_P + (size_t)r * NSL * P_SRC, NSL * (unsigned)P_SRC);
    v4u outq[4];
    auto pack = [&]() {
#pragma unroll
        for (int t = 0; t < 2; ++t)
#pragma unroll
            for (int tp = 0; tp < 2; ++tp) {
                outq[t * 2 + tp] = v4u{pk2(acc[t][2 * tp][0], acc[t][2 * tp][1]), pk2(acc[t][2 * tp][2], acc[t][2 * tp][3]), pk2(acc[t][2 * tp + 1][0], acc[t][2 * tp + 1][1]), pk2(acc[t][2 * tp + 1][2], acc[t][2 * tp + 1][3])};
                PIN(outq[t * 2 + tp]); } };
    auto put = [&](int k, int ps) { const int gi = (A ? 0 : 4) + 2 * ps + (k >> 1), dt = 4 * (gi < j ? gi : gi + 1) + s;
        bst128<16>(outq[k], rp, lane * 16, j * (int)P_SRC + (dt * 2 + (k & 1)) * 1024); };
    ffn_pass<2, G2 % RD>(acc, ring, rw, (G2 + RD) * 1024, hp, lane, none);
    pack();
    ffn_pass<2, (G2 + 36) % RD>(acc, ring, rw, (G2 + 36 + RD) * 1024, hp, lane, [&](int u) { if (u >= 1 && u <= 7 && (u & 1)) put(u >> 1, 0); });
    pack();
    if (!A) {
#pragma unroll
        for (int k = 0; k < 4; ++k) put(k, 1); }
    TS(19);
    if (A) { ffn_pass<1, (G2 + 72) % RD>(acc, ring, rw, (G2 + 72 + RD) * 1024, hp, lane, [&](int u) { if (u <= 6 && !(u & 1)) put(u >> 1, 1); });
#pragma unroll
        for (int u = 0; u < 2; ++u) *(LAS f32x4*)(smem + K4_OWN + (s * 2 + u) * 1024 + lane * 16) = acc[0][2 + u]; }
    STAMP(ws, 12);
    asm volatile("s_waitcnt vmcnt(0) lgkmcnt(0)\n\ts_barrier" ::: "memory");
    TS(20);
    if (tid == 448) { flag_add(ws, FL_P + r); flag_wait(ws, FL_P + r, 8); }
    BAR();
    STAMP(ws, 13);
    TS(21);
    {
        const int dt = 4 * j + s, tp = A ? 0 : 1, dx = 32 + (j >> 1), tpx = j & 1;
        const bool extra = !A && s == (j & 3);
        v4u pv[8], px[8];
#pragma unroll
        for (int src = 0; src < 8; ++src) pv[src] = bld128_sc1(rp, lane * 16, src != j ? src * (int)P_SRC + (dt * 2 + tp) * 1024 : 0x7fff0000);
        if (extra) {
#pragma unroll
            for (int src = 0; src < 8; ++src) px[src] = bld128_sc1(rp, lane * 16, src * (int)P_SRC + (dx * 2 + tpx) * 1024); }
        const v2u b2v = *(const GAS v2u*)((const char*)a.b2 + (16 * dt + 4 * g) * 2), b2x = *(const GAS v2u*)((const char*)a.b2 + (16 * dx + 4 * g) * 2);
        SB();
        auto fin = [&](const v4u (&p)[8], int d_t, int t_p, v2u b2, bool own) {
#pragma unroll
            for (int u = 0; u < 2; ++u) { const int tt = 2 * t_p + u;
                const v2u hv = lds64(smem + K4_X + (tt * 18 + (d_t >> 1)) * 1024 + ((2 * (d_t & 1) + (g >> 1)) * 16 + i) * 16 + (g & 1) * 8);
                f32x4 o = {blo(hv.x) + blo(b2.x), bhi(hv.x) + bhi(b2.x), blo(hv.y) + blo(b2.y), bhi(hv.y) + bhi(b2.y)};
                if (own) { if (A) o += acc[0][u]; else o += *(const LAS f32x4*)(smem + K4_OWN + (s * 2 + u) * 1024 + lane * 16); }
#pragma unroll
                for (int src = 0; src < 8; ++src) o += f32x4{blo(p[src][2 * u]), bhi(p[src][2 * u]), blo(p[src][2 * u + 1]), bhi(p[src][2 * u + 1])};
                *(GAS v2u*)((char*)a.out + (size_t)(64 * r + 16 * tt + i) * (D * 2) + (16 * d_t + 4 * g) * 2) = v2u{pk2(o[0], o[1]), pk2(o[2], o[3])};
            } };
        fin(pv, dt, tp, b2v, true);
        if (extra) fin(px, dx, tpx, b2x, false);
    }
    STAMP(ws, 14);
    TSW(22);
    TSDUMP(ws, 1);
}

__global__ __launch_bounds__(512, 2) void k2_rest(K2Args a) {
    extern __shared__ __attribute__((aligned(16))) char smem_[];
    LAS char* smem = (LAS char*)smem_;
    const unsigned lds0 = (unsigned)(size_t)smem_;
    char* ws = a.ws;
    const int tid = threadIdx.x, lane = tid & 63, w = __builtin_amdgcn_readfirstlane(tid >> 6), b = blockIdx.x;
    const int j = wg_j(b), r = wg_r(b);
    STAMP(ws, 3);
    TSDECL; TS(0);
    const rsrc_t rws = mkrsrc(ws, (unsigned)WS_END);
    {
        const int hl = w & 3, qh = w >> 2, l31 = lane & 31, hh = lane >> 5;
        const rsrc_t rk = mkrsrc(ws + OFF_QKV + (8 + j) * QKV_G, S * 144), rv = mkrsrc(ws + OFF_QKV + (16 + j) * QKV_G, S * 144);
        const int tok0 = tid / 9, mm = tid - 9 * tok0;
        v4u kk[6], vv[6];
#pragma unroll
        for (int m = 0; m < 6; ++m) { const int tok = tok0 + 56 * m, o_ = (64 * r - 256) * 144 + tid * 16 + m * 56 * 144;
            const int off = (tid < 504 && tok < 320 && o_ >= 0) ? o_ : 0x7ffffff0;
            kk[m] = bld128(rk, off, 0); vv[m] = bld128(rv, off, 0); }
        const char* qp = ws + OFF_QKV + j * QKV_G + (size_t)(64 * r + 32 * qh + l31) * 144 + 36 * hl;
        v4u q0, q1;
        { const GAS unsigned* qq = (const GAS unsigned*)(qp + 16 * hh); q0 = v4u{qq[0], qq[1], qq[2], qq[3]};
          const unsigned t = *(const GAS unsigned*)(qp + 32); q1 = v4u{hh == 0 ? t : 0u, 0u, 0u, 0u}; }
        const Unit ua = unit_decode(a.w1, a.w2, a.wo, 1458 + b * 8 + w);
        for (int n = tid; n < 1280; n += 512) { *(LAS unsigned*)(smem + K2_K + n * 40 + 36) = 0u; *(LAS unsigned*)(smem + K2_V + n * 40 + 36) = 0x3f80u; }
        if (tid == 0) *(LAS unsigned*)(smem + K4_CNT) = 0u;
        int lo_[4];
#pragma unroll
        for (int e = 0; e < 4; ++e) { const int dw = 4 * mm + e, h = dw / 9; lo_[e] = (h * 320 + tok0) * 40 + (dw - 9 * h) * 4; }
        TS(1);
        if (tid < 504) {
#pragma unroll
            for (int m = 0; m < 6; ++m) if (m < 5 || tok0 + 280 < 320) {
#pragma unroll
                for (int e = 0; e < 4; ++e) { *(LAS unsigned*)(smem + K2_K + lo_[e] + m * 56 * 40) = kk[m][e]; *(LAS unsigned*)(smem + K2_V + lo_[e] + m * 56 * 40) = vv[m][e]; } }
        }
        unit_load(ua, lds0 + K2_SCR + w * 4096, lane);
        TS(2);
        LBAR();
        TS(3);
        STAMP(ws, 4);
        f32x16 oacc;
        if (r < 4) attn_core<true>(smem, q0, q1, r, w, lane, oacc, ua, rws);
        else attn_core<false>(smem, q0, q1, r, w, lane, oacc, ua, rws);
        TS(4);
        const float inv = __builtin_amdgcn_rcpf(lo32(oacc[10]));
        LAS char* op = smem + K2_O + (32 * qh + l31) * 144 + 36 * hl + 8 * hh;
        *(LAS unsigned*)(op) = pk2(oacc[0] * inv, oacc[1] * inv); *(LAS unsigned*)(op + 4) = pk2(oacc[2] * inv, oacc[3] * inv);
        *(LAS unsigned*)(op + 16) = pk2(oacc[4] * inv, oacc[5] * inv); *(LAS unsigned*)(op + 20) = pk2(oacc[6] * inv, oacc[7] * inv);
        if (hh == 0) *(LAS unsigned*)(op + 32) = pk2(oacc[8] * inv, oacc[9] * inv);
        LBAR();
        TS(5);
#pragma unroll
        for (int m2 = 0; m2 < 2; ++m2) { const int n = tid + 512 * m2;
            if (n < 576) { const int i = n & 15, tt = (n >> 4) & 3, m = n >> 6;
                const v4u v = lds128(smem + K2_O + (16 * tt + i) * 144 + 16 * m);
                const int n8 = 72 * j + 8 * m, ks = n8 >> 5, g = (n8 & 31) >> 3;
                bst128<16>(v, rws, (int)OFF_O + (4 * r + tt) * (int)TT_FR + ks * 1024 + (g * 16 + i) * 16, 0); } }
        asm volatile("s_waitcnt vmcnt(0) lgkmcnt(0)\n\ts_barrier" ::: "memory");
        STAMP(ws, 5);
        TS(6);
    }
    if (w < 4) ffn_body<true>(a, smem, r, j, w, lane); else ffn_body<false>(a, smem, r, j, w, lane);
}

extern "C" void kernel_launch(void* const* d_in, const int* in_sizes, int n_in, void* d_out, int out_size, void* d_ws, size_t ws_size, hipStream_t stream) {
    (void)in_sizes; (void)n_in; (void)out_size; (void)ws_size;
    char* ws = (char*)d_ws;
    (void)hipFuncSetAttribute((const void*)k1_qkv, hipFuncAttributeMaxDynamicSharedMemorySize, K1_LDS);
    (void)hipFuncSetAttribute((const void*)k2_rest, hipFuncAttributeMaxDynamicSharedMemorySize, K2_LDS);
    K1Args a1{d_in[0], d_in[1], d_in[2], d_in[4], d_in[6], ws};
    k1_qkv<<<256, 512, K1_LDS, stream>>>(a1);
    K2Args a2{ws, d_in[0], d_in[2], d_in[4], d_in[6], d_in[5], d_in[7], d_out};
    k2_rest<<<256, 512, K2_LDS, stream>>>(a2);
}
```

```cpp
#include <hip/hip_runtime.h>
#include <hip/hip_bf16.h>
#include <stdint.h>

#define GAS __attribute__((address_space(1)))
#define LAS __attribute__((address_space(3)))
#define DI __device__ __forceinline__
typedef unsigned v4u __attribute__((ext_vector_type(4)));
typedef unsigned v2u __attribute__((ext_vector_type(2)));
typedef float f32x4 __attribute__((ext_vector_type(4)));
typedef float f32x16 __attribute__((ext_vector_type(16)));
typedef short bf16x8 __attribute__((ext_vector_type(8)));
typedef __bf16 bf2 __attribute__((ext_vector_type(2)));
typedef GAS unsigned gu32;
typedef __amdgpu_buffer_rsrc_t rsrc_t;

#define STAMPS 0

constexpr int S = 2048, D = 576, FF = 4608, N3 = 3 * D;
constexpr int NR = 32;
constexpr float LN_EPS = 1e-5f;
constexpr float QSCALE = 0.23570226039551584f * 1.4426950408889634f;

constexpr size_t FRAG = 1024;
constexpr size_t OFF_FLAG = 0;
constexpr int FL_O = 0, FL_H = 32, FL_P = 64, FL_N = 96;
constexpr size_t OFF_STAMP = 32768;
constexpr size_t OFF_QKV = 65536;
constexpr size_t QKV_G = (size_t)S * 144;
constexpr int NSL = 8;
constexpr size_t OFF_WS = OFF_QKV + 24 * QKV_G;
constexpr size_t WS_J = (size_t)1296 * FRAG;
constexpr size_t OFF_WO = OFF_WS + NSL * WS_J;
constexpr size_t OFF_O = OFF_WO + (size_t)36 * 18 * FRAG;
constexpr size_t TT_FR = (size_t)18 * FRAG;
constexpr size_t OFF_H = OFF_O + 128 * TT_FR;
constexpr size_t OFF_P = OFF_H + 128 * TT_FR;
constexpr size_t P_SRC = (size_t)36 * 2 * FRAG;
constexpr size_t OFF_CSP = OFF_P + (size_t)NR * NSL * P_SRC;
constexpr size_t WS_END = OFF_CSP + (size_t)18 * FF * 4;

__host__ __device__ constexpr int ffn_stream_base(int wv) { return (wv < 4 ? wv * 180 : 720 + (wv - 4) * 144) * 1024; }
#define T0B 2
__host__ __device__ constexpr int ffn_pos1(bool a, int t, int ks) { const int t0 = a ? 3 : T0B, nt = a ? 5 : 4; return t < t0 ? ks * t0 + t : 18 * t0 + ks * (nt - t0) + (t - t0); }
__host__ __device__ constexpr int ffn_pos2(bool a, int t, int ks) { return (a ? 90 : 72) + (t < 4 ? (t >> 1) * 36 + ks * 2 + (t & 1) : 72 + ks); }
DI int wg_r(int b) { return 8 * ((b & 7) >> 1) + ((b >> 3) & 7); }
DI int wg_j(int b) { return 4 * (b & 1) + (b >> 6); }

DI f32x4 mfma16(v4u a, v4u b, f32x4 c) { return __builtin_amdgcn_mfma_f32_16x16x32_bf16(__builtin_bit_cast(bf16x8, a), __builtin_bit_cast(bf16x8, b), c, 0, 0, 0); }
DI f32x16 mfma32(v4u a, v4u b, f32x16 c) { return __builtin_amdgcn_mfma_f32_32x32x16_bf16(__builtin_bit_cast(bf16x8, a), __builtin_bit_cast(bf16x8, b), c, 0, 0, 0); }
DI unsigned pk2(float a, float b) { bf2 v = {(__bf16)a, (__bf16)b}; return __builtin_bit_cast(unsigned, v); }
DI float blo(unsigned u) { return __builtin_bit_cast(float, u << 16); }
DI float bhi(unsigned u) { return __builtin_bit_cast(float, u & 0xffff0000u); }
DI rsrc_t mkrsrc(const void* p, unsigned bytes) { return __builtin_amdgcn_make_buffer_rsrc((void*)p, 0, bytes, 0x00020000); }
DI v4u bld128(rsrc_t r, int voff, int soff) { return __builtin_amdgcn_raw_buffer_load_b128(r, voff, soff, 0); }
DI v4u bld128_sc1(rsrc_t r, int voff, int soff) { return __builtin_amdgcn_raw_buffer_load_b128(r, voff, soff, 16); }
DI v2u bld64(rsrc_t r, int voff, int soff) { return __builtin_amdgcn_raw_buffer_load_b64(r, voff, soff, 0); }
template <int AUX> DI void bst128(v4u v, rsrc_t r, int voff, int soff) { __builtin_amdgcn_raw_buffer_store_b128(v, r, voff, soff, AUX); asm volatile("s_nop 1" :: "v"(v)); }
DI v4u lds128(const LAS char* p) { return *(const LAS v4u*)p; }
DI v2u lds64(const LAS char* p) { return *(const LAS v2u*)p; }
DI v2u lds64v(const LAS char* p) { return *(const volatile LAS v2u*)p; }
typedef short v4i16_t __attribute__((ext_vector_type(4)));
DI v2u ldstr(const LAS char* p) { return __builtin_bit_cast(v2u, __builtin_amdgcn_ds_read_tr16_b64_v4i16((LAS v4i16_t*)p)); }
DI float dot2(unsigned a, float c) { return __builtin_amdgcn_fdot2_f32_bf16(__builtin_bit_cast(bf2, a), __builtin_bit_cast(bf2, 0x3f803f80u), c, false); }
DI float dot2sq(unsigned a, float c) { const bf2 v = __builtin_bit_cast(bf2, a); return __builtin_amdgcn_fdot2_f32_bf16(v, v, c, false); }
DI void dma16(rsrc_t r, int voff, int soff, unsigned lds) {
    asm volatile("s_mov_b32 m0, %0\n\ts_nop 0\n\tbuffer_load_dwordx4 %1, %2, %3 offen lds" :: "s"(lds), "v"(voff), "s"(r), "s"(soff) : "memory");
}
#define WAITV(n) asm volatile("s_waitcnt vmcnt(" #n ")" ::: "memory")
#define BAR() asm volatile("s_barrier" ::: "memory")
#define LBAR() asm volatile("s_waitcnt lgkmcnt(0)\n\ts_barrier" ::: "memory")
#define SB() __builtin_amdgcn_sched_barrier(0)
#define PIN(x) asm volatile("" : "+v"(x))
DI float sum16(float v) { const unsigned u = __builtin_bit_cast(unsigned, v); const auto r = __builtin_amdgcn_permlane16_swap(u, u, false, false);
    return __builtin_bit_cast(float, (unsigned)r[0]) + __builtin_bit_cast(float, (unsigned)r[1]); }
DI float sum32(float v) { const unsigned u = __builtin_bit_cast(unsigned, v); const auto r = __builtin_amdgcn_permlane32_swap(u, u, false, false);
    return __builtin_bit_cast(float, (unsigned)r[0]) + __builtin_bit_cast(float, (unsigned)r[1]); }
DI float max32(float v) { const unsigned u = __builtin_bit_cast(unsigned, v); const auto r = __builtin_amdgcn_permlane32_swap(u, u, false, false);
    return __builtin_fmaxf(__builtin_bit_cast(float, (unsigned)r[0]), __builtin_bit_cast(float, (unsigned)r[1])); }
DI float lo32(float v) { const unsigned u = __builtin_bit_cast(unsigned, v); const auto r = __builtin_amdgcn_permlane32_swap(u, u, false, false);
    return __builtin_bit_cast(float, (unsigned)r[0]); }
typedef float f32x2 __attribute__((ext_vector_type(2)));
DI void ln_gelu4(f32x4 acc, f32x4 cs, float b0, float b1, float b2, float b3, float nmean, float rs, unsigned& o01, unsigned& o23) {
    float z0, z1, z2, z3, t0, t1, t2, t3;
    asm("v_fma_f32 %0, %16, %12, %8\n\tv_fma_f32 %1, %16, %13, %9\n\tv_fma_f32 %2, %16, %14, %10\n\tv_fma_f32 %3, %16, %15, %11\n\t"
        "v_fma_f32 %0, %17, %0, %18\n\tv_fma_f32 %1, %17, %1, %19\n\tv_fma_f32 %2, %17, %2, %20\n\tv_fma_f32 %3, %17, %3, %21\n\t"
        "v_mul_f32 %4, %0, %0\n\tv_mul_f32 %5, %1, %1\n\tv_mul_f32 %6, %2, %2\n\tv_mul_f32 %7, %3, %3\n\t"
        "v_fmaak_f32 %4, %22, %4, 0xc0135761\n\tv_fmaak_f32 %5, %22, %5, 0xc0135761\n\tv_fmaak_f32 %6, %22, %6, 0xc0135761\n\tv_fmaak_f32 %7, %22, %7, 0xc0135761\n\t"
        "v_mul_f32 %4, %0, %4\n\tv_mul_f32 %5, %1, %5\n\tv_mul_f32 %6, %2, %6\n\tv_mul_f32 %7, %3, %7\n\t"
        "v_exp_f32 %4, %4\n\tv_exp_f32 %5, %5\n\tv_exp_f32 %6, %6\n\tv_exp_f32 %7, %7\n\t"
        "v_add_f32 %4, 1.0, %4\n\tv_add_f32 %5, 1.0, %5\n\tv_add_f32 %6, 1.0, %6\n\tv_add_f32 %7, 1.0, %7\n\t"
        "v_rcp_f32 %4, %4\n\tv_rcp_f32 %5, %5\n\tv_rcp_f32 %6, %6\n\tv_rcp_f32 %7, %7\n\t"
        "v_mul_f32 %0, %0, %4\n\tv_mul_f32 %1, %1, %5\n\tv_mul_f32 %2, %2, %6\n\tv_mul_f32 %3, %3, %7"
        : "=&v"(z0), "=&v"(z1), "=&v"(z2), "=&v"(z3), "=&v"(t0), "=&v"(t1), "=&v"(t2), "=&v"(t3)
        : "v"(acc[0]), "v"(acc[1]), "v"(acc[2]), "v"(acc[3]), "v"(cs[0]), "v"(cs[1]), "v"(cs[2]), "v"(cs[3]), "v"(nmean), "v"(rs), "v"(b0), "v"(b1), "v"(b2), "v"(b3),
          "v"(-0.1029432397f));
    o01 = pk2(z0, z1); o23 = pk2(z2, z3);
}
#define STAMP(ws, i) do { } while (0)
#define FINE 0
#define TS(n) do { } while (0)
#define TSW(n) do { } while (0)
#define TSDECL
#define TSDUMP(ws, kidx) do { } while (0)

struct Unit { rsrc_t rs; int soff, ld, w2, live; int dst[4]; int cs; };
constexpr int NUNIT = 1296 + 1296 + 162;
struct UnitTab { int v[NUNIT + 1][8]; };
constexpr UnitTab make_units() {
    UnitTab T{};
    for (int u = 0; u <= NUNIT; ++u) {
        const bool iswo = u < 162, isw1 = !iswo && u < 1458, isw2 = u >= 1458 && u < NUNIT;
        const int v = iswo ? u : isw1 ? u - 162 : u - 1458;
        const int j = isw1 ? (v % 72) / 9 : v / 162, ks = isw1 ? v / 72 : (v % 162) % 18, G = isw1 ? v % 9 : (v % 162) / 18;
        T.v[u][0] = isw1 ? 0 : isw2 ? 1 : iswo ? 2 : 3;
        T.v[u][1] = isw1 ? (32 * ks * FF + 576 * j + 64 * G) * 2 : ((isw2 ? 576 * j : 0) + 32 * ks) * (D * 2) + 128 * G;
        const int base = (int)OFF_WS + j * (int)WS_J, gi = G < j ? G : G - 1;
        const bool A = isw1 ? G < 5 : (G == j || gi < 4);
        const int fp = isw1 ? ffn_pos1(A, A ? G : G - 5, ks) : ffn_pos2(A, G == j ? 4 : gi & 3, ks);
        for (int c = 0; c < 4; ++c)
            T.v[u][2 + c] = (isw1 || isw2) ? base + ffn_stream_base(A ? c : 4 + c) + fp * 1024 : iswo ? (int)OFF_WO + ((4 * G + c) * 18 + ks) * 1024 : 0x7fff0000;
        T.v[u][6] = isw1 ? (int)OFF_CSP + (ks * FF + 576 * j + 64 * G) * 4 : 0x7fff0000;
        T.v[u][7] = 0;
    }
    return T;
}
__device__ const UnitTab UNITS = make_units();
DI Unit unit_decode(const void* w1, const void* w2, const void* wo, int u) {
    const int* e = UNITS.v[u < NUNIT ? u : NUNIT];
    const int kind = e[0];
    Unit r;
    r.rs = mkrsrc(kind == 0 ? w1 : kind == 1 ? w2 : wo, kind == 3 ? 0u : kind == 2 ? D * D * 2 : D * FF * 2);
    r.ld = kind == 0 ? FF * 2 : D * 2; r.w2 = kind == 1; r.live = kind != 3; r.soff = e[1];
#pragma unroll
    for (int c = 0; c < 4; ++c) r.dst[c] = e[2 + c];
    r.cs = e[6];
    return r;
}
#define UST 16
DI void unit_load(const Unit& t, unsigned scr, int lane) {
    const int voff = (lane >> 3) * t.ld + (lane & 7) * 16;
#pragma unroll
    for (int m = 0; m < 4; ++m) dma16(t.rs, voff, t.soff + 8 * m * t.ld, scr + m * 1024);
}
DI void unit_store(const Unit& t, const LAS char* scr, rsrc_t rws, int lane) {
    const int g = lane >> 4, q = (lane & 15) >> 2, p = lane & 3;
    const int row0 = t.w2 ? 4 * g + q : 8 * g + q, rstep = t.w2 ? 16 : 4;
    float cs[4];
#pragma unroll
    for (int c = 0; c < 4; ++c) {
        const v2u lo = ldstr(scr + row0 * 128 + (16 * c + 4 * p) * 2);
        const v2u hi = ldstr(scr + (row0 + rstep) * 128 + (16 * c + 4 * p) * 2);
        bst128<UST>(v4u{lo.x, lo.y, hi.x, hi.y}, rws, lane * 16, t.dst[c]);
        cs[c] = sum32(sum16(dot2(hi.y, dot2(hi.x, dot2(lo.y, dot2(lo.x, 0.f))))));
    }
    const float mine = g == 0 ? cs[0] : g == 1 ? cs[1] : g == 2 ? cs[2] : cs[3];
    __builtin_amdgcn_raw_buffer_store_b32(__builtin_bit_cast(unsigned, mine), rws, lane * 4, t.cs, 16);
}

DI void unit_store_w2(const Unit& t, const LAS char* scr, rsrc_t rws, int lane) {
    const int g = lane >> 4, q = (lane & 15) >> 2, p = lane & 3;
    v2u lo[4], hi[4];
#pragma unroll
    for (int c = 0; c < 4; ++c) { lo[c] = ldstr(scr + (4 * g + q) * 128 + 32 * c + 8 * p); hi[c] = ldstr(scr + (4 * g + q + 16) * 128 + 32 * c + 8 * p); }
#pragma unroll
    for (int c = 0; c < 4; ++c) bst128<UST>(v4u{lo[c].x, lo[c].y, hi[c].x, hi[c].y}, rws, lane * 16, t.dst[c]);
}

#define QST 0
struct K1Args { const void* x; const void* wqkv; const void* wo; const void* w1; const void* w2; char* ws; };
constexpr int K1_STAGE = 32768;
constexpr int K1_TAB = 4 * K1_STAGE;
constexpr int K1_LDS = K1_TAB + 4096;

__global__ __launch_bounds__(512, 2) void k1_qkv(K1Args a) {
    extern __shared__ __attribute__((aligned(16))) char smem_[];
    LAS char* smem = (LAS char*)smem_;
    const unsigned lds0 = (unsigned)(size_t)smem_;
    const int tid = threadIdx.x, lane = tid & 63, w = __builtin_amdgcn_readfirstlane(tid >> 6), b = blockIdx.x;
    STAMP(a.ws, 0);
    TSDECL; TS(0);
    if (b == 0 && tid < FL_N) *(GAS unsigned*)(a.ws + OFF_FLAG + tid * 256) = 0u;
    if (b >= 224) {
        const rsrc_t rws = mkrsrc(a.ws, (unsigned)WS_END);
        const int u0 = ((b - 224) * 8 + w) * 4;
        Unit t[4];
#pragma unroll
        for (int m = 0; m < 4; ++m) { t[m] = unit_decode(a.w1, a.w2, a.wo, u0 + m); unit_load(t[m], lds0 + w * 16384 + m * 4096, lane); }
        WAITV(12); unit_store(t[0], smem + w * 16384, rws, lane);
        WAITV(13); unit_store(t[1], smem + w * 16384 + 4096, rws, lane);
        WAITV(14); unit_store(t[2], smem + w * 16384 + 8192, rws, lane);
        WAITV(15); unit_store(t[3], smem + w * 16384 + 12288, rws, lane);
        STAMP(a.ws, 2);
        return;
    }
    const int xc = b & 7, idx = b >> 3;
    const int rt = 4 * (xc >> 1) + (idx & 3), ct = 7 * (xc & 1) + (idx >> 2);
    if (w >= 4) {
        const int lw = w - 4;
        const rsrc_t rx = mkrsrc(a.x, S * D * 2), rw = mkrsrc(a.wqkv, D * N3 * 2);
        const int vx0 = (128 * rt + 32 * lw + (lane >> 3)) * (D * 2) + ((lane & 7) ^ (lane >> 4)) * 16, vx1 = vx0 ^ 64;
        const int c16 = lane & 15, q = lane >> 4;
        const int vw0 = (16 * lw + q) * (N3 * 2) + 256 * ct + ((((c16 >> 1) ^ (q << 1)) << 1) | (c16 & 1)) * 16;
        const int vw1 = (16 * lw + q) * (N3 * 2) + 256 * ct + ((((c16 >> 1) ^ ((q << 1) | 1)) << 1) | (c16 & 1)) * 16;
        auto stage = [&](int s) {
            const unsigned sb = lds0 + (s & 3) * K1_STAGE;
#pragma unroll
            for (int m = 0; m < 4; ++m) {
                dma16(rw, m < 2 ? vw0 : vw1, (64 * s + 4 * m) * (N3 * 2), sb + 16384 + (4 * lw + m) * 1024);
                dma16(rx, (m & 1) ? vx1 : vx0, 128 * s + m * 8 * (D * 2), sb + (4 * lw + m) * 1024);
            }
        };
        const int g = lane >> 4, i = lane & 15, q4 = i >> 2, p = lane & 3;
        const int sxo = (32 * lw + i) * 128, swo = 16384 + (8 * g + q4) * 256 + 8 * p, key = (q4 << 1) | (g & 1);
        const v4u ones = {0x3f803f80u, 0x3f803f80u, 0x3f803f80u, 0x3f803f80u};
        f32x4 sx[2], sq[2], cs[2];
#pragma unroll
        for (int u = 0; u < 2; ++u) sx[u] = sq[u] = cs[u] = f32x4{0.f, 0.f, 0.f, 0.f};
        auto stats = [&](int s) {
            const LAS char* sb = smem + (s & 3) * K1_STAGE;
#pragma unroll
            for (int k2 = 0; k2 < 2; ++k2)
#pragma unroll
                for (int u = 0; u < 2; ++u) {
                    const v4u xv = lds128(sb + sxo + u * 2048 + ((4 * k2 + g) ^ ((i >> 1) & 7)) * 16);
                    const LAS char* ap = sb + swo + k2 * 8192 + (((2 * lw + u) ^ key) * 32);
                    const v2u lo = ldstr(ap), hi = ldstr(ap + 1024);
                    sx[u] = mfma16(ones, xv, sx[u]); sq[u] = mfma16(xv, xv, sq[u]); cs[u] = mfma16(v4u{lo.x, lo.y, hi.x, hi.y}, ones, cs[u]);
                }
        };
        stage(0); stage(1); stage(2);
        WAITV(16); TS(1); BAR();
        WAITV(8); BAR();
        stats(0);
#pragma unroll
        for (int s = 3; s < 9; ++s) { stage(s); stats(s - 2); WAITV(8); asm volatile("s_waitcnt lgkmcnt(0)" ::: "memory"); BAR(); }
        stats(7); WAITV(0); BAR();
        stats(8);
        LAS float* tabx = (LAS float*)(smem + K1_TAB);
        LAS float* tabc = tabx + 256;
#pragma unroll
        for (int u = 0; u < 2; ++u) {
            const float s2 = (i & 2) ? ((i & 1) ? sq[u][3] : sq[u][2]) : ((i & 1) ? sq[u][1] : sq[u][0]);
            const float mean = sx[u][0] * (1.0f / D), var = __builtin_fmaxf(__builtin_fmaf(-mean, mean, s2 * (1.0f / D)), 0.f);
            if (g == q4) *(LAS f32x2*)(tabx + (32 * lw + 16 * u + i) * 2) = f32x2{mean, __builtin_amdgcn_rsqf(var + LN_EPS)};
            if (i == 0) *(LAS f32x4*)(tabc + 32 * lw + 16 * u + 4 * g) = cs[u]; }
        LBAR();
        TS(9);
        TSDUMP(a.ws, 0);
        return;
    }
    const int th = w & 1, ch = w >> 1, h = lane >> 5, l31 = lane & 31;
    const int q = (lane & 15) >> 2, p = lane & 3, blk = (lane >> 4) & 1;
    const int xrow = (64 * th + l31) * 128, xkey = (l31 >> 1) & 7;
    const int woff = 16384 + (8 * h + q) * 256 + (((4 * ch + blk) ^ ((q << 1) | h)) * 32) + 8 * p;
    f32x16 acc[2][2];
#pragma unroll
    for (int cx = 0; cx < 2; ++cx)
#pragma unroll
        for (int ty = 0; ty < 2; ++ty)
#pragma unroll
            for (int e = 0; e < 16; ++e) acc[cx][ty][e] = 0.f;
    int xo[4];
#pragma unroll
    for (int k4 = 0; k4 < 4; ++k4) xo[k4] = xrow + ((2 * k4 + h) ^ xkey) * 16;
    v4u bA[2][2], aA[2][2], bB[2][2], aB[2][2];
    auto rd = [&](v4u (&bf)[2][2], v4u (&af)[2][2], const LAS char* sb, int k2) {
#pragma unroll
        for (int u = 0; u < 2; ++u) {
#pragma unroll
            for (int cx = 0; cx < 2; ++cx) { const LAS char* ap = sb + ((woff ^ (cx << 6)) + (2 * k2 + u) * 4096);
                const v2u lo = ldstr(ap), hi = ldstr(ap + 1024); af[u][cx] = v4u{lo.x, lo.y, hi.x, hi.y}; }
#pragma unroll
            for (int ty = 0; ty < 2; ++ty) bf[u][ty] = lds128(sb + xo[2 * k2 + u] + ty * 4096);
        }
    };
    auto mm = [&](const v4u (&bf)[2][2], const v4u (&af)[2][2]) {
#pragma unroll
        for (int u = 0; u < 2; ++u)
#pragma unroll
            for (int cx = 0; cx < 2; ++cx)
#pragma unroll
                for (int ty = 0; ty < 2; ++ty) acc[cx][ty] = mfma32(af[u][cx], bf[u][ty], acc[cx][ty]);
    };
    int dst[2][2];
#pragma unroll
    for (int cx = 0; cx < 2; ++cx)
#pragma unroll
        for (int m = 0; m < 2; ++m) { const int n8 = 128 * ct + 64 * ch + 32 * cx + 16 * m + 8 * h, ja = n8 / 72;
            dst[cx][m] = n8 < N3 ? (int)OFF_QKV + ja * (int)QKV_G + (128 * rt + 64 * th + l31) * 144 + (n8 - 72 * ja) * 2 : 0x7fff0000; }
    auto half = [&](v4u (&bn)[2][2], v4u (&an)[2][2], const LAS char* nb, int k2, const v4u (&bc)[2][2], const v4u (&ac)[2][2]) {
        SB();
        rd(bn, an, nb, k2);
        mm(bc, ac);
#pragma unroll
        for (int e = 0; e < 4; ++e) { __builtin_amdgcn_sched_group_barrier(0x008, 1, 0); __builtin_amdgcn_sched_group_barrier(0x100, 2, 0); }
#pragma unroll
        for (int e = 0; e < 4; ++e) { __builtin_amdgcn_sched_group_barrier(0x008, 1, 0); __builtin_amdgcn_sched_group_barrier(0x100, 1, 0); }
        SB();
    };
#define K1_SYNC() asm volatile("s_waitcnt lgkmcnt(12)\n\ts_barrier" ::: "memory")
    BAR();
    TS(1);
    unsigned pf[2];
#pragma unroll
    for (int m = 0; m < 2; ++m) {
        const int lx = (idx >> 2) * 55 + tid, lw2 = (idx & 3) * 96 + (tid - 55), wo = (192 + 192 * m + (lw2 >> 1)) * (N3 * 2) + 256 * ct + (lw2 & 1) * 128;
        const char* p = tid < 55 ? (const char*)a.x + (size_t)(128 * rt + (lx < 384 ? lx / 3 : 0)) * (D * 2) + (3 + 3 * m + lx % 3) * 128
                                 : (const char*)a.wqkv + ((tid < 151 && wo < D * N3 * 2) ? wo : 0);
        asm volatile("global_load_dword %0, %1, off" : "=v"(pf[m]) : "v"(p));
    }
    __builtin_amdgcn_s_waitcnt(0xC07F);
    rd(bA, aA, smem, 0);
    half(bB, aB, smem, 1, bA, aA);
    BAR();
    half(bA, aA, smem + K1_STAGE, 0, bB, aB);
    K1_SYNC();
#pragma nounroll
    for (int s = 1; s < 7; ++s) {
        half(bB, aB, smem + (s & 3) * K1_STAGE, 1, bA, aA);
        half(bA, aA, smem + ((s + 1) & 3) * K1_STAGE, 0, bB, aB);
        K1_SYNC();
    }
    half(bB, aB, smem + 3 * K1_STAGE, 1, bA, aA);
    half(bA, aA, smem, 0, bB, aB);
    half(bB, aB, smem, 1, bA, aA);
    SB(); mm(bB, aB); SB();
    TS(4);
    asm volatile("s_waitcnt vmcnt(0)" :: "v"(pf[0]), "v"(pf[1]));
    LBAR();
    const LAS float* tabc = (const LAS float*)(smem + K1_TAB) + 256;
    const LAS float* tabx = (const LAS float*)(smem + K1_TAB);
    TS(5);
    const rsrc_t rws = mkrsrc(a.ws, (unsigned)OFF_WS);
#pragma unroll
    for (int ty = 0; ty < 2; ++ty) {
        const int t = 64 * th + 32 * ty + l31;
        const float mean = tabx[t * 2], rstd = tabx[t * 2 + 1];
#pragma unroll
        for (int cx = 0; cx < 2; ++cx) {
            const float rs = rstd * (128 * ct + 64 * ch + 32 * cx < D ? QSCALE : 1.0f);
            unsigned pk[8];
#pragma unroll
            for (int qd = 0; qd < 4; ++qd) { const f32x4 c = *(const LAS f32x4*)(tabc + 64 * ch + 32 * cx + 8 * qd + 4 * h);
                pk[2 * qd] = pk2(rs * __builtin_fmaf(-mean, c[0], acc[cx][ty][4 * qd]), rs * __builtin_fmaf(-mean, c[1], acc[cx][ty][4 * qd + 1]));
                pk[2 * qd + 1] = pk2(rs * __builtin_fmaf(-mean, c[2], acc[cx][ty][4 * qd + 2]), rs * __builtin_fmaf(-mean, c[3], acc[cx][ty][4 * qd + 3])); }
#pragma unroll
            for (int m = 0; m < 2; ++m) {
                const auto s0 = __builtin_amdgcn_permlane32_swap(pk[4 * m], pk[4 * m + 2], false, false), s1 = __builtin_amdgcn_permlane32_swap(pk[4 * m + 1], pk[4 * m + 3], false, false);
                bst128<QST>(v4u{(unsigned)s0[0], (unsigned)s1[0], (unsigned)s0[1], (unsigned)s1[1]}, rws, dst[cx][m], ty * 32 * 144); }
        }
    }
    STAMP(a.ws, 2);
    TS(6); TSW(7);
    TSDUMP(a.ws, 0);
}

struct K2Args { char* ws; const void* x; const void* wo; const void* w1; const void* w2; const void* b1; const void* b2; void* out; };
constexpr int KV_HEAD = 320 * 40;
constexpr int K2_K = 0, K2_V = 4 * KV_HEAD, K2_O = 8 * KV_HEAD, K2_SCR = K2_O + 64 * 144;
constexpr int K4_X = 0, K4_H = 73728, K4_ST = 147456, K4_CS = K4_ST + 512, K4_CNT = K4_CS + 2304, K4_OWN = K4_CNT + 256, K2_LDS = K4_OWN + 8192;
static_assert(K2_SCR + 8 * 4096 <= K4_ST, "LDS map");

DI void flag_add(char* ws, int f) { __hip_atomic_fetch_add((gu32*)(ws + OFF_FLAG + f * 256), 1u, __ATOMIC_RELAXED, __HIP_MEMORY_SCOPE_AGENT); }
DI void flag_wait(char* ws, int f, unsigned n) {
    gu32* p = (gu32*)(ws + OFF_FLAG + f * 256);
    for (unsigned spins = 0; __hip_atomic_load(p, __ATOMIC_RELAXED, __HIP_MEMORY_SCOPE_AGENT) < n && spins < (1u << 20); ++spins) __builtin_amdgcn_s_sleep(1);
    __builtin_amdgcn_fence(__ATOMIC_ACQUIRE, "wavefront");
}
DI void flag_wait_all(char* ws, int f, unsigned n, int lane) {
    gu32* p = (gu32*)(ws + OFF_FLAG + (f + (lane & 31)) * 256);
    for (unsigned spins = 0; !__all(__hip_atomic_load(p, __ATOMIC_RELAXED, __HIP_MEMORY_SCOPE_AGENT) >= n) && spins < (1u << 20); ++spins) __builtin_amdgcn_s_sleep(1);
    __builtin_amdgcn_fence(__ATOMIC_ACQUIRE, "wavefront");
}
DI void tile_in(const char* src, LAS char* smem, int tid) {
    const rsrc_t rs = mkrsrc(src, 4 * (unsigned)TT_FR);
    v4u t[9];
#pragma unroll
    for (int m = 0; m < 9; ++m) t[m] = bld128_sc1(rs, tid * 16, m * 8192);
#pragma unroll
    for (int m = 0; m < 9; ++m) *(LAS v4u*)(smem + tid * 16 + m * 8192) = t[m];
}

template <bool EDGE> DI void attn_core(LAS char* smem, const v4u q0, const v4u q1, int r, int w, int lane, f32x16& oacc, const Unit& ua, rsrc_t rws) {
    const int hl = w & 3, qh = w >> 2, l31 = lane & 31, hh = lane >> 5;
    const int tmin = EDGE ? ((256 - 64 * r - 32 * qh) >> 5) : 0;
    const float ninf = -__builtin_inff();
    const LAS char* kp = smem + K2_K + (hl * 320 + 32 * qh + l31) * 40;
    v4u ka[9], kb[9];
#pragma unroll
    for (int t = 0; t < 9; ++t) { const v2u a0 = lds64v(kp + t * 1280 + 16 * hh), a1 = lds64v(kp + t * 1280 + 16 * hh + 8);
        ka[t] = v4u{a0.x, a0.y, a1.x, a1.y};
        const v2u c0 = lds64v(kp + t * 1280 + 32), c1 = lds64v(kp + t * 1280 + 24);
        kb[t] = v4u{c0.x, c0.y, c1.x, c1.y}; }
    auto score = [&](int t, const f32x16& c) { return mfma32(kb[t], q1, mfma32(ka[t], q0, c)); };
    bool le[16];
#pragma unroll
    for (int rg = 0; rg < 16; ++rg) le[rg] = (rg & 3) + 8 * (rg >> 2) + 4 * hh <= l31;
    auto merged = [&](const f32x16& c) { const f32x16 s8 = score(8, c); f32x16 s0;
        if (EDGE) { s0 = s8;
#pragma unroll
            for (int rg = 0; rg < 16; ++rg) s0[rg] = ninf; } else s0 = score(0, c);
        f32x16 m;
#pragma unroll
        for (int rg = 0; rg < 16; ++rg) m[rg] = le[rg] ? s8[rg] : s0[rg];
        return m; };
    const f32x16 zero = {};
    float mx;
    { const f32x16 m = merged(zero); mx = __builtin_fmaxf(m[0], m[1]);
#pragma unroll
      for (int rg = 2; rg < 16; rg += 2) mx = __builtin_fmaxf(__builtin_fmaxf(mx, m[rg]), m[rg + 1]); }
#pragma unroll
    for (int t = 1; t < 8; ++t) if (!EDGE || t >= tmin) { const f32x16 s = score(t, zero);
#pragma unroll
        for (int rg = 0; rg < 16; rg += 2) mx = __builtin_fmaxf(__builtin_fmaxf(mx, s[rg]), s[rg + 1]); }
    mx = max32(mx);
    if (ua.live) { WAITV(0); if (ua.w2) unit_store_w2(ua, smem + K2_SCR + w * 4096, rws, lane); else unit_store(ua, smem + K2_SCR + w * 4096, rws, lane); }
    f32x16 cm;
#pragma unroll
    for (int rg = 0; rg < 16; ++rg) cm[rg] = -mx;
    const LAS char* vp = smem + K2_V + (hl * 320 + 32 * qh + 4 * hh + ((lane & 15) >> 2)) * 40 + (((lane >> 4) & 1) ? 32 : 8 * (lane & 3));
    auto pv = [&](int t, const unsigned (&pw)[8]) {
#pragma unroll
        for (int s = 0; s < 2; ++s) {
            const v2u lo = ldstr(vp + (t * 32 + 16 * s) * 40), hi = ldstr(vp + (t * 32 + 16 * s + 8) * 40);
            oacc = mfma32(v4u{lo.x, lo.y, hi.x, hi.y}, v4u{pw[4 * s], pw[4 * s + 1], pw[4 * s + 2], pw[4 * s + 3]}, oacc);
        } };
    oacc = zero;
    { const f32x16 m = merged(cm); unsigned pw[8], p8[8], p0[8];
#pragma unroll
      for (int e = 0; e < 8; ++e) { pw[e] = pk2(__builtin_amdgcn_exp2f(m[2 * e]), __builtin_amdgcn_exp2f(m[2 * e + 1]));
          const unsigned mk = (le[2 * e] ? 0xffffu : 0u) | (le[2 * e + 1] ? 0xffff0000u : 0u); p8[e] = pw[e] & mk; p0[e] = pw[e] & ~mk; }
      pv(8, p8); if (!EDGE) pv(0, p0); }
#pragma unroll
    for (int t = 1; t < 8; ++t) if (!EDGE || t >= tmin) { const f32x16 s = score(t, cm); unsigned pw[8];
#pragma unroll
        for (int e = 0; e < 8; ++e) pw[e] = pk2(__builtin_amdgcn_exp2f(s[2 * e]), __builtin_amdgcn_exp2f(s[2 * e + 1]));
        pv(t, pw); }
}

#define RD 24

template <int T, int OFS, class Side>
DI void ffn_pass(f32x4 (&acc)[3][4], v4u (&ring)[RD], rsrc_t rw, int pos, const LAS char* bp, int lane, Side side) {
    v4u bf[4];
#pragma unroll
    for (int tt = 0; tt < 4; ++tt) bf[tt] = lds128(bp + tt * 18432);
#pragma unroll
    for (int t = 0; t < T; ++t)
#pragma unroll
        for (int tt = 0; tt < 4; ++tt) acc[t][tt] = f32x4{0.f, 0.f, 0.f, 0.f};
#pragma unroll
    for (int u = 0; u < 18; ++u) {
        SB();
        v4u bn[4];
        if (u + 1 < 18) {
#pragma unroll
            for (int tt = 0; tt < 4; ++tt) bn[tt] = lds128(bp + tt * 18432 + (u + 1) * 1024);
        }
#pragma unroll
        for (int t = 0; t < T; ++t) {
            const int n = u * T + t, sl = (n + OFS) % RD;
#pragma unroll
            for (int tt = 0; tt < 4; ++tt) acc[t][tt] = mfma16(ring[sl], bf[tt], acc[t][tt]);
            ring[sl] = bld128_sc1(rw, lane * 16 + (n & 3) * 1024, pos + (n >> 2) * 4096);
        }
        side(u);
#pragma unroll
        for (int t = 0; t < T; ++t) {
            if (T == 1) {
#pragma unroll
                for (int e = 0; e < 4; ++e) { __builtin_amdgcn_sched_group_barrier(0x008, 1, 0); __builtin_amdgcn_sched_group_barrier(0x100, 1, 0); }
            } else if (T == 2) {
#pragma unroll
                for (int e = 0; e < 2; ++e) { __builtin_amdgcn_sched_group_barrier(0x008, 2, 0); __builtin_amdgcn_sched_group_barrier(0x100, 1, 0); }
            } else { __builtin_amdgcn_sched_group_barrier(0x008, 4, 0); if (t < 4) __builtin_amdgcn_sched_group_barrier(0x100, 1, 0); }
            __builtin_amdgcn_sched_group_barrier(0x020, 1, 0);
        }
        SB();
        if (u + 1 < 18) {
#pragma unroll
            for (int tt = 0; tt < 4; ++tt) bf[tt] = bn[tt];
        }
    }
#pragma unroll
    for (int t = 0; t < T; ++t)
#pragma unroll
        for (int tt = 0; tt < 4; ++tt) PIN(acc[t][tt]);
    asm volatile("s_nop 7" ::: "memory");
}

template <bool A> DI void ffn_body(const K2Args& a, LAS char* smem, int r, int j, int w, int lane
) {
    constexpr int NT = A ? 5 : 4;
    const int tid = threadIdx.x, g = lane >> 4, i = lane & 15, s = w & 3;
    char* ws = a.ws;
    const rsrc_t rws = mkrsrc(ws, (unsigned)WS_END);
    v4u ring[RD];
    {
        if (tid == 448) { flag_add(ws, FL_O + r); flag_wait(ws, FL_O + r, 8); }
        const int dt = A ? 4 * j + w : 32 + (j >> 1), tt0 = A ? 0 : 2 * (j & 1);
        const rsrc_t rw = mkrsrc(ws + OFF_WO, 36 * 18 * 1024), rx = mkrsrc(a.x, S * D * 2);
        v4u af[18]; v2u xr[4];
        if (A || w == 4) {
#pragma unroll
            for (int ks = 0; ks < 18; ++ks) af[ks] = bld128(rw, lane * 16 + (ks & 3) * 1024, dt * 18432 + (ks >> 2) * 4096);
#pragma unroll
            for (int u = 0; u < 4; ++u) xr[u] = bld64(rx, (64 * r + 16 * ((tt0 + u) & 3) + i) * (D * 2) + (16 * dt + 4 * g) * 2, 0);
        }
        BAR();
        TS(7);
        STAMP(ws, 6);
        tile_in(ws + OFF_O + 4 * r * TT_FR, smem, tid);
        LBAR();
        TS(8);
        const rsrc_t rf = mkrsrc(ws + OFF_WS + j * WS_J + ffn_stream_base(w), (A ? 180 : 144) * 1024);
        auto fill = [&](int n) { ring[n] = bld128_sc1(rf, lane * 16 + (n & 3) * 1024, (n >> 2) * 4096); };
        if (!(A || w == 4)) {
#pragma unroll
            for (int n = 0; n < RD / 2; ++n) fill(n);
            if (w == 7) flag_wait_all(ws, FL_O, 8, lane);
        }
        if (A || w == 4) {
            f32x4 acc[4];
#pragma unroll
            for (int u = 0; u < 4; ++u) acc[u] = f32x4{0.f, 0.f, 0.f, 0.f};
            const LAS char* bp = smem + lane * 16;
            int to[4];
#pragma unroll
            for (int u = 0; u < 4; ++u) to[u] = ((tt0 + u) & 3) * 18432;
            v4u bf[2][4];
#pragma unroll
            for (int u = 0; u < 4; ++u) bf[0][u] = lds128(bp + to[u]);
#pragma unroll
            for (int ks = 0; ks < 18; ++ks) {
                SB();
                if (ks < 17) {
#pragma unroll
                    for (int u = 0; u < 4; ++u) bf[(ks + 1) & 1][u] = lds128(bp + (ks + 1) * 1024 + to[u]); }
#pragma unroll
                for (int u = 0; u < 4; ++u) acc[u] = mfma16(af[ks], bf[ks & 1][u], acc[u]);
                if (ks < RD / 2) fill(ks);
#pragma unroll
                for (int u = 0; u < 4; ++u) { __builtin_amdgcn_sched_group_barrier(0x008, 1, 0); __builtin_amdgcn_sched_group_barrier(0x100, 1, 0); }
                SB();
            }
#pragma unroll
            for (int u = 0; u < 4; ++u) if (A || u < 2) { const int tt = (tt0 + u) & 3;
                const v2u o = {pk2(acc[u][0] + blo(xr[u].x), acc[u][1] + bhi(xr[u].x)), pk2(acc[u][2] + blo(xr[u].y), acc[u][3] + bhi(xr[u].y))};
                __builtin_amdgcn_raw_buffer_store_b64(o, rws, (int)OFF_H + (4 * r + tt) * (int)TT_FR + (dt >> 1) * 1024 + ((2 * (dt & 1) + (g >> 1)) * 16 + i) * 16 + (g & 1) * 8, 0, 16); }
        }
        TS(9);
        SB();
        if (w != 7) {
#pragma unroll
            for (int n = RD / 2; n < RD; ++n) fill(n); }
        SB();
        asm volatile("s_waitcnt vmcnt(%0) lgkmcnt(0)\n\ts_barrier" :: "n"(RD - RD / 2) : "memory");
        STAMP(ws, 7);
        TS(10);
        if (w == 7) {
            if (lane == 0) { flag_add(ws, FL_H + r); flag_wait(ws, FL_H + r, 8); }
            SB();
#pragma unroll
            for (int n = RD / 2; n < RD; ++n) fill(n); }
    }
    const rsrc_t rw = mkrsrc(ws + OFF_WS + j * WS_J + ffn_stream_base(w), (A ? 180 : 144) * 1024);
    BAR();
    TS(14);
    STAMP(ws, 8);
    tile_in(ws + OFF_H + 4 * r * TT_FR, smem + K4_X, tid);
    LBAR();
    TS(15);
    v2u bb[NT];
#pragma unroll
    for (int t = 0; t < NT; ++t) bb[t] = *(const GAS v2u*)((const char*)a.b1 + (576 * j + 16 * (4 * ((A ? 0 : 5) + t) + s) + 4 * g) * 2);
    unsigned pc[3][18];
    const rsrc_t rc = mkrsrc(ws + OFF_CSP, 18 * FF * 4);
    if (!A) {
#pragma unroll
        for (int m = 0; m < 3; ++m) if (m < 2 || w == 4) {
#pragma unroll
            for (int ks = 0; ks < 18; ++ks) pc[m][ks] = __builtin_amdgcn_raw_buffer_load_b32(rc, (576 * j + 256 * m + tid - 256) * 4, ks * FF * 4, 16); } }
    SB();
    LAS float* st = (LAS float*)(smem + K4_ST);
    if (!A) {
        f32x4 s1 = {0.f, 0.f, 0.f, 0.f}, s2 = s1;
        const v4u ones = {0x3f803f80u, 0x3f803f80u, 0x3f803f80u, 0x3f803f80u};
#pragma unroll
        for (int kh = 0; kh < 2; ++kh) { v4u v[9];
#pragma unroll
            for (int k = 0; k < 9; ++k) v[k] = lds128(smem + K4_X + (s * 18 + 9 * kh + k) * 1024 + lane * 16);
#pragma unroll
            for (int k = 0; k < 9; ++k) { s1 = mfma16(ones, v[k], s1); s2 = mfma16(v[k], v[k], s2); } }
        const float sq = (i & 2) ? ((i & 1) ? s2[3] : s2[2]) : ((i & 1) ? s2[1] : s2[0]);
        const float mean = s1[0] * (1.0f / D), var = __builtin_fmaxf(__builtin_fmaf(-mean, mean, sq * (1.0f / D)), 0.f);
        if (g == (i >> 2)) *(LAS f32x2*)(st + (16 * s + i) * 2) = f32x2{mean, __builtin_amdgcn_rsqf(var + LN_EPS)};
    }
    if (!A) {
#pragma unroll
        for (int m = 0; m < 3; ++m) if (m < 2 || w == 4) { float sum = 0.f;
#pragma unroll
            for (int ks = 0; ks < 18; ++ks) sum += __builtin_bit_cast(float, pc[m][ks]);
            *(LAS float*)(smem + K4_CS + (256 * m + tid - 256) * 4) = sum; }
        asm volatile("s_waitcnt lgkmcnt(0)" ::: "memory");
        if (lane == 0) __hip_atomic_fetch_add((LAS unsigned*)(smem + K4_CNT), 1u, __ATOMIC_RELAXED, __HIP_MEMORY_SCOPE_WORKGROUP); }
    if (A) __builtin_amdgcn_s_setprio(1);
    STAMP(ws, 9);
    f32x4 acc[3][4];
    auto none = [](int) {};
    auto act = [&](int slot, int t) { const int ft = 4 * ((A ? 0 : 5) + t) + s;
        const float b0 = blo(bb[t].x), b1 = bhi(bb[t].x), b2 = blo(bb[t].y), b3 = bhi(bb[t].y);
        const f32x4 cs = *(const LAS f32x4*)(smem + K4_CS + (16 * ft + 4 * g) * 4);
#pragma unroll
        for (int tt = 0; tt < 4; ++tt) { const f32x2 mr = *(const LAS f32x2*)(st + (16 * tt + i) * 2);
            unsigned o01, o23; ln_gelu4(acc[slot][tt], cs, b0, b1, b2, b3, -mr[0], mr[1], o01, o23);
            *(LAS v2u*)(smem + K4_H + (tt * 18 + (ft >> 1)) * 1024 + lane * 16 + (ft & 1) * 8) = v2u{o01, o23}; } };
    constexpr int T0 = A ? 3 : T0B;
    ffn_pass<T0, 0>(acc, ring, rw, RD * 1024, smem + K4_X + lane * 16, lane, none);
    TS(16);
    while (*(volatile LAS unsigned*)(smem + K4_CNT) < 4u) { }
    asm volatile("" ::: "memory");
#pragma unroll
    for (int t = 0; t < T0; ++t) act(t, t);
    TS(11);
    ffn_pass<NT - T0, (18 * T0) % RD>(acc, ring, rw, (18 * T0 + RD) * 1024, smem + K4_X + lane * 16, lane, none);
    TS(23);
#pragma unroll
    for (int t = T0; t < NT; ++t) act(t - T0, t);
    TS(17);
    STAMP(ws, 10);
    LBAR();
    TS(18);
    STAMP(ws, 11);
    constexpr int G2 = A ? 90 : 72;
    const LAS char* hp = smem + K4_H + lane * 16;
    const rsrc_t rp = mkrsrc(ws + OFF_P + (size_t)r * NSL * P_SRC, NSL * (unsigned)P_SRC);
    v4u outq[4];
    auto pack = [&]() {
#pragma unroll
        for (int t = 0; t < 2; ++t)
#pragma unroll
            for (int tp = 0; tp < 2; ++tp) {
                outq[t * 2 + tp] = v4u{pk2(acc[t][2 * tp][0], acc[t][2 * tp][1]), pk2(acc[t][2 * tp][2], acc[t][2 * tp][3]), pk2(acc[t][2 * tp + 1][0], acc[t][2 * tp + 1][1]), pk2(acc[t][2 * tp + 1][2], acc[t][2 * tp + 1][3])};
                PIN(outq[t * 2 + tp]); } };
    auto put = [&](int k, int ps) { const int gi = (A ? 0 : 4) + 2 * ps + (k >> 1), dt = 4 * (gi < j ? gi : gi + 1) + s;
        bst128<16>(outq[k], rp, lane * 16, j * (int)P_SRC + (dt * 2 + (k & 1)) * 1024); };
    ffn_pass<2, G2 % RD>(acc, ring, rw, (G2 + RD) * 1024, hp, lane, none);
    pack();
    ffn_pass<2, (G2 + 36) % RD>(acc, ring, rw, (G2 + 36 + RD) * 1024, hp, lane, [&](int u) { if (u >= 1 && u <= 7 && (u & 1)) put(u >> 1, 0); });
    pack();
    if (!A) {
#pragma unroll
        for (int k = 0; k < 4; ++k) put(k, 1); }
    TS(19);
    if (A) { ffn_pass<1, (G2 + 72) % RD>(acc, ring, rw, (G2 + 72 + RD) * 1024, hp, lane, [&](int u) { if (u <= 6 && !(u & 1)) put(u >> 1, 1); });
#pragma unroll
        for (int u = 0; u < 2; ++u) *(LAS f32x4*)(smem + K4_OWN + (s * 2 + u) * 1024 + lane * 16) = acc[0][2 + u]; }
    STAMP(ws, 12);
    asm volatile("s_waitcnt vmcnt(0) lgkmcnt(0)\n\ts_barrier" ::: "memory");
    TS(20);
    if (tid == 448) { flag_add(ws, FL_P + r); flag_wait(ws, FL_P + r, 8); }
    BAR();
    STAMP(ws, 13);
    TS(21);
    {
        const int dt = 4 * j + s, tp = A ? 0 : 1, dx = 32 + (j >> 1), tpx = j & 1;
        const bool extra = !A && s == (j & 3);
        v4u pv[8], px[8];
#pragma unroll
        for (int src = 0; src < 8; ++src) pv[src] = bld128_sc1(rp, lane * 16, src != j ? src * (int)P_SRC + (dt * 2 + tp) * 1024 : 0x7fff0000);
        if (extra) {
#pragma unroll
            for (int src = 0; src < 8; ++src) px[src] = bld128_sc1(rp, lane * 16, src * (int)P_SRC + (dx * 2 + tpx) * 1024); }
        const v2u b2v = *(const GAS v2u*)((const char*)a.b2 + (16 * dt + 4 * g) * 2), b2x = *(const GAS v2u*)((const char*)a.b2 + (16 * dx + 4 * g) * 2);
        SB();
        auto fin = [&](const v4u (&p)[8], int d_t, int t_p, v2u b2, bool own) {
#pragma unroll
            for (int u = 0; u < 2; ++u) { const int tt = 2 * t_p + u;
                const v2u hv = lds64(smem + K4_X + (tt * 18 + (d_t >> 1)) * 1024 + ((2 * (d_t & 1) + (g >> 1)) * 16 + i) * 16 + (g & 1) * 8);
                f32x4 o = {blo(hv.x) + blo(b2.x), bhi(hv.x) + bhi(b2.x), blo(hv.y) + blo(b2.y), bhi(hv.y) + bhi(b2.y)};
                if (own) { if (A) o += acc[0][u]; else o += *(const LAS f32x4*)(smem + K4_OWN + (s * 2 + u) * 1024 + lane * 16); }
#pragma unroll
                for (int src = 0; src < 8; ++src) o += f32x4{blo(p[src][2 * u]), bhi(p[src][2 * u]), blo(p[src][2 * u + 1]), bhi(p[src][2 * u + 1])};
                *(GAS v2u*)((char*)a.out + (size_t)(64 * r + 16 * tt + i) * (D * 2) + (16 * d_t + 4 * g) * 2) = v2u{pk2(o[0], o[1]), pk2(o[2], o[3])};
            } };
        fin(pv, dt, tp, b2v, true);
        if (extra) fin(px, dx, tpx, b2x, false);
    }
    STAMP(ws, 14);
    TSW(22);
    TSDUMP(ws, 1);
}

__global__ __launch_bounds__(512, 2) void k2_rest(K2Args a) {
    extern __shared__ __attribute__((aligned(16))) char smem_[];
    LAS char* smem = (LAS char*)smem_;
    const unsigned lds0 = (unsigned)(size_t)smem_;
    char* ws = a.ws;
    const int tid = threadIdx.x, lane = tid & 63, w = __builtin_amdgcn_readfirstlane(tid >> 6), b = blockIdx.x;
    const int j = wg_j(b), r = wg_r(b);
    STAMP(ws, 3);
    TSDECL; TS(0);
    const rsrc_t rws = mkrsrc(ws, (unsigned)WS_END);
    {
        const int hl = w & 3, qh = w >> 2, l31 = lane & 31, hh = lane >> 5;
        const rsrc_t rk = mkrsrc(ws + OFF_QKV + (8 + j) * QKV_G, S * 144), rv = mkrsrc(ws + OFF_QKV + (16 + j) * QKV_G, S * 144);
        const int tok0 = tid / 9, mm = tid - 9 * tok0;
        v4u kk[6], vv[6];
#pragma unroll
        for (int m = 0; m < 6; ++m) { const int tok = tok0 + 56 * m, o_ = (64 * r - 256) * 144 + tid * 16 + m * 56 * 144;
            const int off = (tid < 504 && tok < 320 && o_ >= 0) ? o_ : 0x7ffffff0;
            kk[m] = bld128(rk, off, 0); vv[m] = bld128(rv, off, 0); }
        const char* qp = ws + OFF_QKV + j * QKV_G + (size_t)(64 * r + 32 * qh + l31) * 144 + 36 * hl;
        v4u q0, q1;
        { const GAS unsigned* qq = (const GAS unsigned*)(qp + 16 * hh); q0 = v4u{qq[0], qq[1], qq[2], qq[3]};
          const unsigned t = *(const GAS unsigned*)(qp + 32); q1 = v4u{hh == 0 ? t : 0u, 0u, 0u, 0u}; }
        const Unit ua = unit_decode(a.w1, a.w2, a.wo, w < 6 ? 1024 + b * 6 + w : (w == 6 && b < 194) ? 1024 + 1536 + b : NUNIT);
        for (int n = tid; n < 1280; n += 512) { *(LAS unsigned*)(smem + K2_K + n * 40 + 36) = 0u; *(LAS unsigned*)(smem + K2_V + n * 40 + 36) = 0x3f80u; }
        if (tid == 0) *(LAS unsigned*)(smem + K4_CNT) = 0u;
        int lo_[4];
#pragma unroll
        for (int e = 0; e < 4; ++e) { const int dw = 4 * mm + e, h = dw / 9; lo_[e] = (h * 320 + tok0) * 40 + (dw - 9 * h) * 4; }
        TS(1);
        if (tid < 504) {
#pragma unroll
            for (int m = 0; m < 6; ++m) if (m < 5 || tok0 + 280 < 320) {
#pragma unroll
                for (int e = 0; e < 4; ++e) { *(LAS unsigned*)(smem + K2_K + lo_[e] + m * 56 * 40) = kk[m][e]; *(LAS unsigned*)(smem + K2_V + lo_[e] + m * 56 * 40) = vv[m][e]; } }
        }
        if (ua.live) unit_load(ua, lds0 + K2_SCR + w * 4096, lane);
        TS(2);
        LBAR();
        TS(3);
        STAMP(ws, 4);
        f32x16 oacc;
        if (r < 4) attn_core<true>(smem, q0, q1, r, w, lane, oacc, ua, rws);
        else attn_core<false>(smem, q0, q1, r, w, lane, oacc, ua, rws);
        TS(4);
        const float inv = __builtin_amdgcn_rcpf(lo32(oacc[10]));
        LAS char* op = smem + K2_O + (32 * qh + l31) * 144 + 36 * hl + 8 * hh;
        *(LAS unsigned*)(op) = pk2(oacc[0] * inv, oacc[1] * inv); *(LAS unsigned*)(op + 4) = pk2(oacc[2] * inv, oacc[3] * inv);
        *(LAS unsigned*)(op + 16) = pk2(oacc[4] * inv, oacc[5] * inv); *(LAS unsigned*)(op + 20) = pk2(oacc[6] * inv, oacc[7] * inv);
        if (hh == 0) *(LAS unsigned*)(op + 32) = pk2(oacc[8] * inv, oacc[9] * inv);
        LBAR();
        TS(5);
#pragma unroll
        for (int m2 = 0; m2 < 2; ++m2) { const int n = tid + 512 * m2;
            if (n < 576) { const int i = n & 15, tt = (n >> 4) & 3, m = n >> 6;
                const v4u v = lds128(smem + K2_O + (16 * tt + i) * 144 + 16 * m);
                const int n8 = 72 * j + 8 * m, ks = n8 >> 5, g = (n8 & 31) >> 3;
                bst128<16>(v, rws, (int)OFF_O + (4 * r + tt) * (int)TT_FR + ks * 1024 + (g * 16 + i) * 16, 0); } }
        asm volatile("s_waitcnt vmcnt(0) lgkmcnt(0)\n\ts_barrier" ::: "memory");
        STAMP(ws, 5);
        TS(6);
    }
    if (w < 4) ffn_body<true>(a, smem, r, j, w, lane); else ffn_body<false>(a, smem, r, j, w, lane);
}

extern "C" void kernel_launch(void* const* d_in, const int* in_sizes, int n_in, void* d_out, int out_size, void* d_ws, size_t ws_size, hipStream_t stream) {
    (void)in_sizes; (void)n_in; (void)out_size; (void)ws_size;
    char* ws = (char*)d_ws;
    (void)hipFuncSetAttribute((const void*)k1_qkv, hipFuncAttributeMaxDynamicSharedMemorySize, K1_LDS);
    (void)hipFuncSetAttribute((const void*)k2_rest, hipFuncAttributeMaxDynamicSharedMemorySize, K2_LDS);
    K1Args a1{d_in[0], d_in[1], d_in[2], d_in[4], d_in[6], ws};
    k1_qkv<<<256, 512, K1_LDS, stream>>>(a1);
    K2Args a2{ws, d_in[0], d_in[2], d_in[4], d_in[6], d_in[5], d_in[7], d_out};
    k2_rest<<<256, 512, K2_LDS, stream>>>(a2);
}
```

```cpp
#include <hip/hip_runtime.h>
#include <hip/hip_bf16.h>
#include <stdint.h>

#define GAS __attribute__((address_space(1)))
#define LAS __attribute__((address_space(3)))
#define DI __device__ __forceinline__
typedef unsigned v4u __attribute__((ext_vector_type(4)));
typedef unsigned v2u __attribute__((ext_vector_type(2)));
typedef float f32x4 __attribute__((ext_vector_type(4)));
typedef float f32x16 __attribute__((ext_vector_type(16)));
typedef short bf16x8 __attribute__((ext_vector_type(8)));
typedef __bf16 bf2 __attribute__((ext_vector_type(2)));
typedef GAS unsigned gu32;
typedef __amdgpu_buffer_rsrc_t rsrc_t;

constexpr int S = 2048, D = 576, FF = 4608, N3 = 3 * D;
constexpr int NR = 32;
constexpr float LN_EPS = 1e-5f;
constexpr float QSCALE = 0.23570226039551584f * 1.4426950408889634f;

constexpr size_t FRAG = 1024;
constexpr size_t OFF_FLAG = 0;
constexpr int FL_O = 0, FL_H = 32, FL_P = 64, FL_N = 96;
constexpr size_t OFF_QKV = 65536;
constexpr size_t QKV_G = (size_t)S * 144;
constexpr int NSL = 8;
constexpr size_t OFF_WS = OFF_QKV + 24 * QKV_G;
constexpr size_t WS_J = (size_t)1296 * FRAG;
constexpr size_t OFF_WO = OFF_WS + NSL * WS_J;
constexpr size_t OFF_O = OFF_WO + (size_t)36 * 18 * FRAG;
constexpr size_t TT_FR = (size_t)18 * FRAG;
constexpr size_t OFF_H = OFF_O + 128 * TT_FR;
constexpr size_t OFF_P = OFF_H + 128 * TT_FR;
constexpr size_t P_SRC = (size_t)36 * 2 * FRAG;
constexpr size_t OFF_CSP = OFF_P + (size_t)NR * NSL * P_SRC;
constexpr size_t WS_END = OFF_CSP + (size_t)18 * FF * 4;

__host__ __device__ constexpr int ffn_stream_base(int wv) { return (wv < 4 ? wv * 180 : 720 + (wv - 4) * 144) * 1024; }
#define T0B 2
__host__ __device__ constexpr int ffn_pos1(bool a, int t, int ks) { const int t0 = a ? 3 : T0B, nt = a ? 5 : 4; return t < t0 ? ks * t0 + t : 18 * t0 + ks * (nt - t0) + (t - t0); }
__host__ __device__ constexpr int ffn_pos2(bool a, int t, int ks) { return (a ? 90 : 72) + (t < 4 ? (t >> 1) * 36 + ks * 2 + (t & 1) : 72 + ks); }
DI int wg_r(int b) { return 8 * ((b & 7) >> 1) + ((b >> 3) & 7); }
DI int wg_j(int b) { return 4 * (b & 1) + (b >> 6); }

DI f32x4 mfma16(v4u a, v4u b, f32x4 c) { return __builtin_amdgcn_mfma_f32_16x16x32_bf16(__builtin_bit_cast(bf16x8, a), __builtin_bit_cast(bf16x8, b), c, 0, 0, 0); }
DI f32x16 mfma32(v4u a, v4u b, f32x16 c) { return __builtin_amdgcn_mfma_f32_32x32x16_bf16(__builtin_bit_cast(bf16x8, a), __builtin_bit_cast(bf16x8, b), c, 0, 0, 0); }
DI unsigned pk2(float a, float b) { bf2 v = {(__bf16)a, (__bf16)b}; return __builtin_bit_cast(unsigned, v); }
DI float blo(unsigned u) { return __builtin_bit_cast(float, u << 16); }
DI float bhi(unsigned u) { return __builtin_bit_cast(float, u & 0xffff0000u); }
DI rsrc_t mkrsrc(const void* p, unsigned bytes) { return __builtin_amdgcn_make_buffer_rsrc((void*)p, 0, bytes, 0x00020000); }
DI v4u bld128(rsrc_t r, int voff, int soff) { return __builtin_amdgcn_raw_buffer_load_b128(r, voff, soff, 0); }
DI v4u bld128_sc1(rsrc_t r, int voff, int soff) { return __builtin_amdgcn_raw_buffer_load_b128(r, voff, soff, 16); }
DI v2u bld64(rsrc_t r, int voff, int soff) { return __builtin_amdgcn_raw_buffer_load_b64(r, voff, soff, 0); }
template <int AUX> DI void bst128(v4u v, rsrc_t r, int voff, int soff) { __builtin_amdgcn_raw_buffer_store_b128(v, r, voff, soff, AUX); asm volatile("s_nop 1" :: "v"(v)); }
DI v4u lds128(const LAS char* p) { return *(const LAS v4u*)p; }
DI v2u lds64(const LAS char* p) { return *(const LAS v2u*)p; }
DI v2u lds64v(const LAS char* p) { return *(const volatile LAS v2u*)p; }
typedef short v4i16_t __attribute__((ext_vector_type(4)));
DI v2u ldstr(const LAS char* p) { return __builtin_bit_cast(v2u, __builtin_amdgcn_ds_read_tr16_b64_v4i16((LAS v4i16_t*)p)); }
DI float dot2(unsigned a, float c) { return __builtin_amdgcn_fdot2_f32_bf16(__builtin_bit_cast(bf2, a), __builtin_bit_cast(bf2, 0x3f803f80u), c, false); }
DI void dma16(rsrc_t r, int voff, int soff, unsigned lds) {
    asm volatile("s_mov_b32 m0, %0\n\ts_nop 0\n\tbuffer_load_dwordx4 %1, %2, %3 offen lds" :: "s"(lds), "v"(voff), "s"(r), "s"(soff) : "memory");
}
#define WAITV(n) asm volatile("s_waitcnt vmcnt(" #n ")" ::: "memory")
#define BAR() asm volatile("s_barrier" ::: "memory")
#define LBAR() asm volatile("s_waitcnt lgkmcnt(0)\n\ts_barrier" ::: "memory")
#define SB() __builtin_amdgcn_sched_barrier(0)
#define PIN(x) asm volatile("" : "+v"(x))
DI float sum16(float v) { const unsigned u = __builtin_bit_cast(unsigned, v); const auto r = __builtin_amdgcn_permlane16_swap(u, u, false, false);
    return __builtin_bit_cast(float, (unsigned)r[0]) + __builtin_bit_cast(float, (unsigned)r[1]); }
DI float sum32(float v) { const unsigned u = __builtin_bit_cast(unsigned, v); const auto r = __builtin_amdgcn_permlane32_swap(u, u, false, false);
    return __builtin_bit_cast(float, (unsigned)r[0]) + __builtin_bit_cast(float, (unsigned)r[1]); }
DI float max32(float v) { const unsigned u = __builtin_bit_cast(unsigned, v); const auto r = __builtin_amdgcn_permlane32_swap(u, u, false, false);
    return __builtin_fmaxf(__builtin_bit_cast(float, (unsigned)r[0]), __builtin_bit_cast(float, (unsigned)r[1])); }
DI float lo32(float v) { const unsigned u = __builtin_bit_cast(unsigned, v); const auto r = __builtin_amdgcn_permlane32_swap(u, u, false, false);
    return __builtin_bit_cast(float, (unsigned)r[0]); }
typedef float f32x2 __attribute__((ext_vector_type(2)));
DI void ln_gelu4(f32x4 acc, f32x4 cs, float b0, float b1, float b2, float b3, float nmean, float rs, unsigned& o01, unsigned& o23) {
    float z0, z1, z2, z3, t0, t1, t2, t3;
    asm("v_fma_f32 %0, %16, %12, %8\n\tv_fma_f32 %1, %16, %13, %9\n\tv_fma_f32 %2, %16, %14, %10\n\tv_fma_f32 %3, %16, %15, %11\n\t"
        "v_fma_f32 %0, %17, %0, %18\n\tv_fma_f32 %1, %17, %1, %19\n\tv_fma_f32 %2, %17, %2, %20\n\tv_fma_f32 %3, %17, %3, %21\n\t"
        "v_mul_f32 %4, %0, %0\n\tv_mul_f32 %5, %1, %1\n\tv_mul_f32 %6, %2, %2\n\tv_mul_f32 %7, %3, %3\n\t"
        "v_fmaak_f32 %4, %22, %4, 0xc0135761\n\tv_fmaak_f32 %5, %22, %5, 0xc0135761\n\tv_fmaak_f32 %6, %22, %6, 0xc0135761\n\tv_fmaak_f32 %7, %22, %7, 0xc0135761\n\t"
        "v_mul_f32 %4, %0, %4\n\tv_mul_f32 %5, %1, %5\n\tv_mul_f32 %6, %2, %6\n\tv_mul_f32 %7, %3, %7\n\t"
        "v_exp_f32 %4, %4\n\tv_exp_f32 %5, %5\n\tv_exp_f32 %6, %6\n\tv_exp_f32 %7, %7\n\t"
        "v_add_f32 %4, 1.0, %4\n\tv_add_f32 %5, 1.0, %5\n\tv_add_f32 %6, 1.0, %6\n\tv_add_f32 %7, 1.0, %7\n\t"
        "v_rcp_f32 %4, %4\n\tv_rcp_f32 %5, %5\n\tv_rcp_f32 %6, %6\n\tv_rcp_f32 %7, %7\n\t"
        "v_mul_f32 %0, %0, %4\n\tv_mul_f32 %1, %1, %5\n\tv_mul_f32 %2, %2, %6\n\tv_mul_f32 %3, %3, %7"
        : "=&v"(z0), "=&v"(z1), "=&v"(z2), "=&v"(z3), "=&v"(t0), "=&v"(t1), "=&v"(t2), "=&v"(t3)
        : "v"(acc[0]), "v"(acc[1]), "v"(acc[2]), "v"(acc[3]), "v"(cs[0]), "v"(cs[1]), "v"(cs[2]), "v"(cs[3]), "v"(nmean), "v"(rs), "v"(b0), "v"(b1), "v"(b2), "v"(b3),
          "v"(-0.1029432397f));
    o01 = pk2(z0, z1); o23 = pk2(z2, z3);
}

struct Unit { rsrc_t rs; int soff, ld, w2, live; int dst[4]; int cs; };
constexpr int NUNIT = 1296 + 1296 + 162;
constexpr int NEARLY = 1024;
struct UnitTab { int v[NUNIT + 1][8]; };
constexpr UnitTab make_units() {
    UnitTab T{};
    for (int u = 0; u <= NUNIT; ++u) {
        const bool iswo = u < 162, isw1 = !iswo && u < 1458, isw2 = u >= 1458 && u < NUNIT;
        const int v = iswo ? u : isw1 ? u - 162 : u - 1458;
        const int j = isw1 ? (v % 72) / 9 : v / 162, ks = isw1 ? v / 72 : (v % 162) % 18, G = isw1 ? v % 9 : (v % 162) / 18;
        T.v[u][0] = isw1 ? 0 : isw2 ? 1 : iswo ? 2 : 3;
        T.v[u][1] = isw1 ? (32 * ks * FF + 576 * j + 64 * G) * 2 : ((isw2 ? 576 * j : 0) + 32 * ks) * (D * 2) + 128 * G;
        const int base = (int)OFF_WS + j * (int)WS_J, gi = G < j ? G : G - 1;
        const bool A = isw1 ? G < 5 : (G == j || gi < 4);
        const int fp = isw1 ? ffn_pos1(A, A ? G : G - 5, ks) : ffn_pos2(A, G == j ? 4 : gi & 3, ks);
        for (int c = 0; c < 4; ++c)
            T.v[u][2 + c] = (isw1 || isw2) ? base + ffn_stream_base(A ? c : 4 + c) + fp * 1024 : iswo ? (int)OFF_WO + ((4 * G + c) * 18 + ks) * 1024 : 0x7fff0000;
        T.v[u][6] = isw1 ? (int)OFF_CSP + (ks * FF + 576 * j + 64 * G) * 4 : 0x7fff0000;
        T.v[u][7] = 0;
    }
    return T;
}
__device__ const UnitTab UNITS = make_units();
DI Unit unit_decode(const void* w1, const void* w2, const void* wo, int u) {
    const int* e = UNITS.v[u < NUNIT ? u : NUNIT];
    const int kind = e[0];
    Unit r;
    r.rs = mkrsrc(kind == 0 ? w1 : kind == 1 ? w2 : wo, kind == 3 ? 0u : kind == 2 ? D * D * 2 : D * FF * 2);
    r.ld = kind == 0 ? FF * 2 : D * 2; r.w2 = kind == 1; r.live = kind != 3; r.soff = e[1];
#pragma unroll
    for (int c = 0; c < 4; ++c) r.dst[c] = e[2 + c];
    r.cs = e[6];
    return r;
}
#define UST 16
DI void unit_load(const Unit& t, unsigned scr, int lane) {
    const int voff = (lane >> 3) * t.ld + (lane & 7) * 16;
#pragma unroll
    for (int m = 0; m < 4; ++m) dma16(t.rs, voff, t.soff + 8 * m * t.ld, scr + m * 1024);
}
DI void unit_store(const Unit& t, const LAS char* scr, rsrc_t rws, int lane) {
    const int g = lane >> 4, q = (lane & 15) >> 2, p = lane & 3;
    const int row0 = t.w2 ? 4 * g + q : 8 * g + q, rstep = t.w2 ? 16 : 4;
    float cs[4];
#pragma unroll
    for (int c = 0; c < 4; ++c) {
        const v2u lo = ldstr(scr + row0 * 128 + (16 * c + 4 * p) * 2);
        const v2u hi = ldstr(scr + (row0 + rstep) * 128 + (16 * c + 4 * p) * 2);
        bst128<UST>(v4u{lo.x, lo.y, hi.x, hi.y}, rws, lane * 16, t.dst[c]);
        cs[c] = sum32(sum16(dot2(hi.y, dot2(hi.x, dot2(lo.y, dot2(lo.x, 0.f))))));
    }
    const float mine = g == 0 ? cs[0] : g == 1 ? cs[1] : g == 2 ? cs[2] : cs[3];
    __builtin_amdgcn_raw_buffer_store_b32(__builtin_bit_cast(unsigned, mine), rws, lane * 4, t.cs, 16);
}

DI void unit_store_w2(const Unit& t, const LAS char* scr, rsrc_t rws, int lane) {
    const int g = lane >> 4, q = (lane & 15) >> 2, p = lane & 3;
    v2u lo[4], hi[4];
#pragma unroll
    for (int c = 0; c < 4; ++c) { lo[c] = ldstr(scr + (4 * g + q) * 128 + 32 * c + 8 * p); hi[c] = ldstr(scr + (4 * g + q + 16) * 128 + 32 * c + 8 * p); }
#pragma unroll
    for (int c = 0; c < 4; ++c) bst128<UST>(v4u{lo[c].x, lo[c].y, hi[c].x, hi[c].y}, rws, lane * 16, t.dst[c]);
}

#define QST 0
struct K1Args { const void* x; const void* wqkv; const void* wo; const void* w1; const void* w2; char* ws; };
constexpr int K1_STAGE = 32768;
constexpr int K1_TAB = 4 * K1_STAGE;
constexpr int K1_LDS = K1_TAB + 4096;

__global__ __launch_bounds__(512, 2) void k1_qkv(K1Args a) {
    extern __shared__ __attribute__((aligned(16))) char smem_[];
    LAS char* smem = (LAS char*)smem_;
    const unsigned lds0 = (unsigned)(size_t)smem_;
    const int tid = threadIdx.x, lane = tid & 63, w = __builtin_amdgcn_readfirstlane(tid >> 6), b = blockIdx.x;
    if (b == 0 && tid < FL_N) *(GAS unsigned*)(a.ws + OFF_FLAG + tid * 256) = 0u;
    if (b >= 224) {
        const rsrc_t rws = mkrsrc(a.ws, (unsigned)WS_END);
        const int u0 = ((b - 224) * 8 + w) * 4;
        Unit t[4];
#pragma unroll
        for (int m = 0; m < 4; ++m) { t[m] = unit_decode(a.w1, a.w2, a.wo, u0 + m); unit_load(t[m], lds0 + w * 16384 + m * 4096, lane); }
        WAITV(12); unit_store(t[0], smem + w * 16384, rws, lane);
        WAITV(13); unit_store(t[1], smem + w * 16384 + 4096, rws, lane);
        WAITV(14); unit_store(t[2], smem + w * 16384 + 8192, rws, lane);
        WAITV(15); unit_store(t[3], smem + w * 16384 + 12288, rws, lane);
        return;
    }
    const int xc = b & 7, idx = b >> 3;
    const int rt = 4 * (xc >> 1) + (idx & 3), ct = 7 * (xc & 1) + (idx >> 2);
    if (w >= 4) {
        const int lw = w - 4;
        __builtin_amdgcn_s_setprio(1);
        const rsrc_t rx = mkrsrc(a.x, S * D * 2), rw = mkrsrc(a.wqkv, D * N3 * 2);
        const int vx0 = (128 * rt + 32 * lw + (lane >> 3)) * (D * 2) + ((lane & 7) ^ (lane >> 4)) * 16, vx1 = vx0 ^ 64;
        const int c16 = lane & 15, q = lane >> 4;
        const int vw0 = (16 * lw + q) * (N3 * 2) + 256 * ct + ((((c16 >> 1) ^ (q << 1)) << 1) | (c16 & 1)) * 16;
        const int vw1 = (16 * lw + q) * (N3 * 2) + 256 * ct + ((((c16 >> 1) ^ ((q << 1) | 1)) << 1) | (c16 & 1)) * 16;
        auto stage = [&](int s) {
            const unsigned sb = lds0 + (s & 3) * K1_STAGE;
#pragma unroll
            for (int m = 0; m < 4; ++m) {
                dma16(rw, m < 2 ? vw0 : vw1, (64 * s + 4 * m) * (N3 * 2), sb + 16384 + (4 * lw + m) * 1024);
                dma16(rx, (m & 1) ? vx1 : vx0, 128 * s + m * 8 * (D * 2), sb + (4 * lw + m) * 1024);
            }
        };
        const int g = lane >> 4, i = lane & 15, q4 = i >> 2, p = lane & 3;
        const int sxo = (32 * lw + i) * 128, swo = 16384 + (8 * g + q4) * 256 + 8 * p, key = (q4 << 1) | (g & 1);
        const v4u ones = {0x3f803f80u, 0x3f803f80u, 0x3f803f80u, 0x3f803f80u};
        f32x4 sx[2], sq[2], cs[2];
#pragma unroll
        for (int u = 0; u < 2; ++u) sx[u] = sq[u] = cs[u] = f32x4{0.f, 0.f, 0.f, 0.f};
        auto stats = [&](int s) {
            const LAS char* sb = smem + (s & 3) * K1_STAGE;
#pragma unroll
            for (int k2 = 0; k2 < 2; ++k2)
#pragma unroll
                for (int u = 0; u < 2; ++u) {
                    const v4u xv = lds128(sb + sxo + u * 2048 + ((4 * k2 + g) ^ ((i >> 1) & 7)) * 16);
                    const LAS char* ap = sb + swo + k2 * 8192 + (((2 * lw + u) ^ key) * 32);
                    const v2u lo = ldstr(ap), hi = ldstr(ap + 1024);
                    sx[u] = mfma16(ones, xv, sx[u]); sq[u] = mfma16(xv, xv, sq[u]); cs[u] = mfma16(v4u{lo.x, lo.y, hi.x, hi.y}, ones, cs[u]);
                }
        };
        stage(0); stage(1); stage(2);
        WAITV(16); BAR();
        WAITV(8); BAR();
        stats(0);
#pragma unroll
        for (int s = 3; s < 9; ++s) { stage(s); stats(s - 2); WAITV(8); asm volatile("s_waitcnt lgkmcnt(0)" ::: "memory"); BAR(); }
        stats(7); WAITV(0); BAR();
        stats(8);
        LAS float* tabx = (LAS float*)(smem + K1_TAB);
        LAS float* tabc = tabx + 256;
#pragma unroll
        for (int u = 0; u < 2; ++u) {
            const float s2 = (i & 2) ? ((i & 1) ? sq[u][3] : sq[u][2]) : ((i & 1) ? sq[u][1] : sq[u][0]);
            const float mean = sx[u][0] * (1.0f / D), var = __builtin_fmaxf(__builtin_fmaf(-mean, mean, s2 * (1.0f / D)), 0.f);
            if (g == q4) *(LAS f32x2*)(tabx + (32 * lw + 16 * u + i) * 2) = f32x2{mean, __builtin_amdgcn_rsqf(var + LN_EPS)};
            if (i == 0) *(LAS f32x4*)(tabc + 32 * lw + 16 * u + 4 * g) = cs[u]; }
        LBAR();
        return;
    }
    const int th = w & 1, ch = w >> 1, h = lane >> 5, l31 = lane & 31;
    const int q = (lane & 15) >> 2, p = lane & 3, blk = (lane >> 4) & 1;
    const int xrow = (64 * th + l31) * 128, xkey = (l31 >> 1) & 7;
    const int woff = 16384 + (8 * h + q) * 256 + (((4 * ch + blk) ^ ((q << 1) | h)) * 32) + 8 * p;
    f32x16 acc[2][2];
#pragma unroll
    for (int cx = 0; cx < 2; ++cx)
#pragma unroll
        for (int ty = 0; ty < 2; ++ty)
#pragma unroll
            for (int e = 0; e < 16; ++e) acc[cx][ty][e] = 0.f;
    int xo[4];
#pragma unroll
    for (int k4 = 0; k4 < 4; ++k4) xo[k4] = xrow + ((2 * k4 + h) ^ xkey) * 16;
    v4u bA[2][2], aA[2][2], bB[2][2], aB[2][2];
    auto rd = [&](v4u (&bf)[2][2], v4u (&af)[2][2], const LAS char* sb, int k2) {
#pragma unroll
        for (int u = 0; u < 2; ++u) {
#pragma unroll
            for (int cx = 0; cx < 2; ++cx) { const LAS char* ap = sb + ((woff ^ (cx << 6)) + (2 * k2 + u) * 4096);
                const v2u lo = ldstr(ap), hi = ldstr(ap + 1024); af[u][cx] = v4u{lo.x, lo.y, hi.x, hi.y}; }
#pragma unroll
            for (int ty = 0; ty < 2; ++ty) bf[u][ty] = lds128(sb + xo[2 * k2 + u] + ty * 4096);
        }
    };
    auto mm = [&](const v4u (&bf)[2][2], const v4u (&af)[2][2]) {
#pragma unroll
        for (int u = 0; u < 2; ++u)
#pragma unroll
            for (int cx = 0; cx < 2; ++cx)
#pragma unroll
                for (int ty = 0; ty < 2; ++ty) acc[cx][ty] = mfma32(af[u][cx], bf[u][ty], acc[cx][ty]);
    };
    int dst[2][2];
#pragma unroll
    for (int cx = 0; cx < 2; ++cx)
#pragma unroll
        for (int m = 0; m < 2; ++m) { const int n8 = 128 * ct + 64 * ch + 32 * cx + 16 * m + 8 * h, ja = n8 / 72;
            dst[cx][m] = n8 < N3 ? (int)OFF_QKV + ja * (int)QKV_G + (128 * rt + 64 * th + l31) * 144 + (n8 - 72 * ja) * 2 : 0x7fff0000; }
    auto half = [&](v4u (&bn)[2][2], v4u (&an)[2][2], const LAS char* nb, int k2, const v4u (&bc)[2][2], const v4u (&ac)[2][2]) {
        SB();
        rd(bn, an, nb, k2);
        mm(bc, ac);
#pragma unroll
        for (int e = 0; e < 4; ++e) { __builtin_amdgcn_sched_group_barrier(0x008, 1, 0); __builtin_amdgcn_sched_group_barrier(0x100, 2, 0); }
#pragma unroll
        for (int e = 0; e < 4; ++e) { __builtin_amdgcn_sched_group_barrier(0x008, 1, 0); __builtin_amdgcn_sched_group_barrier(0x100, 1, 0); }
        SB();
    };
#define K1_SYNC() asm volatile("s_waitcnt lgkmcnt(12)\n\ts_barrier" ::: "memory")
    BAR();
    unsigned pf[2];
#pragma unroll
    for (int m = 0; m < 2; ++m) {
        const int lx = (idx >> 2) * 55 + tid, lw2 = (idx & 3) * 96 + (tid - 55), wo = (192 + 192 * m + (lw2 >> 1)) * (N3 * 2) + 256 * ct + (lw2 & 1) * 128;
        const char* p = tid < 55 ? (const char*)a.x + (size_t)(128 * rt + (lx < 384 ? lx / 3 : 0)) * (D * 2) + (3 + 3 * m + lx % 3) * 128
                                 : (const char*)a.wqkv + ((tid < 151 && wo < D * N3 * 2) ? wo : 0);
        asm volatile("global_load_dword %0, %1, off" : "=v"(pf[m]) : "v"(p));
    }
    __builtin_amdgcn_s_waitcnt(0xC07F);
    rd(bA, aA, smem, 0);
    half(bB, aB, smem, 1, bA, aA);
    BAR();
    half(bA, aA, smem + K1_STAGE, 0, bB, aB);
    K1_SYNC();
#pragma nounroll
    for (int s = 1; s < 7; ++s) {
        half(bB, aB, smem + (s & 3) * K1_STAGE, 1, bA, aA);
        half(bA, aA, smem + ((s + 1) & 3) * K1_STAGE, 0, bB, aB);
        K1_SYNC();
    }
    half(bB, aB, smem + 3 * K1_STAGE, 1, bA, aA);
    half(bA, aA, smem, 0, bB, aB);
    half(bB, aB, smem, 1, bA, aA);
    SB(); mm(bB, aB); SB();
    asm volatile("s_waitcnt vmcnt(0)" :: "v"(pf[0]), "v"(pf[1]));
    LBAR();
    const LAS float* tabc = (const LAS float*)(smem + K1_TAB) + 256;
    const LAS float* tabx = (const LAS float*)(smem + K1_TAB);
    const rsrc_t rws = mkrsrc(a.ws, (unsigned)OFF_WS);
#pragma unroll
    for (int ty = 0; ty < 2; ++ty) {
        const int t = 64 * th + 32 * ty + l31;
        const float mean = tabx[t * 2], rstd = tabx[t * 2 + 1];
#pragma unroll
        for (int cx = 0; cx < 2; ++cx) {
            const float rs = rstd * (128 * ct + 64 * ch + 32 * cx < D ? QSCALE : 1.0f);
            unsigned pk[8];
#pragma unroll
            for (int qd = 0; qd < 4; ++qd) { const f32x4 c = *(const LAS f32x4*)(tabc + 64 * ch + 32 * cx + 8 * qd + 4 * h);
                pk[2 * qd] = pk2(rs * __builtin_fmaf(-mean, c[0], acc[cx][ty][4 * qd]), rs * __builtin_fmaf(-mean, c[1], acc[cx][ty][4 * qd + 1]));
                pk[2 * qd + 1] = pk2(rs * __builtin_fmaf(-mean, c[2], acc[cx][ty][4 * qd + 2]), rs * __builtin_fmaf(-mean, c[3], acc[cx][ty][4 * qd + 3])); }
#pragma unroll
            for (int m = 0; m < 2; ++m) {
                const auto s0 = __builtin_amdgcn_permlane32_swap(pk[4 * m], pk[4 * m + 2], false, false), s1 = __builtin_amdgcn_permlane32_swap(pk[4 * m + 1], pk[4 * m + 3], false, false);
                bst128<QST>(v4u{(unsigned)s0[0], (unsigned)s1[0], (unsigned)s0[1], (unsigned)s1[1]}, rws, dst[cx][m], ty * 32 * 144); }
        }
    }
}

struct K2Args { char* ws; const void* x; const void* wo; const void* w1; const void* w2; const void* b1; const void* b2; void* out; };
constexpr int KV_HEAD = 320 * 40;
constexpr int K2_K = 0, K2_V = 4 * KV_HEAD, K2_O = 8 * KV_HEAD, K2_SCR = K2_O + 64 * 144;
constexpr int K4_X = 0, K4_H = 73728, K4_ST = 147456, K4_CS = K4_ST + 512, K4_CNT = K4_CS + 2304, K4_OWN = K4_CNT + 256, K2_LDS = K4_OWN + 8192;
static_assert(K2_SCR + 8 * 4096 <= K4_ST, "LDS map");

DI void flag_add(char* ws, int f) { __hip_atomic_fetch_add((gu32*)(ws + OFF_FLAG + f * 256), 1u, __ATOMIC_RELAXED, __HIP_MEMORY_SCOPE_AGENT); }
DI void flag_wait(char* ws, int f, unsigned n) {
    gu32* p = (gu32*)(ws + OFF_FLAG + f * 256);
    for (unsigned spins = 0; __hip_atomic_load(p, __ATOMIC_RELAXED, __HIP_MEMORY_SCOPE_AGENT) < n && spins < (1u << 20); ++spins) __builtin_amdgcn_s_sleep(1);
    __builtin_amdgcn_fence(__ATOMIC_ACQUIRE, "wavefront");
}
DI void flag_wait_all(char* ws, int f, unsigned n, int lane) {
    gu32* p = (gu32*)(ws + OFF_FLAG + (f + (lane & 31)) * 256);
    for (unsigned spins = 0; !__all(__hip_atomic_load(p, __ATOMIC_RELAXED, __HIP_MEMORY_SCOPE_AGENT) >= n) && spins < (1u << 20); ++spins) __builtin_amdgcn_s_sleep(1);
    __builtin_amdgcn_fence(__ATOMIC_ACQUIRE, "wavefront");
}
DI void tile_in(const char* src, LAS char* smem, int tid) {
    const rsrc_t rs = mkrsrc(src, 4 * (unsigned)TT_FR);
    v4u t[9];
#pragma unroll
    for (int m = 0; m < 9; ++m) t[m] = bld128_sc1(rs, tid * 16, m * 8192);
#pragma unroll
    for (int m = 0; m < 9; ++m) *(LAS v4u*)(smem + tid * 16 + m * 8192) = t[m];
}

template <bool EDGE> DI void attn_core(LAS char* smem, const v4u q0, const v4u q1, int r, int w, int lane, f32x16& oacc, const Unit& ua, rsrc_t rws) {
    const int hl = w & 3, qh = w >> 2, l31 = lane & 31, hh = lane >> 5;
    const int tmin = EDGE ? ((256 - 64 * r - 32 * qh) >> 5) : 0;
    const float ninf = -__builtin_inff();
    const LAS char* kp = smem + K2_K + (hl * 320 + 32 * qh + l31) * 40;
    v4u ka[9], kb[9];
#pragma unroll
    for (int t = 0; t < 9; ++t) { const v2u a0 = lds64v(kp + t * 1280 + 16 * hh), a1 = lds64v(kp + t * 1280 + 16 * hh + 8);
        ka[t] = v4u{a0.x, a0.y, a1.x, a1.y};
        const v2u c0 = lds64v(kp + t * 1280 + 32), c1 = lds64v(kp + t * 1280 + 24);
        kb[t] = v4u{c0.x, c0.y, c1.x, c1.y}; }
    auto score = [&](int t, const f32x16& c) { return mfma32(kb[t], q1, mfma32(ka[t], q0, c)); };
    bool le[16];
#pragma unroll
    for (int rg = 0; rg < 16; ++rg) le[rg] = (rg & 3) + 8 * (rg >> 2) + 4 * hh <= l31;
    auto merged = [&](const f32x16& c) { const f32x16 s8 = score(8, c); f32x16 s0;
        if (EDGE) { s0 = s8;
#pragma unroll
            for (int rg = 0; rg < 16; ++rg) s0[rg] = ninf; } else s0 = score(0, c);
        f32x16 m;
#pragma unroll
        for (int rg = 0; rg < 16; ++rg) m[rg] = le[rg] ? s8[rg] : s0[rg];
        return m; };
    const f32x16 zero = {};
    float mx;
    { const f32x16 m = merged(zero); mx = __builtin_fmaxf(m[0], m[1]);
#pragma unroll
      for (int rg = 2; rg < 16; rg += 2) mx = __builtin_fmaxf(__builtin_fmaxf(mx, m[rg]), m[rg + 1]); }
    if (!EDGE) {
        f32x16 sc = score(1, zero); float m2 = mx;
#pragma unroll
        for (int t = 1; t < 8; ++t) { f32x16 sn; if (t < 7) sn = score(t + 1, zero);
            SB();
#pragma unroll
            for (int rg = 0; rg < 16; rg += 4) { mx = __builtin_fmaxf(__builtin_fmaxf(mx, sc[rg]), sc[rg + 1]); m2 = __builtin_fmaxf(__builtin_fmaxf(m2, sc[rg + 2]), sc[rg + 3]); }
            SB();
            if (t < 7) sc = sn; }
        mx = __builtin_fmaxf(mx, m2);
    } else {
#pragma unroll
        for (int t = 1; t < 8; ++t) if (t >= tmin) { const f32x16 s = score(t, zero);
#pragma unroll
            for (int rg = 0; rg < 16; rg += 2) mx = __builtin_fmaxf(__builtin_fmaxf(mx, s[rg]), s[rg + 1]); }
    }
    mx = max32(mx);
    if (ua.live) { WAITV(0); if (ua.w2) unit_store_w2(ua, smem + K2_SCR + w * 4096, rws, lane); else unit_store(ua, smem + K2_SCR + w * 4096, rws, lane); }
    f32x16 cm;
#pragma unroll
    for (int rg = 0; rg < 16; ++rg) cm[rg] = -mx;
    const LAS char* vp = smem + K2_V + (hl * 320 + 32 * qh + 4 * hh + ((lane & 15) >> 2)) * 40 + (((lane >> 4) & 1) ? 32 : 8 * (lane & 3));
    auto pv = [&](int t, const unsigned (&pw)[8]) {
#pragma unroll
        for (int s = 0; s < 2; ++s) {
            const v2u lo = ldstr(vp + (t * 32 + 16 * s) * 40), hi = ldstr(vp + (t * 32 + 16 * s + 8) * 40);
            oacc = mfma32(v4u{lo.x, lo.y, hi.x, hi.y}, v4u{pw[4 * s], pw[4 * s + 1], pw[4 * s + 2], pw[4 * s + 3]}, oacc);
        } };
    oacc = zero;
    { const f32x16 m = merged(cm); unsigned pw[8], p8[8], p0[8];
#pragma unroll
      for (int e = 0; e < 8; ++e) { pw[e] = pk2(__builtin_amdgcn_exp2f(m[2 * e]), __builtin_amdgcn_exp2f(m[2 * e + 1]));
          const unsigned mk = (le[2 * e] ? 0xffffu : 0u) | (le[2 * e + 1] ? 0xffff0000u : 0u); p8[e] = pw[e] & mk; p0[e] = pw[e] & ~mk; }
      pv(8, p8); if (!EDGE) pv(0, p0); }
    if (!EDGE) {
        f32x16 sc = score(1, cm);
#pragma unroll
        for (int t = 1; t < 8; ++t) { f32x16 sn; if (t < 7) sn = score(t + 1, cm);
            SB();
            unsigned pw[8];
#pragma unroll
            for (int e = 0; e < 8; ++e) pw[e] = pk2(__builtin_amdgcn_exp2f(sc[2 * e]), __builtin_amdgcn_exp2f(sc[2 * e + 1]));
            pv(t, pw);
            SB();
            if (t < 7) sc = sn; }
    } else {
#pragma unroll
        for (int t = 1; t < 8; ++t) if (t >= tmin) { const f32x16 s = score(t, cm); unsigned pw[8];
#pragma unroll
            for (int e = 0; e < 8; ++e) pw[e] = pk2(__builtin_amdgcn_exp2f(s[2 * e]), __builtin_amdgcn_exp2f(s[2 * e + 1]));
            pv(t, pw); }
    }
}

#define RD 24
static_assert(162 + 72 * ((RD - 1) / 3) + 9 * 7 + 2 < NEARLY && 162 + 72 * ((RD - 1) / T0B) + 9 * 7 + 5 + T0B - 1 < NEARLY, "early share");

template <int T, int OFS, class Side>
DI void ffn_pass(f32x4 (&acc)[3][4], v4u (&ring)[RD], rsrc_t rw, int pos, const LAS char* bp, int lane, Side side) {
    v4u bf[4];
#pragma unroll
    for (int tt = 0; tt < 4; ++tt) bf[tt] = lds128(bp + tt * 18432);
#pragma unroll
    for (int t = 0; t < T; ++t)
#pragma unroll
        for (int tt = 0; tt < 4; ++tt) acc[t][tt] = f32x4{0.f, 0.f, 0.f, 0.f};
#pragma unroll
    for (int u = 0; u < 18; ++u) {
        SB();
        v4u bn[4];
        if (u + 1 < 18) {
#pragma unroll
            for (int tt = 0; tt < 4; ++tt) bn[tt] = lds128(bp + tt * 18432 + (u + 1) * 1024);
        }
#pragma unroll
        for (int t = 0; t < T; ++t) {
            const int n = u * T + t, sl = (n + OFS) % RD;
#pragma unroll
            for (int tt = 0; tt < 4; ++tt) acc[t][tt] = mfma16(ring[sl], bf[tt], acc[t][tt]);
            ring[sl] = bld128_sc1(rw, lane * 16 + (n & 3) * 1024, pos + (n >> 2) * 4096);
        }
        side(u);
#pragma unroll
        for (int t = 0; t < T; ++t) {
            if (T == 1) {
#pragma unroll
                for (int e = 0; e < 4; ++e) { __builtin_amdgcn_sched_group_barrier(0x008, 1, 0); __builtin_amdgcn_sched_group_barrier(0x100, 1, 0); }
            } else if (T == 2) {
#pragma unroll
                for (int e = 0; e < 2; ++e) { __builtin_amdgcn_sched_group_barrier(0x008, 2, 0); __builtin_amdgcn_sched_group_barrier(0x100, 1, 0); }
            } else { __builtin_amdgcn_sched_group_barrier(0x008, 4, 0); if (t < 4) __builtin_amdgcn_sched_group_barrier(0x100, 1, 0); }
            __builtin_amdgcn_sched_group_barrier(0x020, 1, 0);
        }
        SB();
        if (u + 1 < 18) {
#pragma unroll
            for (int tt = 0; tt < 4; ++tt) bf[tt] = bn[tt];
        }
    }
#pragma unroll
    for (int t = 0; t < T; ++t)
#pragma unroll
        for (int tt = 0; tt < 4; ++tt) PIN(acc[t][tt]);
    asm volatile("s_nop 7" ::: "memory");
}

template <bool A> DI void ffn_body(const K2Args& a, LAS char* smem, int r, int j, int w, int lane) {
    constexpr int NT = A ? 5 : 4;
    const int tid = threadIdx.x, g = lane >> 4, i = lane & 15, s = w & 3;
    char* ws = a.ws;
    const rsrc_t rws = mkrsrc(ws, (unsigned)WS_END);
    v4u ring[RD];
    {
        if (tid == 448) { flag_add(ws, FL_O + r); flag_wait(ws, FL_O + r, 8); }
        const int dt = A ? 4 * j + w : 32 + (j >> 1), tt0 = A ? 0 : 2 * (j & 1);
        const rsrc_t rw = mkrsrc(ws + OFF_WO, 36 * 18 * 1024), rx = mkrsrc(a.x, S * D * 2);
        v4u af[18]; v2u xr[4];
        if (A || w == 4) {
#pragma unroll
            for (int ks = 0; ks < 18; ++ks) af[ks] = bld128(rw, lane * 16 + (ks & 3) * 1024, dt * 18432 + (ks >> 2) * 4096);
#pragma unroll
            for (int u = 0; u < 4; ++u) xr[u] = bld64(rx, (64 * r + 16 * ((tt0 + u) & 3) + i) * (D * 2) + (16 * dt + 4 * g) * 2, 0);
        }
        BAR();
        tile_in(ws + OFF_O + 4 * r * TT_FR, smem, tid);
        LBAR();
        const rsrc_t rf = mkrsrc(ws + OFF_WS + j * WS_J + ffn_stream_base(w), (A ? 180 : 144) * 1024);
        auto fill = [&](int n) { ring[n] = bld128_sc1(rf, lane * 16 + (n & 3) * 1024, (n >> 2) * 4096); };
        if (!(A || w == 4)) {
#pragma unroll
            for (int n = 0; n < RD / 2; ++n) fill(n);
            if (w == 7) flag_wait_all(ws, FL_O, 8, lane);
        }
        if (A || w == 4) {
            f32x4 acc[4];
#pragma unroll
            for (int u = 0; u < 4; ++u) acc[u] = f32x4{0.f, 0.f, 0.f, 0.f};
            const LAS char* bp = smem + lane * 16;
            int to[4];
#pragma unroll
            for (int u = 0; u < 4; ++u) to[u] = ((tt0 + u) & 3) * 18432;
            v4u bf[2][4];
#pragma unroll
            for (int u = 0; u < 4; ++u) bf[0][u] = lds128(bp + to[u]);
#pragma unroll
            for (int ks = 0; ks < 18; ++ks) {
                SB();
                if (ks < 17) {
#pragma unroll
                    for (int u = 0; u < 4; ++u) bf[(ks + 1) & 1][u] = lds128(bp + (ks + 1) * 1024 + to[u]); }
#pragma unroll
                for (int u = 0; u < 4; ++u) acc[u] = mfma16(af[ks], bf[ks & 1][u], acc[u]);
                if (ks < RD / 2) fill(ks);
#pragma unroll
                for (int u = 0; u < 4; ++u) { __builtin_amdgcn_sched_group_barrier(0x008, 1, 0); __builtin_amdgcn_sched_group_barrier(0x100, 1, 0); }
                SB();
            }
#pragma unroll
            for (int u = 0; u < 4; ++u) if (A || u < 2) { const int tt = (tt0 + u) & 3;
                const v2u o = {pk2(acc[u][0] + blo(xr[u].x), acc[u][1] + bhi(xr[u].x)), pk2(acc[u][2] + blo(xr[u].y), acc[u][3] + bhi(xr[u].y))};
                __builtin_amdgcn_raw_buffer_store_b64(o, rws, (int)OFF_H + (4 * r + tt) * (int)TT_FR + (dt >> 1) * 1024 + ((2 * (dt & 1) + (g >> 1)) * 16 + i) * 16 + (g & 1) * 8, 0, 16); }
        }
        SB();
        if (w != 7) {
#pragma unroll
            for (int n = RD / 2; n < RD; ++n) fill(n); }
        SB();
        asm volatile("s_waitcnt vmcnt(%0) lgkmcnt(0)\n\ts_barrier" :: "n"(RD - RD / 2) : "memory");
        if (w == 7) {
            if (lane == 0) { flag_add(ws, FL_H + r); flag_wait(ws, FL_H + r, 8); }
            SB();
#pragma unroll
            for (int n = RD / 2; n < RD; ++n) fill(n); }
    }
    const rsrc_t rw = mkrsrc(ws + OFF_WS + j * WS_J + ffn_stream_base(w), (A ? 180 : 144) * 1024);
    BAR();
    tile_in(ws + OFF_H + 4 * r * TT_FR, smem + K4_X, tid);
    LBAR();
    v2u bb[NT];
#pragma unroll
    for (int t = 0; t < NT; ++t) bb[t] = *(const GAS v2u*)((const char*)a.b1 + (576 * j + 16 * (4 * ((A ? 0 : 5) + t) + s) + 4 * g) * 2);
    unsigned pc[3][18];
    const rsrc_t rc = mkrsrc(ws + OFF_CSP, 18 * FF * 4);
    if (!A) {
#pragma unroll
        for (int m = 0; m < 3; ++m) if (m < 2 || w == 4) {
#pragma unroll
            for (int ks = 0; ks < 18; ++ks) pc[m][ks] = __builtin_amdgcn_raw_buffer_load_b32(rc, (576 * j + 256 * m + tid - 256) * 4, ks * FF * 4, 16); } }
    SB();
    LAS float* st = (LAS float*)(smem + K4_ST);
    if (!A) {
        f32x4 s1 = {0.f, 0.f, 0.f, 0.f}, s2 = s1;
        const v4u ones = {0x3f803f80u, 0x3f803f80u, 0x3f803f80u, 0x3f803f80u};
#pragma unroll
        for (int kh = 0; kh < 2; ++kh) { v4u v[9];
#pragma unroll
            for (int k = 0; k < 9; ++k) v[k] = lds128(smem + K4_X + (s * 18 + 9 * kh + k) * 1024 + lane * 16);
#pragma unroll
            for (int k = 0; k < 9; ++k) { s1 = mfma16(ones, v[k], s1); s2 = mfma16(v[k], v[k], s2); } }
        const float sq = (i & 2) ? ((i & 1) ? s2[3] : s2[2]) : ((i & 1) ? s2[1] : s2[0]);
        const float mean = s1[0] * (1.0f / D), var = __builtin_fmaxf(__builtin_fmaf(-mean, mean, sq * (1.0f / D)), 0.f);
        if (g == (i >> 2)) *(LAS f32x2*)(st + (16 * s + i) * 2) = f32x2{mean, __builtin_amdgcn_rsqf(var + LN_EPS)};
    }
    if (!A) {
#pragma unroll
        for (int m = 0; m < 3; ++m) if (m < 2 || w == 4) { float sum = 0.f;
#pragma unroll
            for (int ks = 0; ks < 18; ++ks) sum += __builtin_bit_cast(float, pc[m][ks]);
            *(LAS float*)(smem + K4_CS + (256 * m + tid - 256) * 4) = sum; }
        asm volatile("s_waitcnt lgkmcnt(0)" ::: "memory");
        if (lane == 0) __hip_atomic_fetch_add((LAS unsigned*)(smem + K4_CNT), 1u, __ATOMIC_RELAXED, __HIP_MEMORY_SCOPE_WORKGROUP); }
    if (A) __builtin_amdgcn_s_setprio(1);
    f32x4 acc[3][4];
    auto none = [](int) {};
    auto act = [&](int slot, int t) { const int ft = 4 * ((A ? 0 : 5) + t) + s;
        const float b0 = blo(bb[t].x), b1 = bhi(bb[t].x), b2 = blo(bb[t].y), b3 = bhi(bb[t].y);
        const f32x4 cs = *(const LAS f32x4*)(smem + K4_CS + (16 * ft + 4 * g) * 4);
#pragma unroll
        for (int tt = 0; tt < 4; ++tt) { const f32x2 mr = *(const LAS f32x2*)(st + (16 * tt + i) * 2);
            unsigned o01, o23; ln_gelu4(acc[slot][tt], cs, b0, b1, b2, b3, -mr[0], mr[1], o01, o23);
            *(LAS v2u*)(smem + K4_H + (tt * 18 + (ft >> 1)) * 1024 + lane * 16 + (ft & 1) * 8) = v2u{o01, o23}; } };
    constexpr int T0 = A ? 3 : T0B;
    ffn_pass<T0, 0>(acc, ring, rw, RD * 1024, smem + K4_X + lane * 16, lane, none);
    while (*(volatile LAS unsigned*)(smem + K4_CNT) < 4u) { }
    asm volatile("" ::: "memory");
#pragma unroll
    for (int t = 0; t < T0; ++t) act(t, t);
    ffn_pass<NT - T0, (18 * T0) % RD>(acc, ring, rw, (18 * T0 + RD) * 1024, smem + K4_X + lane * 16, lane, none);
#pragma unroll
    for (int t = T0; t < NT; ++t) act(t - T0, t);
    LBAR();
    constexpr int G2 = A ? 90 : 72;
    const LAS char* hp = smem + K4_H + lane * 16;
    const rsrc_t rp = mkrsrc(ws + OFF_P + (size_t)r * NSL * P_SRC, NSL * (unsigned)P_SRC);
    v4u outq[4];
    auto pack = [&]() {
#pragma unroll
        for (int t = 0; t < 2; ++t)
#pragma unroll
            for (int tp = 0; tp < 2; ++tp) {
                outq[t * 2 + tp] = v4u{pk2(acc[t][2 * tp][0], acc[t][2 * tp][1]), pk2(acc[t][2 * tp][2], acc[t][2 * tp][3]), pk2(acc[t][2 * tp + 1][0], acc[t][2 * tp + 1][1]), pk2(acc[t][2 * tp + 1][2], acc[t][2 * tp + 1][3])};
                PIN(outq[t * 2 + tp]); } };
    auto put = [&](int k, int ps) { const int gi = (A ? 0 : 4) + 2 * ps + (k >> 1), dt = 4 * (gi < j ? gi : gi + 1) + s;
        bst128<16>(outq[k], rp, lane * 16, j * (int)P_SRC + (dt * 2 + (k & 1)) * 1024); };
    ffn_pass<2, G2 % RD>(acc, ring, rw, (G2 + RD) * 1024, hp, lane, none);
    pack();
    ffn_pass<2, (G2 + 36) % RD>(acc, ring, rw, (G2 + 36 + RD) * 1024, hp, lane, [&](int u) { if (u >= 1 && u <= 7 && (u & 1)) put(u >> 1, 0); });
    pack();
    if (!A) {
#pragma unroll
        for (int k = 0; k < 4; ++k) put(k, 1); }
    if (A) { ffn_pass<1, (G2 + 72) % RD>(acc, ring, rw, (G2 + 72 + RD) * 1024, hp, lane, [&](int u) { if (u <= 6 && !(u & 1)) put(u >> 1, 1); });
#pragma unroll
        for (int u = 0; u < 2; ++u) *(LAS f32x4*)(smem + K4_OWN + (s * 2 + u) * 1024 + lane * 16) = acc[0][2 + u]; }
    asm volatile("s_waitcnt vmcnt(0) lgkmcnt(0)\n\ts_barrier" ::: "memory");
    if (tid == 448) { flag_add(ws, FL_P + r); flag_wait(ws, FL_P + r, 8); }
    BAR();
    {
        const int dt = 4 * j + s, tp = A ? 0 : 1, dx = 32 + (j >> 1), tpx = j & 1;
        const bool extra = !A && s == (j & 3);
        v4u pv[8], px[8];
#pragma unroll
        for (int src = 0; src < 8; ++src) pv[src] = bld128_sc1(rp, lane * 16, src != j ? src * (int)P_SRC + (dt * 2 + tp) * 1024 : 0x7fff0000);
        if (extra) {
#pragma unroll
            for (int src = 0; src < 8; ++src) px[src] = bld128_sc1(rp, lane * 16, src * (int)P_SRC + (dx * 2 + tpx) * 1024); }
        const v2u b2v = *(const GAS v2u*)((const char*)a.b2 + (16 * dt + 4 * g) * 2), b2x = *(const GAS v2u*)((const char*)a.b2 + (16 * dx + 4 * g) * 2);
        SB();
        auto fin = [&](const v4u (&p)[8], int d_t, int t_p, v2u b2, bool own) {
#pragma unroll
            for (int u = 0; u < 2; ++u) { const int tt = 2 * t_p + u;
                const v2u hv = lds64(smem + K4_X + (tt * 18 + (d_t >> 1)) * 1024 + ((2 * (d_t & 1) + (g >> 1)) * 16 + i) * 16 + (g & 1) * 8);
                f32x4 o = {blo(hv.x) + blo(b2.x), bhi(hv.x) + bhi(b2.x), blo(hv.y) + blo(b2.y), bhi(hv.y) + bhi(b2.y)};
                if (own) { if (A) o += acc[0][u]; else o += *(const LAS f32x4*)(smem + K4_OWN + (s * 2 + u) * 1024 + lane * 16); }
#pragma unroll
                for (int src = 0; src < 8; ++src) o += f32x4{blo(p[src][2 * u]), bhi(p[src][2 * u]), blo(p[src][2 * u + 1]), bhi(p[src][2 * u + 1])};
                *(GAS v2u*)((char*)a.out + (size_t)(64 * r + 16 * tt + i) * (D * 2) + (16 * d_t + 4 * g) * 2) = v2u{pk2(o[0], o[1]), pk2(o[2], o[3])};
            } };
        fin(pv, dt, tp, b2v, true);
        if (extra) fin(px, dx, tpx, b2x, false);
    }
}

__global__ __launch_bounds__(512, 2) void k2_rest(K2Args a) {
    extern __shared__ __attribute__((aligned(16))) char smem_[];
    LAS char* smem = (LAS char*)smem_;
    const unsigned lds0 = (unsigned)(size_t)smem_;
    char* ws = a.ws;
    const int tid = threadIdx.x, lane = tid & 63, w = __builtin_amdgcn_readfirstlane(tid >> 6), b = blockIdx.x;
    const int j = wg_j(b), r = wg_r(b);
    const rsrc_t rws = mkrsrc(ws, (unsigned)WS_END);
    {
        const int hl = w & 3, qh = w >> 2, l31 = lane & 31, hh = lane >> 5;
        const rsrc_t rk = mkrsrc(ws + OFF_QKV + (8 + j) * QKV_G, S * 144), rv = mkrsrc(ws + OFF_QKV + (16 + j) * QKV_G, S * 144);
        const int tok0 = tid / 9, mm = tid - 9 * tok0;
        v4u kk[6], vv[6];
#pragma unroll
        for (int m = 0; m < 6; ++m) { const int tok = tok0 + 56 * m, o_ = (64 * r - 256) * 144 + tid * 16 + m * 56 * 144;
            const int off = (tid < 504 && tok < 320 && o_ >= 0) ? o_ : 0x7ffffff0;
            kk[m] = bld128(rk, off, 0); vv[m] = bld128(rv, off, 0); }
        const char* qp = ws + OFF_QKV + j * QKV_G + (size_t)(64 * r + 32 * qh + l31) * 144 + 36 * hl;
        v4u q0, q1;
        { const GAS unsigned* qq = (const GAS unsigned*)(qp + 16 * hh); q0 = v4u{qq[0], qq[1], qq[2], qq[3]};
          const unsigned t = *(const GAS unsigned*)(qp + 32); q1 = v4u{hh == 0 ? t : 0u, 0u, 0u, 0u}; }
        const Unit ua = unit_decode(a.w1, a.w2, a.wo, w < 6 ? NEARLY + b * 6 + w : (w == 6 && b < 194) ? NEARLY + 1536 + b : NUNIT);
        for (int n = tid; n < 1280; n += 512) { *(LAS unsigned*)(smem + K2_K + n * 40 + 36) = 0u; *(LAS unsigned*)(smem + K2_V + n * 40 + 36) = 0x3f80u; }
        if (tid == 0) *(LAS unsigned*)(smem + K4_CNT) = 0u;
        int lo_[4];
#pragma unroll
        for (int e = 0; e < 4; ++e) { const int dw = 4 * mm + e, h = dw / 9; lo_[e] = (h * 320 + tok0) * 40 + (dw - 9 * h) * 4; }
        if (tid < 504) {
#pragma unroll
            for (int m = 0; m < 6; ++m) if (m < 5 || tok0 + 280 < 320) {
#pragma unroll
                for (int e = 0; e < 4; ++e) { *(LAS unsigned*)(smem + K2_K + lo_[e] + m * 56 * 40) = kk[m][e]; *(LAS unsigned*)(smem + K2_V + lo_[e] + m * 56 * 40) = vv[m][e]; } }
        }
        if (ua.live) unit_load(ua, lds0 + K2_SCR + w * 4096, lane);
        LBAR();
        f32x16 oacc;
        if (r < 4) attn_core<true>(smem, q0, q1, r, w, lane, oacc, ua, rws);
        else attn_core<false>(smem, q0, q1, r, w, lane, oacc, ua, rws);
        const float inv = __builtin_amdgcn_rcpf(lo32(oacc[10]));
        LAS char* op = smem + K2_O + (32 * qh + l31) * 144 + 36 * hl + 8 * hh;
        *(LAS unsigned*)(op) = pk2(oacc[0] * inv, oacc[1] * inv); *(LAS unsigned*)(op + 4) = pk2(oacc[2] * inv, oacc[3] * inv);
        *(LAS unsigned*)(op + 16) = pk2(oacc[4] * inv, oacc[5] * inv); *(LAS unsigned*)(op + 20) = pk2(oacc[6] * inv, oacc[7] * inv);
        if (hh == 0) *(LAS unsigned*)(op + 32) = pk2(oacc[8] * inv, oacc[9] * inv);
        LBAR();
#pragma unroll
        for (int m2 = 0; m2 < 2; ++m2) { const int n = tid + 512 * m2;
            if (n < 576) { const int i = n & 15, tt = (n >> 4) & 3, m = n >> 6;
                const v4u v = lds128(smem + K2_O + (16 * tt + i) * 144 + 16 * m);
                const int n8 = 72 * j + 8 * m, ks = n8 >> 5, g = (n8 & 31) >> 3;
                bst128<16>(v, rws, (int)OFF_O + (4 * r + tt) * (int)TT_FR + ks * 1024 + (g * 16 + i) * 16, 0); } }
        asm volatile("s_waitcnt vmcnt(0) lgkmcnt(0)\n\ts_barrier" ::: "memory");
    }
    if (w < 4) ffn_body<true>(a, smem, r, j, w, lane); else ffn_body<false>(a, smem, r, j, w, lane);
}

extern "C" void kernel_launch(void* const* d_in, const int* in_sizes, int n_in, void* d_out, int out_size, void* d_ws, size_t ws_size, hipStream_t stream) {
    (void)in_sizes; (void)n_in; (void)out_size; (void)ws_size;
    char* ws = (char*)d_ws;
    (void)hipFuncSetAttribute((const void*)k1_qkv, hipFuncAttributeMaxDynamicSharedMemorySize, K1_LDS);
    (void)hipFuncSetAttribute((const void*)k2_rest, hipFuncAttributeMaxDynamicSharedMemorySize, K2_LDS);
    K1Args a1{d_in[0], d_in[1], d_in[2], d_in[4], d_in[6], ws};
    k1_qkv<<<256, 512, K1_LDS, stream>>>(a1);
    K2Args a2{ws, d_in[0], d_in[2], d_in[4], d_in[6], d_in[5], d_in[7], d_out};
    k2_rest<<<256, 512, K2_LDS, stream>>>(a2);
}
```

```cpp
#include <hip/hip_runtime.h>
#include <hip/hip_bf16.h>
#include <stdint.h>

#define GAS __attribute__((address_space(1)))
#define LAS __attribute__((address_space(3)))
#define DI __device__ __forceinline__
typedef unsigned v4u __attribute__((ext_vector_type(4)));
typedef unsigned v2u __attribute__((ext_vector_type(2)));
typedef float f32x4 __attribute__((ext_vector_type(4)));
typedef float f32x16 __attribute__((ext_vector_type(16)));
typedef short bf16x8 __attribute__((ext_vector_type(8)));
typedef __bf16 bf2 __attribute__((ext_vector_type(2)));
typedef GAS unsigned gu32;
typedef __amdgpu_buffer_rsrc_t rsrc_t;

constexpr int S = 2048, D = 576, FF = 4608, N3 = 3 * D;
constexpr int NR = 32;
constexpr float LN_EPS = 1e-5f;
constexpr float QSCALE = 0.23570226039551584f * 1.4426950408889634f;

constexpr size_t FRAG = 1024;
constexpr size_t OFF_FLAG = 0;
constexpr int FL_O = 0, FL_H = 32, FL_P = 64, FL_N = 96;
constexpr size_t OFF_QKV = 65536;
constexpr size_t QKV_G = (size_t)S * 144;
constexpr int NSL = 8;
constexpr size_t OFF_WS = OFF_QKV + 24 * QKV_G;
constexpr size_t WS_J = (size_t)1296 * FRAG;
constexpr size_t OFF_WO = OFF_WS + NSL * WS_J;
constexpr size_t OFF_O = OFF_WO + (size_t)36 * 18 * FRAG;
constexpr size_t TT_FR = (size_t)18 * FRAG;
constexpr size_t OFF_H = OFF_O + 128 * TT_FR;
constexpr size_t OFF_P = OFF_H + 128 * TT_FR;
constexpr size_t P_SRC = (size_t)36 * 2 * FRAG;
constexpr size_t OFF_CSP = OFF_P + (size_t)NR * NSL * P_SRC;
constexpr size_t WS_END = OFF_CSP + (size_t)18 * FF * 4;

__host__ __device__ constexpr int ffn_stream_base(int wv) { return (wv < 4 ? wv * 180 : 720 + (wv - 4) * 144) * 1024; }
#define T0B 2
__host__ __device__ constexpr int ffn_pos1(bool a, int t, int ks) { const int t0 = a ? 3 : T0B, nt = a ? 5 : 4; return t < t0 ? ks * t0 + t : 18 * t0 + ks * (nt - t0) + (t - t0); }
__host__ __device__ constexpr int ffn_pos2(bool a, int t, int ks) { return (a ? 90 : 72) + (t < 4 ? (t >> 1) * 36 + ks * 2 + (t & 1) : 72 + ks); }
DI int wg_r(int b) { return 8 * ((b & 7) >> 1) + ((b >> 3) & 7); }
DI int wg_j(int b) { return 4 * (b & 1) + (b >> 6); }

DI f32x4 mfma16(v4u a, v4u b, f32x4 c) { return __builtin_amdgcn_mfma_f32_16x16x32_bf16(__builtin_bit_cast(bf16x8, a), __builtin_bit_cast(bf16x8, b), c, 0, 0, 0); }
DI f32x16 mfma32(v4u a, v4u b, f32x16 c) { return __builtin_amdgcn_mfma_f32_32x32x16_bf16(__builtin_bit_cast(bf16x8, a), __builtin_bit_cast(bf16x8, b), c, 0, 0, 0); }
DI unsigned pk2(float a, float b) { bf2 v = {(__bf16)a, (__bf16)b}; return __builtin_bit_cast(unsigned, v); }
DI float blo(unsigned u) { return __builtin_bit_cast(float, u << 16); }
DI float bhi(unsigned u) { return __builtin_bit_cast(float, u & 0xffff0000u); }
DI rsrc_t mkrsrc(const void* p, unsigned bytes) { return __builtin_amdgcn_make_buffer_rsrc((void*)p, 0, bytes, 0x00020000); }
DI v4u bld128(rsrc_t r, int voff, int soff) { return __builtin_amdgcn_raw_buffer_load_b128(r, voff, soff, 0); }
DI v4u bld128_sc1(rsrc_t r, int voff, int soff) { return __builtin_amdgcn_raw_buffer_load_b128(r, voff, soff, 16); }
DI v2u bld64(rsrc_t r, int voff, int soff) { return __builtin_amdgcn_raw_buffer_load_b64(r, voff, soff, 0); }
template <int AUX> DI void bst128(v4u v, rsrc_t r, int voff, int soff) { __builtin_amdgcn_raw_buffer_store_b128(v, r, voff, soff, AUX); asm volatile("s_nop 1" :: "v"(v)); }
DI v4u lds128(const LAS char* p) { return *(const LAS v4u*)p; }
DI v2u lds64(const LAS char* p) { return *(const LAS v2u*)p; }
DI v2u lds64v(const LAS char* p) { return *(const volatile LAS v2u*)p; }
typedef short v4i16_t __attribute__((ext_vector_type(4)));
DI v2u ldstr(const LAS char* p) { return __builtin_bit_cast(v2u, __builtin_amdgcn_ds_read_tr16_b64_v4i16((LAS v4i16_t*)p)); }
DI float dot2(unsigned a, float c) { return __builtin_amdgcn_fdot2_f32_bf16(__builtin_bit_cast(bf2, a), __builtin_bit_cast(bf2, 0x3f803f80u), c, false); }
DI void dma16(rsrc_t r, int voff, int soff, unsigned lds) {
    asm volatile("s_mov_b32 m0, %0\n\ts_nop 0\n\tbuffer_load_dwordx4 %1, %2, %3 offen lds" :: "s"(lds), "v"(voff), "s"(r), "s"(soff) : "memory");
}
#define WAITV(n) asm volatile("s_waitcnt vmcnt(" #n ")" ::: "memory")
#define BAR() asm volatile("s_barrier" ::: "memory")
#define LBAR() asm volatile("s_waitcnt lgkmcnt(0)\n\ts_barrier" ::: "memory")
#define SB() __builtin_amdgcn_sched_barrier(0)
#define PIN(x) asm volatile("" : "+v"(x))
DI float sum16(float v) { const unsigned u = __builtin_bit_cast(unsigned, v); const auto r = __builtin_amdgcn_permlane16_swap(u, u, false, false);
    return __builtin_bit_cast(float, (unsigned)r[0]) + __builtin_bit_cast(float, (unsigned)r[1]); }
DI float sum32(float v) { const unsigned u = __builtin_bit_cast(unsigned, v); const auto r = __builtin_amdgcn_permlane32_swap(u, u, false, false);
    return __builtin_bit_cast(float, (unsigned)r[0]) + __builtin_bit_cast(float, (unsigned)r[1]); }
DI float max32(float v) { const unsigned u = __builtin_bit_cast(unsigned, v); const auto r = __builtin_amdgcn_permlane32_swap(u, u, false, false);
    return __builtin_fmaxf(__builtin_bit_cast(float, (unsigned)r[0]), __builtin_bit_cast(float, (unsigned)r[1])); }
DI float lo32(float v) { const unsigned u = __builtin_bit_cast(unsigned, v); const auto r = __builtin_amdgcn_permlane32_swap(u, u, false, false);
    return __builtin_bit_cast(float, (unsigned)r[0]); }
typedef float f32x2 __attribute__((ext_vector_type(2)));
DI void ln_gelu4(f32x4 acc, f32x4 cs, float b0, float b1, float b2, float b3, float nmean, float rs, unsigned& o01, unsigned& o23) {
    float z0, z1, z2, z3, t0, t1, t2, t3;
    asm("v_fma_f32 %0, %16, %12, %8\n\tv_fma_f32 %1, %16, %13, %9\n\tv_fma_f32 %2, %16, %14, %10\n\tv_fma_f32 %3, %16, %15, %11\n\t"
        "v_fma_f32 %0, %17, %0, %18\n\tv_fma_f32 %1, %17, %1, %19\n\tv_fma_f32 %2, %17, %2, %20\n\tv_fma_f32 %3, %17, %3, %21\n\t"
        "v_mul_f32 %4, %0, %0\n\tv_mul_f32 %5, %1, %1\n\tv_mul_f32 %6, %2, %2\n\tv_mul_f32 %7, %3, %3\n\t"
        "v_fmaak_f32 %4, %22, %4, 0xc0135761\n\tv_fmaak_f32 %5, %22, %5, 0xc0135761\n\tv_fmaak_f32 %6, %22, %6, 0xc0135761\n\tv_fmaak_f32 %7, %22, %7, 0xc0135761\n\t"
        "v_mul_f32 %4, %0, %4\n\tv_mul_f32 %5, %1, %5\n\tv_mul_f32 %6, %2, %6\n\tv_mul_f32 %7, %3, %7\n\t"
        "v_exp_f32 %4, %4\n\tv_exp_f32 %5, %5\n\tv_exp_f32 %6, %6\n\tv_exp_f32 %7, %7\n\t"
        "v_add_f32 %4, 1.0, %4\n\tv_add_f32 %5, 1.0, %5\n\tv_add_f32 %6, 1.0, %6\n\tv_add_f32 %7, 1.0, %7\n\t"
        "v_rcp_f32 %4, %4\n\tv_rcp_f32 %5, %5\n\tv_rcp_f32 %6, %6\n\tv_rcp_f32 %7, %7\n\t"
        "v_mul_f32 %0, %0, %4\n\tv_mul_f32 %1, %1, %5\n\tv_mul_f32 %2, %2, %6\n\tv_mul_f32 %3, %3, %7"
        : "=&v"(z0), "=&v"(z1), "=&v"(z2), "=&v"(z3), "=&v"(t0), "=&v"(t1), "=&v"(t2), "=&v"(t3)
        : "v"(acc[0]), "v"(acc[1]), "v"(acc[2]), "v"(acc[3]), "v"(cs[0]), "v"(cs[1]), "v"(cs[2]), "v"(cs[3]), "v"(nmean), "v"(rs), "v"(b0), "v"(b1), "v"(b2), "v"(b3),
          "v"(-0.1029432397f));
    o01 = pk2(z0, z1); o23 = pk2(z2, z3);
}

struct Unit { rsrc_t rs; int soff, ld, w2, live; int dst[4]; int cs; };
constexpr int NUNIT = 1296 + 1296 + 162;
constexpr int NEARLY = 1024;
struct UnitTab { int v[NUNIT + 1][8]; };
constexpr UnitTab make_units() {
    UnitTab T{};
    for (int u = 0; u <= NUNIT; ++u) {
        const bool iswo = u < 162, isw1 = !iswo && u < 1458, isw2 = u >= 1458 && u < NUNIT;
        const int v = iswo ? u : isw1 ? u - 162 : u - 1458;
        const int j = isw1 ? (v % 72) / 9 : v / 162, ks = isw1 ? v / 72 : (v % 162) % 18, G = isw1 ? v % 9 : (v % 162) / 18;
        T.v[u][0] = isw1 ? 0 : isw2 ? 1 : iswo ? 2 : 3;
        T.v[u][1] = isw1 ? (32 * ks * FF + 576 * j + 64 * G) * 2 : ((isw2 ? 576 * j : 0) + 32 * ks) * (D * 2) + 128 * G;
        const int base = (int)OFF_WS + j * (int)WS_J, gi = G < j ? G : G - 1;
        const bool A = isw1 ? G < 5 : (G == j || gi < 4);
        const int fp = isw1 ? ffn_pos1(A, A ? G : G - 5, ks) : ffn_pos2(A, G == j ? 4 : gi & 3, ks);
        for (int c = 0; c < 4; ++c)
            T.v[u][2 + c] = (isw1 || isw2) ? base + ffn_stream_base(A ? c : 4 + c) + fp * 1024 : iswo ? (int)OFF_WO + ((4 * G + c) * 18 + ks) * 1024 : 0x7fff0000;
        T.v[u][6] = isw1 ? (int)OFF_CSP + (ks * FF + 576 * j + 64 * G) * 4 : 0x7fff0000;
        T.v[u][7] = 0;
    }
    return T;
}
__device__ const UnitTab UNITS = make_units();
DI Unit unit_decode(const void* w1, const void* w2, const void* wo, int u) {
    const int* e = UNITS.v[u < NUNIT ? u : NUNIT];
    const int kind = e[0];
    Unit r;
    r.rs = mkrsrc(kind == 0 ? w1 : kind == 1 ? w2 : wo, kind == 3 ? 0u : kind == 2 ? D * D * 2 : D * FF * 2);
    r.ld = kind == 0 ? FF * 2 : D * 2; r.w2 = kind == 1; r.live = kind != 3; r.soff = e[1];
#pragma unroll
    for (int c = 0; c < 4; ++c) r.dst[c] = e[2 + c];
    r.cs = e[6];
    return r;
}
#define UST 16
DI void unit_load(const Unit& t, unsigned scr, int lane) {
    const int voff = (lane >> 3) * t.ld + (lane & 7) * 16;
#pragma unroll
    for (int m = 0; m < 4; ++m) dma16(t.rs, voff, t.soff + 8 * m * t.ld, scr + m * 1024);
}
DI void unit_store(const Unit& t, const LAS char* scr, rsrc_t rws, int lane) {
    const int g = lane >> 4, q = (lane & 15) >> 2, p = lane & 3;
    const int row0 = t.w2 ? 4 * g + q : 8 * g + q, rstep = t.w2 ? 16 : 4;
    float cs[4];
#pragma unroll
    for (int c = 0; c < 4; ++c) {
        const v2u lo = ldstr(scr + row0 * 128 + (16 * c + 4 * p) * 2);
        const v2u hi = ldstr(scr + (row0 + rstep) * 128 + (16 * c + 4 * p) * 2);
        bst128<UST>(v4u{lo.x, lo.y, hi.x, hi.y}, rws, lane * 16, t.dst[c]);
        cs[c] = sum32(sum16(dot2(hi.y, dot2(hi.x, dot2(lo.y, dot2(lo.x, 0.f))))));
    }
    const float mine = g == 0 ? cs[0] : g == 1 ? cs[1] : g == 2 ? cs[2] : cs[3];
    __builtin_amdgcn_raw_buffer_store_b32(__builtin_bit_cast(unsigned, mine), rws, lane * 4, t.cs, 16);
}

DI void unit_store_w2(const Unit& t, const LAS char* scr, rsrc_t rws, int lane) {
    const int g = lane >> 4, q = (lane & 15) >> 2, p = lane & 3;
    v2u lo[4], hi[4];
#pragma unroll
    for (int c = 0; c < 4; ++c) { lo[c] = ldstr(scr + (4 * g + q) * 128 + 32 * c + 8 * p); hi[c] = ldstr(scr + (4 * g + q + 16) * 128 + 32 * c + 8 * p); }
#pragma unroll
    for (int c = 0; c < 4; ++c) bst128<UST>(v4u{lo[c].x, lo[c].y, hi[c].x, hi[c].y}, rws, lane * 16, t.dst[c]);
}

#define QST 0
struct K1Args { const void* x; const void* wqkv; const void* wo; const void* w1; const void* w2; char* ws; };
constexpr int K1_STAGE = 32768;
constexpr int K1_TAB = 4 * K1_STAGE;
constexpr int K1_LDS = K1_TAB + 4096;

__global__ __launch_bounds__(512, 2) void k1_qkv(K1Args a) {
    extern __shared__ __attribute__((aligned(16))) char smem_[];
    LAS char* smem = (LAS char*)smem_;
    const unsigned lds0 = (unsigned)(size_t)smem_;
    const int tid = threadIdx.x, lane = tid & 63, w = __builtin_amdgcn_readfirstlane(tid >> 6), b = blockIdx.x;
    if (b == 0 && tid < FL_N) *(GAS unsigned*)(a.ws + OFF_FLAG + tid * 256) = 0u;
    if (b >= 224) {
        const rsrc_t rws = mkrsrc(a.ws, (unsigned)WS_END);
        const int u0 = ((b - 224) * 8 + w) * 4;
        Unit t[4];
#pragma unroll
        for (int m = 0; m < 4; ++m) { t[m] = unit_decode(a.w1, a.w2, a.wo, u0 + m); unit_load(t[m], lds0 + w * 16384 + m * 4096, lane); }
        WAITV(12); unit_store(t[0], smem + w * 16384, rws, lane);
        WAITV(13); unit_store(t[1], smem + w * 16384 + 4096, rws, lane);
        WAITV(14); unit_store(t[2], smem + w * 16384 + 8192, rws, lane);
        WAITV(15); unit_store(t[3], smem + w * 16384 + 12288, rws, lane);
        return;
    }
    const int xc = b & 7, idx = b >> 3;
    const int rt = 4 * (xc >> 1) + (idx & 3), ct = 7 * (xc & 1) + (idx >> 2);
    if (w >= 4) {
        const int lw = w - 4;
        __builtin_amdgcn_s_setprio(1);
        const rsrc_t rx = mkrsrc(a.x, S * D * 2), rw = mkrsrc(a.wqkv, D * N3 * 2);
        const int vx0 = (128 * rt + 32 * lw + (lane >> 3)) * (D * 2) + ((lane & 7) ^ (lane >> 4)) * 16, vx1 = vx0 ^ 64;
        const int c16 = lane & 15, q = lane >> 4;
        const int vw0 = (16 * lw + q) * (N3 * 2) + 256 * ct + ((((c16 >> 1) ^ (q << 1)) << 1) | (c16 & 1)) * 16;
        const int vw1 = (16 * lw + q) * (N3 * 2) + 256 * ct + ((((c16 >> 1) ^ ((q << 1) | 1)) << 1) | (c16 & 1)) * 16;
        auto stage = [&](int s) {
            const unsigned sb = lds0 + (s & 3) * K1_STAGE;
#pragma unroll
            for (int m = 0; m < 4; ++m) {
                dma16(rw, m < 2 ? vw0 : vw1, (64 * s + 4 * m) * (N3 * 2), sb + 16384 + (4 * lw + m) * 1024);
                dma16(rx, (m & 1) ? vx1 : vx0, 128 * s + m * 8 * (D * 2), sb + (4 * lw + m) * 1024);
            }
        };
        const int g = lane >> 4, i = lane & 15, q4 = i >> 2, p = lane & 3;
        const int sxo = (32 * lw + i) * 128, swo = 16384 + (8 * g + q4) * 256 + 8 * p, key = (q4 << 1) | (g & 1);
        const v4u ones = {0x3f803f80u, 0x3f803f80u, 0x3f803f80u, 0x3f803f80u};
        f32x4 sx[2], sq[2], cs[2];
#pragma unroll
        for (int u = 0; u < 2; ++u) sx[u] = sq[u] = cs[u] = f32x4{0.f, 0.f, 0.f, 0.f};
        auto stats = [&](int s) {
            const LAS char* sb = smem + (s & 3) * K1_STAGE;
#pragma unroll
            for (int k2 = 0; k2 < 2; ++k2)
#pragma unroll
                for (int u = 0; u < 2; ++u) {
                    const v4u xv = lds128(sb + sxo + u * 2048 + ((4 * k2 + g) ^ ((i >> 1) & 7)) * 16);
                    const LAS char* ap = sb + swo + k2 * 8192 + (((2 * lw + u) ^ key) * 32);
                    const v2u lo = ldstr(ap), hi = ldstr(ap + 1024);
                    sx[u] = mfma16(ones, xv, sx[u]); sq[u] = mfma16(xv, xv, sq[u]); cs[u] = mfma16(v4u{lo.x, lo.y, hi.x, hi.y}, ones, cs[u]);
                }
        };
        stage(0); stage(1); stage(2);
        WAITV(16); BAR();
        WAITV(8); BAR();
        stats(0);
#pragma unroll
        for (int s = 3; s < 9; ++s) { stage(s); stats(s - 2); WAITV(8); asm volatile("s_waitcnt lgkmcnt(0)" ::: "memory"); BAR(); }
        stats(7); WAITV(0); BAR();
        stats(8);
        LAS float* tabx = (LAS float*)(smem + K1_TAB);
        LAS float* tabc = tabx + 256;
#pragma unroll
        for (int u = 0; u < 2; ++u) {
            const float s2 = (i & 2) ? ((i & 1) ? sq[u][3] : sq[u][2]) : ((i & 1) ? sq[u][1] : sq[u][0]);
            const float mean = sx[u][0] * (1.0f / D), var = __builtin_fmaxf(__builtin_fmaf(-mean, mean, s2 * (1.0f / D)), 0.f);
            if (g == q4) *(LAS f32x2*)(tabx + (32 * lw + 16 * u + i) * 2) = f32x2{mean, __builtin_amdgcn_rsqf(var + LN_EPS)};
            if (i == 0) *(LAS f32x4*)(tabc + 32 * lw + 16 * u + 4 * g) = cs[u]; }
        LBAR();
        return;
    }
    const int th = w & 1, ch = w >> 1, h = lane >> 5, l31 = lane & 31;
    const int q = (lane & 15) >> 2, p = lane & 3, blk = (lane >> 4) & 1;
    const int xrow = (64 * th + l31) * 128, xkey = (l31 >> 1) & 7;
    const int woff = 16384 + (8 * h + q) * 256 + (((4 * ch + blk) ^ ((q << 1) | h)) * 32) + 8 * p;
    f32x16 acc[2][2];
#pragma unroll
    for (int cx = 0; cx < 2; ++cx)
#pragma unroll
        for (int ty = 0; ty < 2; ++ty)
#pragma unroll
            for (int e = 0; e < 16; ++e) acc[cx][ty][e] = 0.f;
    int xo[4];
#pragma unroll
    for (int k4 = 0; k4 < 4; ++k4) xo[k4] = xrow + ((2 * k4 + h) ^ xkey) * 16;
    v4u bA[2][2], aA[2][2], bB[2][2], aB[2][2];
    auto rd = [&](v4u (&bf)[2][2], v4u (&af)[2][2], const LAS char* sb, int k2) {
#pragma unroll
        for (int u = 0; u < 2; ++u) {
#pragma unroll
            for (int cx = 0; cx < 2; ++cx) { const LAS char* ap = sb + ((woff ^ (cx << 6)) + (2 * k2 + u) * 4096);
                const v2u lo = ldstr(ap), hi = ldstr(ap + 1024); af[u][cx] = v4u{lo.x, lo.y, hi.x, hi.y}; }
#pragma unroll
            for (int ty = 0; ty < 2; ++ty) bf[u][ty] = lds128(sb + xo[2 * k2 + u] + ty * 4096);
        }
    };
    auto mm = [&](const v4u (&bf)[2][2], const v4u (&af)[2][2]) {
#pragma unroll
        for (int u = 0; u < 2; ++u)
#pragma unroll
            for (int cx = 0; cx < 2; ++cx)
#pragma unroll
                for (int ty = 0; ty < 2; ++ty) acc[cx][ty] = mfma32(af[u][cx], bf[u][ty], acc[cx][ty]);
    };
    int dst[2][2];
#pragma unroll
    for (int cx = 0; cx < 2; ++cx)
#pragma unroll
        for (int m = 0; m < 2; ++m) { const int n8 = 128 * ct + 64 * ch + 32 * cx + 16 * m + 8 * h, ja = n8 / 72;
            dst[cx][m] = n8 < N3 ? (int)OFF_QKV + ja * (int)QKV_G + (128 * rt + 64 * th + l31) * 144 + (n8 - 72 * ja) * 2 : 0x7fff0000; }
    auto half = [&](v4u (&bn)[2][2], v4u (&an)[2][2], const LAS char* nb, int k2, const v4u (&bc)[2][2], const v4u (&ac)[2][2]) {
        SB();
        rd(bn, an, nb, k2);
        mm(bc, ac);
#pragma unroll
        for (int e = 0; e < 4; ++e) { __builtin_amdgcn_sched_group_barrier(0x008, 1, 0); __builtin_amdgcn_sched_group_barrier(0x100, 2, 0); }
#pragma unroll
        for (int e = 0; e < 4; ++e) { __builtin_amdgcn_sched_group_barrier(0x008, 1, 0); __builtin_amdgcn_sched_group_barrier(0x100, 1, 0); }
        SB();
    };
#define K1_SYNC() asm volatile("s_waitcnt lgkmcnt(12)\n\ts_barrier" ::: "memory")
    BAR();
    unsigned pf[2];
#pragma unroll
    for (int m = 0; m < 2; ++m) {
        const int lx = (idx >> 2) * 55 + tid, lw2 = (idx & 3) * 96 + (tid - 55), wo = (192 + 192 * m + (lw2 >> 1)) * (N3 * 2) + 256 * ct + (lw2 & 1) * 128;
        const char* p = tid < 55 ? (const char*)a.x + (size_t)(128 * rt + (lx < 384 ? lx / 3 : 0)) * (D * 2) + (3 + 3 * m + lx % 3) * 128
                                 : (const char*)a.wqkv + ((tid < 151 && wo < D * N3 * 2) ? wo : 0);
        asm volatile("global_load_dword %0, %1, off" : "=v"(pf[m]) : "v"(p));
    }
    __builtin_amdgcn_s_waitcnt(0xC07F);
    rd(bA, aA, smem, 0);
    half(bB, aB, smem, 1, bA, aA);
    BAR();
    half(bA, aA, smem + K1_STAGE, 0, bB, aB);
    K1_SYNC();
#pragma nounroll
    for (int s = 1; s < 7; ++s) {
        half(bB, aB, smem + (s & 3) * K1_STAGE, 1, bA, aA);
        half(bA, aA, smem + ((s + 1) & 3) * K1_STAGE, 0, bB, aB);
        K1_SYNC();
    }
    half(bB, aB, smem + 3 * K1_STAGE, 1, bA, aA);
    half(bA, aA, smem, 0, bB, aB);
    half(bB, aB, smem, 1, bA, aA);
    SB(); mm(bB, aB); SB();
    asm volatile("s_waitcnt vmcnt(0)" :: "v"(pf[0]), "v"(pf[1]));
    LBAR();
    const LAS float* tabc = (const LAS float*)(smem + K1_TAB) + 256;
    const LAS float* tabx = (const LAS float*)(smem + K1_TAB);
    const rsrc_t rws = mkrsrc(a.ws, (unsigned)OFF_WS);
    f32x4 cv[2][4]; f32x2 mr[2];
#pragma unroll
    for (int cx = 0; cx < 2; ++cx)
#pragma unroll
        for (int qd = 0; qd < 4; ++qd) cv[cx][qd] = *(const LAS f32x4*)(tabc + 64 * ch + 32 * cx + 8 * qd + 4 * h);
#pragma unroll
    for (int ty = 0; ty < 2; ++ty) mr[ty] = *(const LAS f32x2*)(tabx + (64 * th + 32 * ty + l31) * 2);
    SB();
#pragma unroll
    for (int ty = 0; ty < 2; ++ty) {
        const float mean = mr[ty][0], rstd = mr[ty][1];
#pragma unroll
        for (int cx = 0; cx < 2; ++cx) {
            const float rs = rstd * (128 * ct + 64 * ch + 32 * cx < D ? QSCALE : 1.0f);
            unsigned pk[8];
#pragma unroll
            for (int qd = 0; qd < 4; ++qd) { const f32x4 c = cv[cx][qd];
                pk[2 * qd] = pk2(rs * __builtin_fmaf(-mean, c[0], acc[cx][ty][4 * qd]), rs * __builtin_fmaf(-mean, c[1], acc[cx][ty][4 * qd + 1]));
                pk[2 * qd + 1] = pk2(rs * __builtin_fmaf(-mean, c[2], acc[cx][ty][4 * qd + 2]), rs * __builtin_fmaf(-mean, c[3], acc[cx][ty][4 * qd + 3])); }
#pragma unroll
            for (int m = 0; m < 2; ++m) {
                const auto s0 = __builtin_amdgcn_permlane32_swap(pk[4 * m], pk[4 * m + 2], false, false), s1 = __builtin_amdgcn_permlane32_swap(pk[4 * m + 1], pk[4 * m + 3], false, false);
                bst128<QST>(v4u{(unsigned)s0[0], (unsigned)s1[0], (unsigned)s0[1], (unsigned)s1[1]}, rws, dst[cx][m], ty * 32 * 144); }
        }
    }
}

struct K2Args { char* ws; const void* x; const void* wo; const void* w1; const void* w2; const void* b1; const void* b2; void* out; };
constexpr int KV_HEAD = 320 * 40;
constexpr int K2_K = 0, K2_V = 4 * KV_HEAD, K2_O = 8 * KV_HEAD, K2_SCR = K2_O + 64 * 144;
constexpr int K4_X = 0, K4_H = 73728, K4_ST = 147456, K4_CS = K4_ST + 512, K4_CNT = K4_CS + 2304, K4_OWN = K4_CNT + 256, K2_LDS = K4_OWN + 8192;
static_assert(K2_SCR + 8 * 4096 <= K4_ST, "LDS map");

DI void flag_add(char* ws, int f) { __hip_atomic_fetch_add((gu32*)(ws + OFF_FLAG + f * 256), 1u, __ATOMIC_RELAXED, __HIP_MEMORY_SCOPE_AGENT); }
DI void flag_wait(char* ws, int f, unsigned n) {
    gu32* p = (gu32*)(ws + OFF_FLAG + f * 256);
    for (unsigned spins = 0; __hip_atomic_load(p, __ATOMIC_RELAXED, __HIP_MEMORY_SCOPE_AGENT) < n && spins < (1u << 20); ++spins) __builtin_amdgcn_s_sleep(1);
    __builtin_amdgcn_fence(__ATOMIC_ACQUIRE, "wavefront");
}
DI void flag_wait_all(char* ws, int f, unsigned n, int lane) {
    gu32* p = (gu32*)(ws + OFF_FLAG + (f + (lane & 31)) * 256);
    for (unsigned spins = 0; !__all(__hip_atomic_load(p, __ATOMIC_RELAXED, __HIP_MEMORY_SCOPE_AGENT) >= n) && spins < (1u << 20); ++spins) __builtin_amdgcn_s_sleep(1);
    __builtin_amdgcn_fence(__ATOMIC_ACQUIRE, "wavefront");
}
DI void tile_in(const char* src, LAS char* smem, int tid) {
    const rsrc_t rs = mkrsrc(src, 4 * (unsigned)TT_FR);
    v4u t[9];
#pragma unroll
    for (int m = 0; m < 9; ++m) t[m] = bld128_sc1(rs, tid * 16, m * 8192);
#pragma unroll
    for (int m = 0; m < 9; ++m) *(LAS v4u*)(smem + tid * 16 + m * 8192) = t[m];
}

template <bool EDGE> DI void attn_core(LAS char* smem, const v4u q0, const v4u q1, int r, int w, int lane, f32x16& oacc, const Unit& ua, rsrc_t rws) {
    const int hl = w & 3, qh = w >> 2, l31 = lane & 31, hh = lane >> 5;
    const int tmin = EDGE ? ((256 - 64 * r - 32 * qh) >> 5) : 0;
    const float ninf = -__builtin_inff();
    const LAS char* kp = smem + K2_K + (hl * 320 + 32 * qh + l31) * 40;
    v4u ka[9], kb[9];
#pragma unroll
    for (int t = 0; t < 9; ++t) { const v2u a0 = lds64v(kp + t * 1280 + 16 * hh), a1 = lds64v(kp + t * 1280 + 16 * hh + 8);
        ka[t] = v4u{a0.x, a0.y, a1.x, a1.y};
        const v2u c0 = lds64v(kp + t * 1280 + 32), c1 = lds64v(kp + t * 1280 + 24);
        kb[t] = v4u{c0.x, c0.y, c1.x, c1.y}; }
    auto score = [&](int t, const f32x16& c) { return mfma32(kb[t], q1, mfma32(ka[t], q0, c)); };
    bool le[16];
#pragma unroll
    for (int rg = 0; rg < 16; ++rg) le[rg] = (rg & 3) + 8 * (rg >> 2) + 4 * hh <= l31;
    auto merged = [&](const f32x16& c) { const f32x16 s8 = score(8, c); f32x16 s0;
        if (EDGE) { s0 = s8;
#pragma unroll
            for (int rg = 0; rg < 16; ++rg) s0[rg] = ninf; } else s0 = score(0, c);
        f32x16 m;
#pragma unroll
        for (int rg = 0; rg < 16; ++rg) m[rg] = le[rg] ? s8[rg] : s0[rg];
        return m; };
    const f32x16 zero = {};
    float mx;
    { const f32x16 m = merged(zero); mx = __builtin_fmaxf(m[0], m[1]);
#pragma unroll
      for (int rg = 2; rg < 16; rg += 2) mx = __builtin_fmaxf(__builtin_fmaxf(mx, m[rg]), m[rg + 1]); }
    if (!EDGE) {
        f32x16 sc = score(1, zero); float m2 = mx;
#pragma unroll
        for (int t = 1; t < 8; ++t) { f32x16 sn; if (t < 7) sn = score(t + 1, zero);
            SB();
#pragma unroll
            for (int rg = 0; rg < 16; rg += 4) { mx = __builtin_fmaxf(__builtin_fmaxf(mx, sc[rg]), sc[rg + 1]); m2 = __builtin_fmaxf(__builtin_fmaxf(m2, sc[rg + 2]), sc[rg + 3]); }
            SB();
            if (t < 7) sc = sn; }
        mx = __builtin_fmaxf(mx, m2);
    } else {
#pragma unroll
        for (int t = 1; t < 8; ++t) if (t >= tmin) { const f32x16 s = score(t, zero);
#pragma unroll
            for (int rg = 0; rg < 16; rg += 2) mx = __builtin_fmaxf(__builtin_fmaxf(mx, s[rg]), s[rg + 1]); }
    }
    mx = max32(mx);
    if (ua.live) { WAITV(0); if (ua.w2) unit_store_w2(ua, smem + K2_SCR + w * 4096, rws, lane); else unit_store(ua, smem + K2_SCR + w * 4096, rws, lane); }
    f32x16 cm;
#pragma unroll
    for (int rg = 0; rg < 16; ++rg) cm[rg] = -mx;
    const LAS char* vp = smem + K2_V + (hl * 320 + 32 * qh + 4 * hh + ((lane & 15) >> 2)) * 40 + (((lane >> 4) & 1) ? 32 : 8 * (lane & 3));
    auto pv = [&](int t, const unsigned (&pw)[8]) {
#pragma unroll
        for (int s = 0; s < 2; ++s) {
            const v2u lo = ldstr(vp + (t * 32 + 16 * s) * 40), hi = ldstr(vp + (t * 32 + 16 * s + 8) * 40);
            oacc = mfma32(v4u{lo.x, lo.y, hi.x, hi.y}, v4u{pw[4 * s], pw[4 * s + 1], pw[4 * s + 2], pw[4 * s + 3]}, oacc);
        } };
    oacc = zero;
    { const f32x16 m = merged(cm); unsigned pw[8], p8[8], p0[8];
#pragma unroll
      for (int e = 0; e < 8; ++e) { pw[e] = pk2(__builtin_amdgcn_exp2f(m[2 * e]), __builtin_amdgcn_exp2f(m[2 * e + 1]));
          const unsigned mk = (le[2 * e] ? 0xffffu : 0u) | (le[2 * e + 1] ? 0xffff0000u : 0u); p8[e] = pw[e] & mk; p0[e] = pw[e] & ~mk; }
      pv(8, p8); if (!EDGE) pv(0, p0); }
    if (!EDGE) {
        f32x16 sc = score(1, cm);
#pragma unroll
        for (int t = 1; t < 8; ++t) { f32x16 sn; if (t < 7) sn = score(t + 1, cm);
            SB();
            unsigned pw[8];
#pragma unroll
            for (int e = 0; e < 8; ++e) pw[e] = pk2(__builtin_amdgcn_exp2f(sc[2 * e]), __builtin_amdgcn_exp2f(sc[2 * e + 1]));
            pv(t, pw);
            SB();
            if (t < 7) sc = sn; }
    } else {
#pragma unroll
        for (int t = 1; t < 8; ++t) if (t >= tmin) { const f32x16 s = score(t, cm); unsigned pw[8];
#pragma unroll
            for (int e = 0; e < 8; ++e) pw[e] = pk2(__builtin_amdgcn_exp2f(s[2 * e]), __builtin_amdgcn_exp2f(s[2 * e + 1]));
            pv(t, pw); }
    }
}

#define RD 24
static_assert(162 + 72 * ((RD - 1) / 3) + 9 * 7 + 2 < NEARLY && 162 + 72 * ((RD - 1) / T0B) + 9 * 7 + 5 + T0B - 1 < NEARLY, "early share");

template <int T, int OFS, class Side>
DI void ffn_pass(f32x4 (&acc)[3][4], v4u (&ring)[RD], rsrc_t rw, int pos, const LAS char* bp, int lane, Side side) {
    v4u bf[4];
#pragma unroll
    for (int tt = 0; tt < 4; ++tt) bf[tt] = lds128(bp + tt * 18432);
#pragma unroll
    for (int t = 0; t < T; ++t)
#pragma unroll
        for (int tt = 0; tt < 4; ++tt) acc[t][tt] = f32x4{0.f, 0.f, 0.f, 0.f};
#pragma unroll
    for (int u = 0; u < 18; ++u) {
        SB();
        v4u bn[4];
        if (u + 1 < 18) {
#pragma unroll
            for (int tt = 0; tt < 4; ++tt) bn[tt] = lds128(bp + tt * 18432 + (u + 1) * 1024);
        }
#pragma unroll
        for (int t = 0; t < T; ++t) {
            const int n = u * T + t, sl = (n + OFS) % RD;
#pragma unroll
            for (int tt = 0; tt < 4; ++tt) acc[t][tt] = mfma16(ring[sl], bf[tt], acc[t][tt]);
            ring[sl] = bld128_sc1(rw, lane * 16 + (n & 3) * 1024, pos + (n >> 2) * 4096);
        }
        side(u);
#pragma unroll
        for (int t = 0; t < T; ++t) {
            if (T == 1) {
#pragma unroll
                for (int e = 0; e < 4; ++e) { __builtin_amdgcn_sched_group_barrier(0x008, 1, 0); __builtin_amdgcn_sched_group_barrier(0x100, 1, 0); }
            } else if (T == 2) {
#pragma unroll
                for (int e = 0; e < 2; ++e) { __builtin_amdgcn_sched_group_barrier(0x008, 2, 0); __builtin_amdgcn_sched_group_barrier(0x100, 1, 0); }
            } else { __builtin_amdgcn_sched_group_barrier(0x008, 4, 0); if (t < 4) __builtin_amdgcn_sched_group_barrier(0x100, 1, 0); }
            __builtin_amdgcn_sched_group_barrier(0x020, 1, 0);
        }
        SB();
        if (u + 1 < 18) {
#pragma unroll
            for (int tt = 0; tt < 4; ++tt) bf[tt] = bn[tt];
        }
    }
#pragma unroll
    for (int t = 0; t < T; ++t)
#pragma unroll
        for (int tt = 0; tt < 4; ++tt) PIN(acc[t][tt]);
    asm volatile("s_nop 7" ::: "memory");
}

template <bool A> DI void ffn_body(const K2Args& a, LAS char* smem, int r, int j, int w, int lane) {
    constexpr int NT = A ? 5 : 4;
    const int tid = threadIdx.x, g = lane >> 4, i = lane & 15, s = w & 3;
    char* ws = a.ws;
    const rsrc_t rws = mkrsrc(ws, (unsigned)WS_END);
    v4u ring[RD];
    {
        if (tid == 448) { flag_add(ws, FL_O + r); flag_wait(ws, FL_O + r, 8); }
        const int dt = A ? 4 * j + w : 32 + (j >> 1), tt0 = A ? 0 : 2 * (j & 1);
        const rsrc_t rw = mkrsrc(ws + OFF_WO, 36 * 18 * 1024), rx = mkrsrc(a.x, S * D * 2);
        v4u af[18]; v2u xr[4];
        if (A || w == 4) {
#pragma unroll
            for (int ks = 0; ks < 18; ++ks) af[ks] = bld128(rw, lane * 16 + (ks & 3) * 1024, dt * 18432 + (ks >> 2) * 4096);
#pragma unroll
            for (int u = 0; u < 4; ++u) xr[u] = bld64(rx, (64 * r + 16 * ((tt0 + u) & 3) + i) * (D * 2) + (16 * dt + 4 * g) * 2, 0);
        }
        BAR();
        tile_in(ws + OFF_O + 4 * r * TT_FR, smem, tid);
        LBAR();
        const rsrc_t rf = mkrsrc(ws + OFF_WS + j * WS_J + ffn_stream_base(w), (A ? 180 : 144) * 1024);
        auto fill = [&](int n) { ring[n] = bld128_sc1(rf, lane * 16 + (n & 3) * 1024, (n >> 2) * 4096); };
        if (!(A || w == 4)) {
#pragma unroll
            for (int n = 0; n < RD / 2; ++n) fill(n);
            if (w == 7) flag_wait_all(ws, FL_O, 8, lane);
        }
        if (A || w == 4) {
            f32x4 acc[4];
#pragma unroll
            for (int u = 0; u < 4; ++u) acc[u] = f32x4{0.f, 0.f, 0.f, 0.f};
            const LAS char* bp = smem + lane * 16;
            int to[4];
#pragma unroll
            for (int u = 0; u < 4; ++u) to[u] = ((tt0 + u) & 3) * 18432;
            v4u bf[2][4];
#pragma unroll
            for (int u = 0; u < 4; ++u) bf[0][u] = lds128(bp + to[u]);
#pragma unroll
            for (int ks = 0; ks < 18; ++ks) {
                SB();
                if (ks < 17) {
#pragma unroll
                    for (int u = 0; u < 4; ++u) bf[(ks + 1) & 1][u] = lds128(bp + (ks + 1) * 1024 + to[u]); }
#pragma unroll
                for (int u = 0; u < 4; ++u) acc[u] = mfma16(af[ks], bf[ks & 1][u], acc[u]);
                if (ks < RD / 2) fill(ks);
#pragma unroll
                for (int u = 0; u < 4; ++u) { __builtin_amdgcn_sched_group_barrier(0x008, 1, 0); __builtin_amdgcn_sched_group_barrier(0x100, 1, 0); }
                SB();
            }
#pragma unroll
            for (int u = 0; u < 4; ++u) if (A || u < 2) { const int tt = (tt0 + u) & 3;
                const v2u o = {pk2(acc[u][0] + blo(xr[u].x), acc[u][1] + bhi(xr[u].x)), pk2(acc[u][2] + blo(xr[u].y), acc[u][3] + bhi(xr[u].y))};
                __builtin_amdgcn_raw_buffer_store_b64(o, rws, (int)OFF_H + (4 * r + tt) * (int)TT_FR + (dt >> 1) * 1024 + ((2 * (dt & 1) + (g >> 1)) * 16 + i) * 16 + (g & 1) * 8, 0, 16); }
        }
        SB();
        if (w != 7) {
#pragma unroll
            for (int n = RD / 2; n < RD; ++n) fill(n); }
        SB();
        asm volatile("s_waitcnt vmcnt(%0) lgkmcnt(0)\n\ts_barrier" :: "n"(RD - RD / 2) : "memory");
        if (w == 7) {
            if (lane == 0) { flag_add(ws, FL_H + r); flag_wait(ws, FL_H + r, 8); }
            SB();
#pragma unroll
            for (int n = RD / 2; n < RD; ++n) fill(n); }
    }
    const rsrc_t rw = mkrsrc(ws + OFF_WS + j * WS_J + ffn_stream_base(w), (A ? 180 : 144) * 1024);
    BAR();
    tile_in(ws + OFF_H + 4 * r * TT_FR, smem + K4_X, tid);
    LBAR();
    v2u bb[NT];
#pragma unroll
    for (int t = 0; t < NT; ++t) bb[t] = *(const GAS v2u*)((const char*)a.b1 + (576 * j + 16 * (4 * ((A ? 0 : 5) + t) + s) + 4 * g) * 2);
    unsigned pc[3][18];
    const rsrc_t rc = mkrsrc(ws + OFF_CSP, 18 * FF * 4);
    if (!A) {
#pragma unroll
        for (int m = 0; m < 3; ++m) if (m < 2 || w == 4) {
#pragma unroll
            for (int ks = 0; ks < 18; ++ks) pc[m][ks] = __builtin_amdgcn_raw_buffer_load_b32(rc, (576 * j + 256 * m + tid - 256) * 4, ks * FF * 4, 16); } }
    SB();
    LAS float* st = (LAS float*)(smem + K4_ST);
    if (!A) {
        f32x4 s1 = {0.f, 0.f, 0.f, 0.f}, s2 = s1;
        const v4u ones = {0x3f803f80u, 0x3f803f80u, 0x3f803f80u, 0x3f803f80u};
#pragma unroll
        for (int kh = 0; kh < 2; ++kh) { v4u v[9];
#pragma unroll
            for (int k = 0; k < 9; ++k) v[k] = lds128(smem + K4_X + (s * 18 + 9 * kh + k) * 1024 + lane * 16);
#pragma unroll
            for (int k = 0; k < 9; ++k) { s1 = mfma16(ones, v[k], s1); s2 = mfma16(v[k], v[k], s2); } }
        const float sq = (i & 2) ? ((i & 1) ? s2[3] : s2[2]) : ((i & 1) ? s2[1] : s2[0]);
        const float mean = s1[0] * (1.0f / D), var = __builtin_fmaxf(__builtin_fmaf(-mean, mean, sq * (1.0f / D)), 0.f);
        if (g == (i >> 2)) *(LAS f32x2*)(st + (16 * s + i) * 2) = f32x2{mean, __builtin_amdgcn_rsqf(var + LN_EPS)};
    }
    if (!A) {
#pragma unroll
        for (int m = 0; m < 3; ++m) if (m < 2 || w == 4) { float sum = 0.f;
#pragma unroll
            for (int ks = 0; ks < 18; ++ks) sum += __builtin_bit_cast(float, pc[m][ks]);
            *(LAS float*)(smem + K4_CS + (256 * m + tid - 256) * 4) = sum; }
        asm volatile("s_waitcnt lgkmcnt(0)" ::: "memory");
        if (lane == 0) __hip_atomic_fetch_add((LAS unsigned*)(smem + K4_CNT), 1u, __ATOMIC_RELAXED, __HIP_MEMORY_SCOPE_WORKGROUP); }
    if (A) __builtin_amdgcn_s_setprio(1);
    f32x4 acc[3][4];
    auto none = [](int) {};
    f32x2 mr[4];
    auto act = [&](int t0, int nt) {
        f32x4 cs[3];
#pragma unroll
        for (int x = 0; x < 3; ++x) if (x < nt) cs[x] = *(const LAS f32x4*)(smem + K4_CS + (16 * (4 * ((A ? 0 : 5) + t0 + x) + s) + 4 * g) * 4);
        SB();
#pragma unroll
        for (int x = 0; x < 3; ++x) if (x < nt) { const int t = t0 + x, ft = 4 * ((A ? 0 : 5) + t) + s;
            const float b0 = blo(bb[t].x), b1 = bhi(bb[t].x), b2 = blo(bb[t].y), b3 = bhi(bb[t].y);
#pragma unroll
            for (int tt = 0; tt < 4; ++tt) {
                unsigned o01, o23; ln_gelu4(acc[x][tt], cs[x], b0, b1, b2, b3, -mr[tt][0], mr[tt][1], o01, o23);
                *(LAS v2u*)(smem + K4_H + (tt * 18 + (ft >> 1)) * 1024 + lane * 16 + (ft & 1) * 8) = v2u{o01, o23}; } } };
    constexpr int T0 = A ? 3 : T0B;
    ffn_pass<T0, 0>(acc, ring, rw, RD * 1024, smem + K4_X + lane * 16, lane, none);
    while (*(volatile LAS unsigned*)(smem + K4_CNT) < 4u) { }
    asm volatile("" ::: "memory");
#pragma unroll
    for (int tt = 0; tt < 4; ++tt) mr[tt] = *(const LAS f32x2*)(st + (16 * tt + i) * 2);
    act(0, T0);
    ffn_pass<NT - T0, (18 * T0) % RD>(acc, ring, rw, (18 * T0 + RD) * 1024, smem + K4_X + lane * 16, lane, none);
    act(T0, NT - T0);
    LBAR();
    constexpr int G2 = A ? 90 : 72;
    const LAS char* hp = smem + K4_H + lane * 16;
    const rsrc_t rp = mkrsrc(ws + OFF_P + (size_t)r * NSL * P_SRC, NSL * (unsigned)P_SRC);
    v4u outq[4];
    auto pack = [&]() {
#pragma unroll
        for (int t = 0; t < 2; ++t)
#pragma unroll
            for (int tp = 0; tp < 2; ++tp) {
                outq[t * 2 + tp] = v4u{pk2(acc[t][2 * tp][0], acc[t][2 * tp][1]), pk2(acc[t][2 * tp][2], acc[t][2 * tp][3]), pk2(acc[t][2 * tp + 1][0], acc[t][2 * tp + 1][1]), pk2(acc[t][2 * tp + 1][2], acc[t][2 * tp + 1][3])};
                PIN(outq[t * 2 + tp]); } };
    auto put = [&](int k, int ps) { const int gi = (A ? 0 : 4) + 2 * ps + (k >> 1), dt = 4 * (gi < j ? gi : gi + 1) + s;
        bst128<16>(outq[k], rp, lane * 16, j * (int)P_SRC + (dt * 2 + (k & 1)) * 1024); };
    ffn_pass<2, G2 % RD>(acc, ring, rw, (G2 + RD) * 1024, hp, lane, none);
    pack();
    ffn_pass<2, (G2 + 36) % RD>(acc, ring, rw, (G2 + 36 + RD) * 1024, hp, lane, [&](int u) { if (u >= 1 && u <= 7 && (u & 1)) put(u >> 1, 0); });
    pack();
    if (!A) {
#pragma unroll
        for (int k = 0; k < 4; ++k) put(k, 1); }
    if (A) { ffn_pass<1, (G2 + 72) % RD>(acc, ring, rw, (G2 + 72 + RD) * 1024, hp, lane, [&](int u) { if (u <= 6 && !(u & 1)) put(u >> 1, 1); });
#pragma unroll
        for (int u = 0; u < 2; ++u) *(LAS f32x4*)(smem + K4_OWN + (s * 2 + u) * 1024 + lane * 16) = acc[0][2 + u]; }
    asm volatile("s_waitcnt vmcnt(0) lgkmcnt(0)\n\ts_barrier" ::: "memory");
    if (tid == 448) { flag_add(ws, FL_P + r); flag_wait(ws, FL_P + r, 8); }
    BAR();
    {
        const int dt = 4 * j + s, tp = A ? 0 : 1, dx = 32 + (j >> 1), tpx = j & 1;
        const bool extra = !A && s == (j & 3);
        v4u pv[8], px[8];
#pragma unroll
        for (int src = 0; src < 8; ++src) pv[src] = bld128_sc1(rp, lane * 16, src != j ? src * (int)P_SRC + (dt * 2 + tp) * 1024 : 0x7fff0000);
        if (extra) {
#pragma unroll
            for (int src = 0; src < 8; ++src) px[src] = bld128_sc1(rp, lane * 16, src * (int)P_SRC + (dx * 2 + tpx) * 1024); }
        const v2u b2v = *(const GAS v2u*)((const char*)a.b2 + (16 * dt + 4 * g) * 2), b2x = *(const GAS v2u*)((const char*)a.b2 + (16 * dx + 4 * g) * 2);
        SB();
        auto fin = [&](const v4u (&p)[8], int d_t, int t_p, v2u b2, bool own) {
#pragma unroll
            for (int u = 0; u < 2; ++u) { const int tt = 2 * t_p + u;
                const v2u hv = lds64(smem + K4_X + (tt * 18 + (d_t >> 1)) * 1024 + ((2 * (d_t & 1) + (g >> 1)) * 16 + i) * 16 + (g & 1) * 8);
                f32x4 o = {blo(hv.x) + blo(b2.x), bhi(hv.x) + bhi(b2.x), blo(hv.y) + blo(b2.y), bhi(hv.y) + bhi(b2.y)};
                if (own) { if (A) o += acc[0][u]; else o += *(const LAS f32x4*)(smem + K4_OWN + (s * 2 + u) * 1024 + lane * 16); }
#pragma unroll
                for (int src = 0; src < 8; ++src) o += f32x4{blo(p[src][2 * u]), bhi(p[src][2 * u]), blo(p[src][2 * u + 1]), bhi(p[src][2 * u + 1])};
                *(GAS v2u*)((char*)a.out + (size_t)(64 * r + 16 * tt + i) * (D * 2) + (16 * d_t + 4 * g) * 2) = v2u{pk2(o[0], o[1]), pk2(o[2], o[3])};
            } };
        fin(pv, dt, tp, b2v, true);
        if (extra) fin(px, dx, tpx, b2x, false);
    }
}

__global__ __launch_bounds__(512, 2) void k2_rest(K2Args a) {
    extern __shared__ __attribute__((aligned(16))) char smem_[];
    LAS char* smem = (LAS char*)smem_;
    const unsigned lds0 = (unsigned)(size_t)smem_;
    char* ws = a.ws;
    const int tid = threadIdx.x, lane = tid & 63, w = __builtin_amdgcn_readfirstlane(tid >> 6), b = blockIdx.x;
    const int j = wg_j(b), r = wg_r(b);
    const rsrc_t rws = mkrsrc(ws, (unsigned)WS_END);
    {
        const int hl = w & 3, qh = w >> 2, l31 = lane & 31, hh = lane >> 5;
        const rsrc_t rk = mkrsrc(ws + OFF_QKV + (8 + j) * QKV_G, S * 144), rv = mkrsrc(ws + OFF_QKV + (16 + j) * QKV_G, S * 144);
        const int tok0 = tid / 9, mm = tid - 9 * tok0;
        v4u kk[6], vv[6];
#pragma unroll
        for (int m = 0; m < 6; ++m) { const int tok = tok0 + 56 * m, o_ = (64 * r - 256) * 144 + tid * 16 + m * 56 * 144;
            const int off = (tid < 504 && tok < 320 && o_ >= 0) ? o_ : 0x7ffffff0;
            kk[m] = bld128(rk, off, 0); vv[m] = bld128(rv, off, 0); }
        const char* qp = ws + OFF_QKV + j * QKV_G + (size_t)(64 * r + 32 * qh + l31) * 144 + 36 * hl;
        v4u q0, q1;
        { const GAS unsigned* qq = (const GAS unsigned*)(qp + 16 * hh); q0 = v4u{qq[0], qq[1], qq[2], qq[3]};
          const unsigned t = *(const GAS unsigned*)(qp + 32); q1 = v4u{hh == 0 ? t : 0u, 0u, 0u, 0u}; }
        const Unit ua = unit_decode(a.w1, a.w2, a.wo, w < 6 ? NEARLY + b * 6 + w : (w == 6 && b < 194) ? NEARLY + 1536 + b : NUNIT);
        for (int n = tid; n < 1280; n += 512) { *(LAS unsigned*)(smem + K2_K + n * 40 + 36) = 0u; *(LAS unsigned*)(smem + K2_V + n * 40 + 36) = 0x3f80u; }
        if (tid == 0) *(LAS unsigned*)(smem + K4_CNT) = 0u;
        int lo_[4];
#pragma unroll
        for (int e = 0; e < 4; ++e) { const int dw = 4 * mm + e, h = dw / 9; lo_[e] = (h * 320 + tok0) * 40 + (dw - 9 * h) * 4; }
        if (tid < 504) {
#pragma unroll
            for (int m = 0; m < 6; ++m) if (m < 5 || tok0 + 280 < 320) {
#pragma unroll
                for (int e = 0; e < 4; ++e) { *(LAS unsigned*)(smem + K2_K + lo_[e] + m * 56 * 40) = kk[m][e]; *(LAS unsigned*)(smem + K2_V + lo_[e] + m * 56 * 40) = vv[m][e]; } }
        }
        if (ua.live) unit_load(ua, lds0 + K2_SCR + w * 4096, lane);
        LBAR();
        f32x16 oacc;
        if (r < 4) attn_core<true>(smem, q0, q1, r, w, lane, oacc, ua, rws);
        else attn_core<false>(smem, q0, q1, r, w, lane, oacc, ua, rws);
        const float inv = __builtin_amdgcn_rcpf(lo32(oacc[10]));
        LAS char* op = smem + K2_O + (32 * qh + l31) * 144 + 36 * hl + 8 * hh;
        *(LAS unsigned*)(op) = pk2(oacc[0] * inv, oacc[1] * inv); *(LAS unsigned*)(op + 4) = pk2(oacc[2] * inv, oacc[3] * inv);
        *(LAS unsigned*)(op + 16) = pk2(oacc[4] * inv, oacc[5] * inv); *(LAS unsigned*)(op + 20) = pk2(oacc[6] * inv, oacc[7] * inv);
        if (hh == 0) *(LAS unsigned*)(op + 32) = pk2(oacc[8] * inv, oacc[9] * inv);
        LBAR();
#pragma unroll
        for (int m2 = 0; m2 < 2; ++m2) { const int n = tid + 512 * m2;
            if (n < 576) { const int i = n & 15, tt = (n >> 4) & 3, m = n >> 6;
                const v4u v = lds128(smem + K2_O + (16 * tt + i) * 144 + 16 * m);
                const int n8 = 72 * j + 8 * m, ks = n8 >> 5, g = (n8 & 31) >> 3;
                bst128<16>(v, rws, (int)OFF_O + (4 * r + tt) * (int)TT_FR + ks * 1024 + (g * 16 + i) * 16, 0); } }
        asm volatile("s_waitcnt vmcnt(0) lgkmcnt(0)\n\ts_barrier" ::: "memory");
    }
    if (w < 4) ffn_body<true>(a, smem, r, j, w, lane); else ffn_body<false>(a, smem, r, j, w, lane);
}

extern "C" void kernel_launch(void* const* d_in, const int* in_sizes, int n_in, void* d_out, int out_size, void* d_ws, size_t ws_size, hipStream_t stream) {
    (void)in_sizes; (void)n_in; (void)out_size; (void)ws_size;
    char* ws = (char*)d_ws;
    (void)hipFuncSetAttribute((const void*)k1_qkv, hipFuncAttributeMaxDynamicSharedMemorySize, K1_LDS);
    (void)hipFuncSetAttribute((const void*)k2_rest, hipFuncAttributeMaxDynamicSharedMemorySize, K2_LDS);
    K1Args a1{d_in[0], d_in[1], d_in[2], d_in[4], d_in[6], ws};
    k1_qkv<<<256, 512, K1_LDS, stream>>>(a1);
    K2Args a2{ws, d_in[0], d_in[2], d_in[4], d_in[6], d_in[5], d_in[7], d_out};
    k2_rest<<<256, 512, K2_LDS, stream>>>(a2);
}
```
